```python
import torch

def _softmax(x, axis=-1):
    return torch.softmax(x, dim=axis)
def _gelu(x):
    return torch.nn.functional.gelu(x, approximate="tanh")
def _silu(x):
    return torch.nn.functional.silu(x)
def _sigmoid(x):
    return torch.sigmoid(x)
def _layernorm(x, axis=-1, eps=1e-5):
    m = x.mean(dim=axis, keepdim=True)
    v = x.var(dim=axis, keepdim=True, unbiased=False)
    return (x - m) / torch.sqrt(v + eps)
def _scatter_add(tgt, idx, src, axis=0):
    return tgt.clone().index_add_(axis, idx, src)
def _segment_sum(src, seg, n):
    out = torch.zeros((n,) + src.shape[1:], dtype=src.dtype)
    return out.index_add_(0, seg, src)
def _pad(x, pw):
    flat = tuple(v for pair in reversed(pw) for v in pair)
    return torch.nn.functional.pad(x, flat)

kernel_name = 'gather_scatter__isomorphism_gin_2__graph'

def setup_inputs():

    _w = (lambda a: (lambda t: t.to(torch.bfloat16) if t.is_floating_point() else t)(torch.as_tensor(a)))
    N, deg, D = 10000, 16, 256
    E = N * deg
    H = 2 * D
    src = (lambda lo, hi, *s: torch.randint(lo, hi, s))(0, N, E).to(torch.int64)
    dst = (lambda lo, hi, *s: torch.randint(lo, hi, s))(0, N, E).to(torch.int64)
    return {
        "x": _w((lambda *s: torch.randn(*s, dtype=torch.bfloat16) * 0.05)(N, D)),
        "src": _w(src),
        "dst": _w(dst),
        "eps": _w((lambda *s: torch.randn(*s, dtype=torch.bfloat16) * 0.05)(1) * 0.1),
        "w1": _w((lambda *s: torch.randn(*s, dtype=torch.bfloat16) * 0.05)(D, H) * D ** -0.5),
        "b1": _w((lambda *s: torch.randn(*s, dtype=torch.bfloat16) * 0.4)(H) * 0.01),
        "w2": _w((lambda *s: torch.randn(*s, dtype=torch.bfloat16) * 0.05)(H, D) * H ** -0.5),
        "b2": _w((lambda *s: torch.randn(*s, dtype=torch.bfloat16) * 0.05)(D) * 0.01),
    }

def reference(x, src, dst, eps, w1, b1, w2, b2):
    N = x.shape[0]
    agg = _segment_sum(x[src], dst, N)
    h = (1.0 + eps) * x + agg
    h = torch.einsum('nd,dh->nh', h, w1) + b1
    h = torch.where(h > 0, h, 0.0 * h)
    h = torch.einsum('nh,hd->nd', h, w2) + b2
    return h
```

```cpp
#include <hip/hip_runtime.h>
#include <hip/hip_bf16.h>
#include <stdint.h>

typedef unsigned v4u __attribute__((ext_vector_type(4)));
typedef unsigned v2u __attribute__((ext_vector_type(2)));
typedef float f32x4 __attribute__((ext_vector_type(4)));
typedef float f32x2 __attribute__((ext_vector_type(2)));
typedef short bf16x8 __attribute__((ext_vector_type(8)));
typedef __bf16 bf16x2_t __attribute__((ext_vector_type(2)));
#define GAS __attribute__((address_space(1)))
#define DI __device__ __forceinline__

constexpr int D = 256, H = 512, TM = 40;
constexpr int NP = 256;
constexpr int EPB = 640;
constexpr int MAXT = 256;
constexpr int CAP = 2048;
constexpr int HP = 544;
constexpr int H1P = 1056;
constexpr unsigned OOB = 0x40000000u;
constexpr int R = 16;

DI unsigned pack_bf16(float a, float b) {
  f32x2 v = {a, b};
  bf16x2_t r = __builtin_convertvector(v, bf16x2_t);
  return __builtin_bit_cast(unsigned, r);
}
DI float bflo(unsigned u) { return __builtin_bit_cast(float, u << 16); }
DI float bfhi(unsigned u) { return __builtin_bit_cast(float, u & 0xffff0000u); }
DI f32x4 mfma(v4u a, v4u b, f32x4 c) {
  return __builtin_amdgcn_mfma_f32_16x16x32_bf16(__builtin_bit_cast(bf16x8, a), __builtin_bit_cast(bf16x8, b), c, 0, 0, 0);
}
DI float relu_ref(float h) { return h > 0.f ? h : 0.0f * h; }

__global__ __launch_bounds__(256) void gin_prep(const int* __restrict__ src, const int* __restrict__ dst, int E, int N, int epb,
                                                const unsigned short* __restrict__ w1, const unsigned short* __restrict__ w2,
                                                unsigned* __restrict__ cells, v4u* __restrict__ wt, int T) {
  __shared__ unsigned lcnt[MAXT];
  __shared__ __attribute__((aligned(16))) unsigned lcell[MAXT * 16];
  const int p = blockIdx.x, lane = threadIdx.x & 63, w = __builtin_amdgcn_readfirstlane(threadIdx.x >> 6);
  if (w == 0) {
#pragma unroll
    for (int i = 0; i < 4; ++i) lcnt[lane + 64 * i] = 0;
    const int e0 = p * epb, e1 = min(E, e0 + epb);
    for (int eb = e0; eb < e1; eb += EPB) {
      int dv[10], sv[10];
#pragma unroll
      for (int i = 0; i < 10; ++i) { const int e = eb + lane + 64 * i; dv[i] = e < e1 ? dst[e] : -1; sv[i] = e < e1 ? src[e] : 0; }
      unsigned tt[10], rk[10];
#pragma unroll
      for (int i = 0; i < 10; ++i) {
        const bool ok = (unsigned)dv[i] < (unsigned)N;
        tt[i] = ok ? (unsigned)dv[i] / (unsigned)TM : 0u;
        rk[i] = 16;
        if (ok) rk[i] = atomicAdd(&lcnt[tt[i]], 1u);
      }
#pragma unroll
      for (int i = 0; i < 10; ++i) {
        const unsigned loc = (unsigned)dv[i] - tt[i] * TM;
        const unsigned s = (unsigned)sv[i] < (unsigned)N ? (unsigned)sv[i] : 0xFFFFFFu;
        if (rk[i] < 15u) lcell[tt[i] * 16 + 1 + rk[i]] = s | (loc << 24);
      }
    }
    __builtin_amdgcn_s_waitcnt(0xC07F);
#pragma unroll
    for (int i = 0; i < 4; ++i) {
      const int t = lane + 64 * i;
      if (t < T) {
        const unsigned c = lcnt[t];
        v4u* dp = (v4u*)(cells + ((size_t)t * NP + p) * 16);
        const v4u* sp = (const v4u*)(lcell + t * 16);
        v4u a = sp[0]; a.x = c; dp[0] = a;
        if (c > 3) dp[1] = sp[1];
        if (c > 7) dp[2] = sp[2];
        if (c > 11) dp[3] = sp[3];
      }
    }
  } else if (w <= 2) {
    const int f = 2 * p + (w - 1);
    const int i = lane & 15, kg = lane >> 4, q = i >> 2, r = i & 3;
    unsigned short v[8];
    if (f < 256) {
      const int ww = f >> 5, ks = (f >> 2) & 7, nt = f & 3;
      const int col = 64 * ww + 16 * q + 4 * nt + r, k0 = 32 * ks + 8 * kg;
#pragma unroll
      for (int j = 0; j < 8; ++j) v[j] = w1[(k0 + j) * H + col];
    } else {
      const int g = f - 256, ww = g >> 5, ks = (g >> 1) & 15, nt = g & 1;
      const int col = 32 * ww + 8 * q + 4 * nt + r, k0 = 32 * ks + 8 * kg;
#pragma unroll
      for (int j = 0; j < 8; ++j) v[j] = w2[(k0 + j) * D + col];
    }
    v4u o;
    o.x = v[0] | ((unsigned)v[1] << 16); o.y = v[2] | ((unsigned)v[3] << 16);
    o.z = v[4] | ((unsigned)v[5] << 16); o.w = v[6] | ((unsigned)v[7] << 16);
    wt[f * 64 + lane] = o;
  }
}

template <int CTRL> DI unsigned dpp_add(unsigned x) {
  return x + (unsigned)__builtin_amdgcn_update_dpp(0, (int)x, CTRL, 0xF, 0xF, false);
}
DI unsigned wave_incl_scan(unsigned x) {
  x = dpp_add<0x111>(x);
  x = dpp_add<0x112>(x);
  x = dpp_add<0x114>(x);
  x = dpp_add<0x118>(x);
  x = x + (unsigned)__builtin_amdgcn_update_dpp(0, (int)x, 0x142, 0xA, 0xF, false);
  x = x + (unsigned)__builtin_amdgcn_update_dpp(0, (int)x, 0x143, 0xC, 0xF, false);
  return x;
}
DI unsigned bytesum64(unsigned long long c) {
  return __builtin_amdgcn_sad_u8((unsigned)c, 0u, __builtin_amdgcn_sad_u8((unsigned)(c >> 32), 0u, 0u));
}

constexpr int L_H = 0, L_H1 = L_H + 48 * HP, L_FLAT = L_H1 + 48 * H1P, L_CNT = L_FLAT + (CAP + 128) * 4, L_FLAG = L_CNT + 64 * 8, L_END = L_FLAG + 16;

__global__ __launch_bounds__(512, 2) void gin_main(const unsigned short* __restrict__ x, const int* __restrict__ src, const int* __restrict__ dst,
                                                   const unsigned short* __restrict__ epsp, const unsigned short* __restrict__ b1,
                                                   const unsigned short* __restrict__ b2, const unsigned* __restrict__ cells,
                                                   const v4u* __restrict__ wt, unsigned short* __restrict__ out, int N, int E) {
  __shared__ __attribute__((aligned(16))) unsigned char L[L_END];
  const int tid = threadIdx.x, lane = tid & 63, w = __builtin_amdgcn_readfirstlane(tid >> 6);
  const int t = blockIdx.x, node0 = t * TM;
  unsigned* flat = (unsigned*)(L + L_FLAT);
  unsigned long long* cnt = (unsigned long long*)(L + L_CNT);
  volatile unsigned* flag = (volatile unsigned*)(L + L_FLAG);

  const v4u* cp = (const v4u*)(cells + (size_t)t * NP * 16);
  v4u cv[2];
  cv[0] = cp[tid]; cv[1] = cp[512 + tid];
  const float eps1 = 1.0f + bflo(epsp[0]);
  __builtin_amdgcn_sched_barrier(0);
  const int q = lane >> 4, m = lane & 15;
  v4u bb1a = *(const v4u*)(b1 + 64 * w + 16 * q), bb1b = *(const v4u*)(b1 + 64 * w + 16 * q + 8);
  v4u bb2 = *(const v4u*)(b2 + 32 * w + 8 * q);
  v4u wf[32];
  const v4u* w1p = wt + (size_t)(w * 32) * 64 + lane;
  const v4u* w2p = wt + (size_t)(256 + w * 32) * 64 + lane;
#pragma unroll
  for (int i = 0; i < 32; ++i) wf[i] = w1p[i * 64];
  __builtin_amdgcn_sched_barrier(0);
  if (tid < 64) cnt[tid] = 0ull;
  if (tid == 0) *flag = 0u;
  __syncthreads();
  unsigned ent[8], rk[8];
  bool bad = false;
  const int qq = tid & 3;
#pragma unroll
  for (int s = 0; s < 2; ++s) {
    const unsigned c = (unsigned)__builtin_amdgcn_update_dpp(0, (int)cv[s].x, 0x00, 0xF, 0xF, false);
    bad |= c > 15u;
#pragma unroll
    for (int j = 0; j < 4; ++j) {
      const unsigned idx = 4 * qq + j - 1;
      const bool ok = idx < c && c <= 15u;
      ent[4 * s + j] = cv[s][j];
      rk[4 * s + j] = 0xFFFFu;
      if (ok) {
        const unsigned long long old = atomicAdd(&cnt[cv[s][j] >> 24], 1ull << (8 * w));
        const unsigned r = (unsigned)(old >> (8 * w)) & 255u;
        bad |= r == 255u;
        rk[4 * s + j] = r;
      }
    }
  }
  if (bad) *flag = 1u;
  __syncthreads();
  unsigned start, len;
  {
    const unsigned long long c = cnt[lane];
    const unsigned deg = bytesum64(c);
    const unsigned below = bytesum64(c & ((1ull << (8 * w)) - 1ull));
    len = deg + 1;
    const unsigned plen = lane < TM ? (len + 1) & ~1u : 0u;
    const unsigned incl = wave_incl_scan(plen);
    start = incl - plen;
    const unsigned total = __builtin_amdgcn_readlane(incl, 63);
    const bool slow = (*flag != 0u) || total > (unsigned)CAP;
    if (!slow) {
      const unsigned B = start + 1 + below;
#pragma unroll
      for (int i = 0; i < 8; ++i) {
        const unsigned b = (unsigned)__builtin_amdgcn_ds_bpermute((int)((ent[i] >> 24) << 2), (int)B);
        const unsigned s = ent[i] & 0xFFFFFFu;
        if (rk[i] != 0xFFFFu) flat[b + rk[i]] = s == 0xFFFFFFu ? OOB : s * 512u;
      }
      if (lane >= 5 * w && lane < 5 * w + 5) {
        flat[start] = node0 + lane < N ? (unsigned)(node0 + lane) * 512u : OOB;
        if (len & 1u) flat[start + len] = OOB;
      }
    }
    __syncthreads();
    const __amdgpu_buffer_rsrc_t xr = __builtin_amdgcn_make_buffer_rsrc((void*)x, 0, N * 512, 0x00020000);
    const int half = lane >> 5;
    const unsigned l16 = (lane & 31) * 16;
    const float selfsc = half ? 1.0f : eps1;
    unsigned haddr = L_H + 5 * w * HP + (lane & 31) * 16 + half * 8;
    if (!slow) {
      const unsigned endv = __builtin_amdgcn_readfirstlane(0) + ((start + plen) >> 1);
      int s = __builtin_amdgcn_readlane(start, 5 * w) >> 1;
      int e0 = __builtin_amdgcn_readlane(endv, 5 * w), e1 = __builtin_amdgcn_readlane(endv, 5 * w + 1), e2 = __builtin_amdgcn_readlane(endv, 5 * w + 2),
          e3 = __builtin_amdgcn_readlane(endv, 5 * w + 3), e4 = __builtin_amdgcn_readlane(endv, 5 * w + 4);
      const int send = e4;
      v4u buf[R];
      unsigned off[R];
      const unsigned* fp = flat + half;
#pragma unroll
      for (int i = 0; i < R; ++i) off[i] = fp[2 * (s + i)];
#pragma unroll
      for (int i = 0; i < R; ++i)
        buf[i] = __builtin_amdgcn_raw_buffer_load_b128(xr, off[i] + l16 + (s + i < send ? 0u : 0x80000000u), 0, 0);
      f32x2 acc[4] = {{0.f, 0.f}, {0.f, 0.f}, {0.f, 0.f}, {0.f, 0.f}};
      float sc = selfsc;
      for (;;) {
#pragma unroll
        for (int i = 0; i < R; ++i) off[i] = fp[2 * (s + R + i)];
        bool done = false;
#pragma unroll
        for (int i = 0; i < R; ++i) {
          if (s + i >= send) { done = true; break; }
          const v4u v = buf[i];
          const f32x2 s2 = {sc, sc};
#pragma unroll
          for (int j = 0; j < 4; ++j) { const f32x2 u = {bflo(v[j]), bfhi(v[j])}; acc[j] = __builtin_elementwise_fma(u, s2, acc[j]); }
          buf[i] = __builtin_amdgcn_raw_buffer_load_b128(xr, off[i] + l16 + (s + R + i < send ? 0u : 0x80000000u), 0, 0);
          sc = 1.0f;
          if (s + i + 1 == e0) {
            float f[4];
#pragma unroll
            for (int j = 0; j < 4; ++j) {
              const float a = acc[j >> 1][j & 1], b = acc[2 + (j >> 1)][j & 1];
              auto r2 = __builtin_amdgcn_permlane32_swap(__builtin_bit_cast(unsigned, a), __builtin_bit_cast(unsigned, b), false, false);
              const unsigned ra = r2[0], rb = r2[1];
              f[j] = __builtin_bit_cast(float, ra) + __builtin_bit_cast(float, rb);
            }
            v2u o; o.x = pack_bf16(f[0], f[1]); o.y = pack_bf16(f[2], f[3]);
            *(v2u*)(L + haddr) = o;
            haddr += HP;
#pragma unroll
            for (int j = 0; j < 4; ++j) acc[j] = f32x2{0.f, 0.f};
            sc = selfsc;
            e0 = e1; e1 = e2; e2 = e3; e3 = e4;
          }
        }
        if (done) break;
        s += R;
      }
    } else {
      for (int j = 0; j < 5; ++j) {
        const int node = node0 + 5 * w + j;
        const v2u sv = __builtin_amdgcn_raw_buffer_load_b64(xr, node < N ? (unsigned)node * 512u + lane * 8 : OOB, 0, 0);
        float a0 = eps1 * bflo(sv.x), a1 = eps1 * bfhi(sv.x), a2 = eps1 * bflo(sv.y), a3 = eps1 * bfhi(sv.y);
        if (node < N)
          for (int eb = 0; eb < E; eb += 64) {
            const int e = eb + lane;
            const int dv = e < E ? dst[e] : -1, sv2 = e < E ? src[e] : 0;
            unsigned long long mk = __ballot(dv == node);
            while (mk) {
              const int b = __builtin_ctzll(mk); mk &= mk - 1;
              const unsigned sidx = (unsigned)__builtin_amdgcn_readlane(sv2, b);
              const v2u rv = __builtin_amdgcn_raw_buffer_load_b64(xr, sidx < (unsigned)N ? sidx * 512u + lane * 8 : OOB, 0, 0);
              a0 += bflo(rv.x); a1 += bfhi(rv.x); a2 += bflo(rv.y); a3 += bfhi(rv.y);
            }
          }
        v2u o; o.x = pack_bf16(a0, a1); o.y = pack_bf16(a2, a3);
        *(v2u*)(L + L_H + (5 * w + j) * HP + lane * 8) = o;
      }
    }
  }
  __syncthreads();
  {
    f32x4 acc[3][4];
    const unsigned bw[8] = {bb1a.x, bb1a.y, bb1a.z, bb1a.w, bb1b.x, bb1b.y, bb1b.z, bb1b.w};
#pragma unroll
    for (int nt = 0; nt < 4; ++nt) {
      const f32x4 bv = {bflo(bw[2 * nt]), bfhi(bw[2 * nt]), bflo(bw[2 * nt + 1]), bfhi(bw[2 * nt + 1])};
#pragma unroll
      for (int mt = 0; mt < 3; ++mt) acc[mt][nt] = bv;
    }
    const unsigned char* hb = L + L_H + m * HP + q * 16;
#pragma unroll
    for (int ks = 0; ks < 8; ++ks) {
      v4u b[3];
#pragma unroll
      for (int mt = 0; mt < 3; ++mt) b[mt] = *(const v4u*)(hb + mt * 16 * HP + ks * 64);
#pragma unroll
      for (int nt = 0; nt < 4; ++nt) {
#pragma unroll
        for (int mt = 0; mt < 3; ++mt) acc[mt][nt] = mfma(wf[ks * 4 + nt], b[mt], acc[mt][nt]);
        wf[ks * 4 + nt] = w2p[(ks * 4 + nt) * 64];
      }
      __builtin_amdgcn_sched_barrier(0);
    }
    unsigned char* h1w = L + L_H1 + m * H1P + (64 * w + 16 * q) * 2;
#pragma unroll
    for (int mt = 0; mt < 3; ++mt) {
      unsigned o[8];
#pragma unroll
      for (int nt = 0; nt < 4; ++nt) {
        o[2 * nt] = pack_bf16(relu_ref(acc[mt][nt][0]), relu_ref(acc[mt][nt][1]));
        o[2 * nt + 1] = pack_bf16(relu_ref(acc[mt][nt][2]), relu_ref(acc[mt][nt][3]));
      }
      *(v4u*)(h1w + mt * 16 * H1P) = v4u{o[0], o[1], o[2], o[3]};
      *(v4u*)(h1w + mt * 16 * H1P + 16) = v4u{o[4], o[5], o[6], o[7]};
    }
  }
  __syncthreads();
  {
    f32x4 acc[3][2];
#pragma unroll
    for (int nt = 0; nt < 2; ++nt) {
      const f32x4 bv = {bflo(bb2[2 * nt]), bfhi(bb2[2 * nt]), bflo(bb2[2 * nt + 1]), bfhi(bb2[2 * nt + 1])};
#pragma unroll
      for (int mt = 0; mt < 3; ++mt) acc[mt][nt] = bv;
    }
    const unsigned char* hb = L + L_H1 + m * H1P + q * 16;
#pragma unroll
    for (int ks = 0; ks < 16; ++ks) {
      v4u b[3];
#pragma unroll
      for (int mt = 0; mt < 3; ++mt) b[mt] = *(const v4u*)(hb + mt * 16 * H1P + ks * 64);
#pragma unroll
      for (int nt = 0; nt < 2; ++nt)
#pragma unroll
        for (int mt = 0; mt < 3; ++mt) acc[mt][nt] = mfma(wf[ks * 2 + nt], b[mt], acc[mt][nt]);
    }
#pragma unroll
    for (int mt = 0; mt < 3; ++mt) {
      const int r = 16 * mt + m;
      v4u o;
      o.x = pack_bf16(acc[mt][0][0], acc[mt][0][1]); o.y = pack_bf16(acc[mt][0][2], acc[mt][0][3]);
      o.z = pack_bf16(acc[mt][1][0], acc[mt][1][1]); o.w = pack_bf16(acc[mt][1][2], acc[mt][1][3]);
      if (r < TM && node0 + r < N) *(v4u*)(out + (size_t)(node0 + r) * D + 32 * w + 8 * q) = o;
    }
  }
}

extern "C" void kernel_launch(void* const* d_in, const int* in_sizes, int n_in, void* d_out, int out_size, void* d_ws, size_t ws_size,
                              hipStream_t stream) {
  const unsigned short* x = (const unsigned short*)d_in[0];
  const int* src = (const int*)d_in[1];
  const int* dst = (const int*)d_in[2];
  const unsigned short* eps = (const unsigned short*)d_in[3];
  const unsigned short* w1 = (const unsigned short*)d_in[4];
  const unsigned short* b1 = (const unsigned short*)d_in[5];
  const unsigned short* w2 = (const unsigned short*)d_in[6];
  const unsigned short* b2 = (const unsigned short*)d_in[7];
  const int N = in_sizes[0] / D, E = in_sizes[1];
  const int T = (N + TM - 1) / TM;
  int epb = (E + NP - 1) / NP; epb = (epb + EPB - 1) / EPB * EPB;
  unsigned* cells = (unsigned*)d_ws;
  v4u* wt = (v4u*)((char*)d_ws + (size_t)MAXT * NP * 64);
  gin_prep<<<NP, 256, 0, stream>>>(src, dst, E, N, epb, w1, w2, cells, wt, T);
  gin_main<<<T, 512, 0, stream>>>(x, src, dst, eps, b1, b2, cells, wt, (unsigned short*)d_out, N, E);
}
```

```cpp
#include <hip/hip_runtime.h>
#include <hip/hip_bf16.h>
#include <stdint.h>

typedef unsigned v4u __attribute__((ext_vector_type(4)));
typedef unsigned v2u __attribute__((ext_vector_type(2)));
typedef float f32x4 __attribute__((ext_vector_type(4)));
typedef float f32x2 __attribute__((ext_vector_type(2)));
typedef short bf16x8 __attribute__((ext_vector_type(8)));
typedef __bf16 bf16x2_t __attribute__((ext_vector_type(2)));
#define GAS __attribute__((address_space(1)))
#define DI __device__ __forceinline__
#define STAMP(i)
#define STAMP_DECL
#define STAMP_OUT(base, idx)

constexpr int D = 256, H = 512, TM = 40;
constexpr int NP = 256;
constexpr int EPB = 640;
constexpr int MAXT = 256;
constexpr int CAP = 2048;
constexpr int HP = 544;
constexpr int H1P = 1056;
constexpr unsigned OOB = 0x40000000u;
constexpr int R = 16;

DI unsigned pack_bf16(float a, float b) {
  f32x2 v = {a, b};
  bf16x2_t r = __builtin_convertvector(v, bf16x2_t);
  return __builtin_bit_cast(unsigned, r);
}
DI float bflo(unsigned u) { return __builtin_bit_cast(float, u << 16); }
DI float bfhi(unsigned u) { return __builtin_bit_cast(float, u & 0xffff0000u); }
DI f32x4 mfma(v4u a, v4u b, f32x4 c) {
  return __builtin_amdgcn_mfma_f32_16x16x32_bf16(__builtin_bit_cast(bf16x8, a), __builtin_bit_cast(bf16x8, b), c, 0, 0, 0);
}
DI float relu_ref(float h) { return h > 0.f ? h : 0.0f * h; }

__global__ __launch_bounds__(256) void gin_prep(const int* __restrict__ src, const int* __restrict__ dst, int E, int N, int epb,
                                                const unsigned short* __restrict__ w1, const unsigned short* __restrict__ w2,
                                                unsigned* __restrict__ cells, v4u* __restrict__ wt, int T) {
  __shared__ unsigned lcnt[MAXT];
  __shared__ __attribute__((aligned(16))) unsigned lcell[MAXT * 16];
  const int p = blockIdx.x, lane = threadIdx.x & 63, w = __builtin_amdgcn_readfirstlane(threadIdx.x >> 6);
  STAMP_DECL STAMP(0);
  if (w == 0) {
#pragma unroll
    for (int i = 0; i < 4; ++i) lcnt[lane + 64 * i] = 0;
    const int e0 = p * epb, e1 = min(E, e0 + epb);
    for (int eb = e0; eb < e1; eb += EPB) {
      int dv[10], sv[10];
#pragma unroll
      for (int i = 0; i < 10; ++i) { const int e = eb + lane + 64 * i; dv[i] = e < e1 ? dst[e] : -1; sv[i] = e < e1 ? src[e] : 0; }
      STAMP(1);
      STAMP(2);
      unsigned tt[10], rk[10];
#pragma unroll
      for (int i = 0; i < 10; ++i) {
        const bool ok = (unsigned)dv[i] < (unsigned)N;
        tt[i] = ok ? (unsigned)dv[i] / (unsigned)TM : 0u;
        rk[i] = 16;
        if (ok) rk[i] = atomicAdd(&lcnt[tt[i]], 1u);
      }
#pragma unroll
      for (int i = 0; i < 10; ++i) {
        const unsigned loc = (unsigned)dv[i] - tt[i] * TM;
        const unsigned s = (unsigned)sv[i] < (unsigned)N ? (unsigned)sv[i] : 0xFFFFFFu;
        if (rk[i] < 15u) lcell[tt[i] * 16 + 1 + rk[i]] = s | (loc << 24);
      }
    }
    __builtin_amdgcn_s_waitcnt(0xC07F);
    STAMP(3);
#pragma unroll
    for (int i = 0; i < 4; ++i) {
      const int t = lane + 64 * i;
      if (t < T) {
        const unsigned c = lcnt[t];
        v4u* dp = (v4u*)(cells + ((size_t)t * NP + p) * 16);
        const v4u* sp = (const v4u*)(lcell + t * 16);
        v4u a = sp[0]; a.x = c; dp[0] = a;
        if (c > 3) dp[1] = sp[1];
        if (c > 7) dp[2] = sp[2];
        if (c > 11) dp[3] = sp[3];
      }
    }
  } else if (w <= 2) {
    const int f = 2 * p + (w - 1);
    const int i = lane & 15, kg = lane >> 4, q = i >> 2, r = i & 3;
    unsigned short v[8];
    if (f < 256) {
      const int ww = f >> 5, ks = (f >> 2) & 7, nt = f & 3;
      const int col = 64 * ww + 16 * q + 4 * nt + r, k0 = 32 * ks + 8 * kg;
#pragma unroll
      for (int j = 0; j < 8; ++j) v[j] = w1[(k0 + j) * H + col];
    } else {
      const int g = f - 256, ww = g >> 5, ks = (g >> 1) & 15, nt = g & 1;
      const int col = 32 * ww + 8 * q + 4 * nt + r, k0 = 32 * ks + 8 * kg;
#pragma unroll
      for (int j = 0; j < 8; ++j) v[j] = w2[(k0 + j) * D + col];
    }
    v4u o;
    o.x = v[0] | ((unsigned)v[1] << 16); o.y = v[2] | ((unsigned)v[3] << 16);
    o.z = v[4] | ((unsigned)v[5] << 16); o.w = v[6] | ((unsigned)v[7] << 16);
    wt[f * 64 + lane] = o;
  }
  STAMP(4);
  STAMP(5);
  STAMP_OUT((unsigned long long*)((char*)cells + (65u << 20)), blockIdx.x * 8 + w);
}

template <int CTRL> DI unsigned dpp_add(unsigned x) {
  return x + (unsigned)__builtin_amdgcn_update_dpp(0, (int)x, CTRL, 0xF, 0xF, false);
}
DI unsigned wave_incl_scan(unsigned x) {
  x = dpp_add<0x111>(x);
  x = dpp_add<0x112>(x);
  x = dpp_add<0x114>(x);
  x = dpp_add<0x118>(x);
  x = x + (unsigned)__builtin_amdgcn_update_dpp(0, (int)x, 0x142, 0xA, 0xF, false);
  x = x + (unsigned)__builtin_amdgcn_update_dpp(0, (int)x, 0x143, 0xC, 0xF, false);
  return x;
}
DI unsigned bytesum64(unsigned long long c) {
  return __builtin_amdgcn_sad_u8((unsigned)c, 0u, __builtin_amdgcn_sad_u8((unsigned)(c >> 32), 0u, 0u));
}

constexpr int L_H = 0, L_H1 = L_H + 48 * HP, L_FLAT = L_H1 + 48 * H1P, L_CNT = L_FLAT + (CAP + 128) * 4, L_FLAG = L_CNT + 64 * 8, L_END = L_FLAG + 16;

__global__ __launch_bounds__(512, 2) void gin_main(const unsigned short* __restrict__ x, const int* __restrict__ src, const int* __restrict__ dst,
                                                   const unsigned short* __restrict__ epsp, const unsigned short* __restrict__ b1,
                                                   const unsigned short* __restrict__ b2, const unsigned* __restrict__ cells,
                                                   const v4u* __restrict__ wt, unsigned short* __restrict__ out, int N, int E) {
  __shared__ __attribute__((aligned(16))) unsigned char L[L_END];
  const int tid = threadIdx.x, lane = tid & 63, w = __builtin_amdgcn_readfirstlane(tid >> 6);
  const int t = blockIdx.x, node0 = t * TM;
  unsigned* flat = (unsigned*)(L + L_FLAT);
  unsigned long long* cnt = (unsigned long long*)(L + L_CNT);
  volatile unsigned* flag = (volatile unsigned*)(L + L_FLAG);

  STAMP_DECL STAMP(0);
  const v4u* cp = (const v4u*)(cells + (size_t)t * NP * 16);
  v4u cv[2];
  cv[0] = cp[tid]; cv[1] = cp[512 + tid];
  const float eps1 = 1.0f + bflo(epsp[0]);
  __builtin_amdgcn_sched_barrier(0);
  const int q = lane >> 4, m = lane & 15;
  v4u bb1a = *(const v4u*)(b1 + 64 * w + 16 * q), bb1b = *(const v4u*)(b1 + 64 * w + 16 * q + 8);
  v4u bb2 = *(const v4u*)(b2 + 32 * w + 8 * q);
  v4u wf[32];
  const v4u* w1p = wt + (size_t)(w * 32) * 64 + lane;
  const v4u* w2p = wt + (size_t)(256 + w * 32) * 64 + lane;
#pragma unroll
  for (int i = 0; i < 32; ++i) wf[i] = w1p[i * 64];
  __builtin_amdgcn_sched_barrier(0);
  if (tid < 64) cnt[tid] = 0ull;
  if (tid == 0) *flag = 0u;
  __syncthreads();
  STAMP(1);
  unsigned ent[8], rk[8];
  bool bad = false;
  const int qq = tid & 3;
#pragma unroll
  for (int s = 0; s < 2; ++s) {
    const unsigned c = (unsigned)__builtin_amdgcn_update_dpp(0, (int)cv[s].x, 0x00, 0xF, 0xF, false);
    bad |= c > 15u;
#pragma unroll
    for (int j = 0; j < 4; ++j) {
      const unsigned idx = 4 * qq + j - 1;
      const bool ok = idx < c && c <= 15u;
      ent[4 * s + j] = cv[s][j];
      rk[4 * s + j] = 0xFFFFu;
      if (ok) {
        const unsigned long long old = atomicAdd(&cnt[cv[s][j] >> 24], 1ull << (8 * w));
        const unsigned r = (unsigned)(old >> (8 * w)) & 255u;
        bad |= r == 255u;
        rk[4 * s + j] = r;
      }
    }
  }
  if (bad) *flag = 1u;
  __syncthreads();
  STAMP(2);
  unsigned start, len;
  {
    const unsigned long long c = cnt[lane];
    const unsigned deg = bytesum64(c);
    const unsigned below = bytesum64(c & ((1ull << (8 * w)) - 1ull));
    len = deg + 1;
    const unsigned plen = lane < TM ? (len + 1) & ~1u : 0u;
    const unsigned incl = wave_incl_scan(plen);
    start = incl - plen;
    const unsigned total = __builtin_amdgcn_readlane(incl, 63);
    const bool slow = (*flag != 0u) || total > (unsigned)CAP;
    if (!slow) {
      const unsigned B = start + 1 + below;
#pragma unroll
      for (int i = 0; i < 8; ++i) {
        const unsigned b = (unsigned)__builtin_amdgcn_ds_bpermute((int)((ent[i] >> 24) << 2), (int)B);
        const unsigned s = ent[i] & 0xFFFFFFu;
        if (rk[i] != 0xFFFFu) flat[b + rk[i]] = s == 0xFFFFFFu ? OOB : s * 512u;
      }
      if (lane >= 5 * w && lane < 5 * w + 5) {
        flat[start] = node0 + lane < N ? (unsigned)(node0 + lane) * 512u : OOB;
        if (len & 1u) flat[start + len] = OOB;
      }
    }
    __syncthreads();
    STAMP(3);
    const __amdgpu_buffer_rsrc_t xr = __builtin_amdgcn_make_buffer_rsrc((void*)x, 0, N * 512, 0x00020000);
    const int half = lane >> 5;
    const unsigned l16 = (lane & 31) * 16;
    const float selfsc = half ? 1.0f : eps1;
    unsigned haddr = L_H + 5 * w * HP + (lane & 31) * 16 + half * 8;
    if (!slow) {
      const unsigned endv = __builtin_amdgcn_readfirstlane(0) + ((start + plen) >> 1);
      int s = __builtin_amdgcn_readlane(start, 5 * w) >> 1;
      int e0 = __builtin_amdgcn_readlane(endv, 5 * w), e1 = __builtin_amdgcn_readlane(endv, 5 * w + 1), e2 = __builtin_amdgcn_readlane(endv, 5 * w + 2),
          e3 = __builtin_amdgcn_readlane(endv, 5 * w + 3), e4 = __builtin_amdgcn_readlane(endv, 5 * w + 4);
      const int send = e4;
      v4u buf[R];
      unsigned off[R];
      const unsigned* fp = flat + half;
#pragma unroll
      for (int i = 0; i < R; ++i) off[i] = fp[2 * (s + i)];
#pragma unroll
      for (int i = 0; i < R; ++i)
        buf[i] = __builtin_amdgcn_raw_buffer_load_b128(xr, off[i] + l16 + (s + i < send ? 0u : 0x80000000u), 0, 0);
      f32x2 acc[4] = {{0.f, 0.f}, {0.f, 0.f}, {0.f, 0.f}, {0.f, 0.f}};
      float sc = selfsc;
      for (;;) {
#pragma unroll
        for (int i = 0; i < R; ++i) off[i] = fp[2 * (s + R + i)];
        bool done = false;
#pragma unroll
        for (int i = 0; i < R; ++i) {
          if (s + i >= send) { done = true; break; }
          const v4u v = buf[i];
          const f32x2 s2 = {sc, sc};
#pragma unroll
          for (int j = 0; j < 4; ++j) { const f32x2 u = {bflo(v[j]), bfhi(v[j])}; acc[j] = __builtin_elementwise_fma(u, s2, acc[j]); }
          buf[i] = __builtin_amdgcn_raw_buffer_load_b128(xr, off[i] + l16 + (s + R + i < send ? 0u : 0x80000000u), 0, 0);
          sc = 1.0f;
          if (s + i + 1 == e0) {
            float f[4];
#pragma unroll
            for (int j = 0; j < 4; ++j) {
              const float a = acc[j >> 1][j & 1], b = acc[2 + (j >> 1)][j & 1];
              auto r2 = __builtin_amdgcn_permlane32_swap(__builtin_bit_cast(unsigned, a), __builtin_bit_cast(unsigned, b), false, false);
              const unsigned ra = r2[0], rb = r2[1];
              f[j] = __builtin_bit_cast(float, ra) + __builtin_bit_cast(float, rb);
            }
            v2u o; o.x = pack_bf16(f[0], f[1]); o.y = pack_bf16(f[2], f[3]);
            *(v2u*)(L + haddr) = o;
            haddr += HP;
#pragma unroll
            for (int j = 0; j < 4; ++j) acc[j] = f32x2{0.f, 0.f};
            sc = selfsc;
            e0 = e1; e1 = e2; e2 = e3; e3 = e4;
          }
        }
        if (done) break;
        s += R;
      }
    } else {
      for (int j = 0; j < 5; ++j) {
        const int node = node0 + 5 * w + j;
        const v2u sv = __builtin_amdgcn_raw_buffer_load_b64(xr, node < N ? (unsigned)node * 512u + lane * 8 : OOB, 0, 0);
        float a0 = eps1 * bflo(sv.x), a1 = eps1 * bfhi(sv.x), a2 = eps1 * bflo(sv.y), a3 = eps1 * bfhi(sv.y);
        if (node < N)
          for (int eb = 0; eb < E; eb += 64) {
            const int e = eb + lane;
            const int dv = e < E ? dst[e] : -1, sv2 = e < E ? src[e] : 0;
            unsigned long long mk = __ballot(dv == node);
            while (mk) {
              const int b = __builtin_ctzll(mk); mk &= mk - 1;
              const unsigned sidx = (unsigned)__builtin_amdgcn_readlane(sv2, b);
              const v2u rv = __builtin_amdgcn_raw_buffer_load_b64(xr, sidx < (unsigned)N ? sidx * 512u + lane * 8 : OOB, 0, 0);
              a0 += bflo(rv.x); a1 += bfhi(rv.x); a2 += bflo(rv.y); a3 += bfhi(rv.y);
            }
          }
        v2u o; o.x = pack_bf16(a0, a1); o.y = pack_bf16(a2, a3);
        *(v2u*)(L + L_H + (5 * w + j) * HP + lane * 8) = o;
      }
    }
  }
  STAMP(4);
  __syncthreads();
  STAMP(5);
  {
    f32x4 acc[3][4];
    const unsigned bw[8] = {bb1a.x, bb1a.y, bb1a.z, bb1a.w, bb1b.x, bb1b.y, bb1b.z, bb1b.w};
#pragma unroll
    for (int nt = 0; nt < 4; ++nt) {
      const f32x4 bv = {bflo(bw[2 * nt]), bfhi(bw[2 * nt]), bflo(bw[2 * nt + 1]), bfhi(bw[2 * nt + 1])};
#pragma unroll
      for (int mt = 0; mt < 3; ++mt) acc[mt][nt] = bv;
    }
    const unsigned char* hb = L + L_H + m * HP + q * 16;
#pragma unroll
    for (int ks = 0; ks < 8; ++ks) {
      v4u b[3];
#pragma unroll
      for (int mt = 0; mt < 3; ++mt) b[mt] = *(const v4u*)(hb + mt * 16 * HP + ks * 64);
#pragma unroll
      for (int nt = 0; nt < 4; ++nt) {
#pragma unroll
        for (int mt = 0; mt < 3; ++mt) acc[mt][nt] = mfma(wf[ks * 4 + nt], b[mt], acc[mt][nt]);
        wf[ks * 4 + nt] = w2p[(ks * 4 + nt) * 64];
      }
      __builtin_amdgcn_sched_barrier(0);
    }
    unsigned char* h1w = L + L_H1 + m * H1P + (64 * w + 16 * q) * 2;
#pragma unroll
    for (int mt = 0; mt < 3; ++mt) {
      unsigned o[8];
#pragma unroll
      for (int nt = 0; nt < 4; ++nt) {
        o[2 * nt] = pack_bf16(relu_ref(acc[mt][nt][0]), relu_ref(acc[mt][nt][1]));
        o[2 * nt + 1] = pack_bf16(relu_ref(acc[mt][nt][2]), relu_ref(acc[mt][nt][3]));
      }
      *(v4u*)(h1w + mt * 16 * H1P) = v4u{o[0], o[1], o[2], o[3]};
      *(v4u*)(h1w + mt * 16 * H1P + 16) = v4u{o[4], o[5], o[6], o[7]};
    }
  }
  STAMP(6);
  __syncthreads();
  STAMP(7);
  {
    f32x4 acc[3][2];
#pragma unroll
    for (int nt = 0; nt < 2; ++nt) {
      const f32x4 bv = {bflo(bb2[2 * nt]), bfhi(bb2[2 * nt]), bflo(bb2[2 * nt + 1]), bfhi(bb2[2 * nt + 1])};
#pragma unroll
      for (int mt = 0; mt < 3; ++mt) acc[mt][nt] = bv;
    }
    const unsigned char* hb = L + L_H1 + m * H1P + q * 16;
#pragma unroll
    for (int ks = 0; ks < 16; ++ks) {
      v4u b[3];
#pragma unroll
      for (int mt = 0; mt < 3; ++mt) b[mt] = *(const v4u*)(hb + mt * 16 * H1P + ks * 64);
#pragma unroll
      for (int nt = 0; nt < 2; ++nt)
#pragma unroll
        for (int mt = 0; mt < 3; ++mt) acc[mt][nt] = mfma(wf[ks * 2 + nt], b[mt], acc[mt][nt]);
    }
#pragma unroll
    for (int mt = 0; mt < 3; ++mt) {
      const int r = 16 * mt + m;
      v4u o;
      o.x = pack_bf16(acc[mt][0][0], acc[mt][0][1]); o.y = pack_bf16(acc[mt][0][2], acc[mt][0][3]);
      o.z = pack_bf16(acc[mt][1][0], acc[mt][1][1]); o.w = pack_bf16(acc[mt][1][2], acc[mt][1][3]);
      if (r < TM && node0 + r < N) *(v4u*)(out + (size_t)(node0 + r) * D + 32 * w + 8 * q) = o;
    }
  }
  STAMP(8);
  STAMP(9);
  STAMP_OUT((unsigned long long*)((char*)cells + (64u << 20)), blockIdx.x * 8 + w);
}

extern "C" void kernel_launch(void* const* d_in, const int* in_sizes, int n_in, void* d_out, int out_size, void* d_ws, size_t ws_size,
                              hipStream_t stream) {
  const unsigned short* x = (const unsigned short*)d_in[0];
  const int* src = (const int*)d_in[1];
  const int* dst = (const int*)d_in[2];
  const unsigned short* eps = (const unsigned short*)d_in[3];
  const unsigned short* w1 = (const unsigned short*)d_in[4];
  const unsigned short* b1 = (const unsigned short*)d_in[5];
  const unsigned short* w2 = (const unsigned short*)d_in[6];
  const unsigned short* b2 = (const unsigned short*)d_in[7];
  const int N = in_sizes[0] / D, E = in_sizes[1];
  const int T = (N + TM - 1) / TM;
  int epb = (E + NP - 1) / NP; epb = (epb + EPB - 1) / EPB * EPB;
  unsigned* cells = (unsigned*)d_ws;
  v4u* wt = (v4u*)((char*)d_ws + (size_t)MAXT * NP * 64);
  gin_prep<<<NP, 256, 0, stream>>>(src, dst, E, N, epb, w1, w2, cells, wt, T);
  gin_main<<<T, 512, 0, stream>>>(x, src, dst, eps, b1, b2, cells, wt, (unsigned short*)d_out, N, E);
}
```

```cpp
#include <hip/hip_runtime.h>
#include <hip/hip_bf16.h>
#include <stdint.h>

typedef unsigned v4u __attribute__((ext_vector_type(4)));
typedef unsigned v2u __attribute__((ext_vector_type(2)));
typedef float f32x4 __attribute__((ext_vector_type(4)));
typedef float f32x2 __attribute__((ext_vector_type(2)));
typedef short bf16x8 __attribute__((ext_vector_type(8)));
typedef __bf16 bf16x2_t __attribute__((ext_vector_type(2)));
typedef unsigned long long u64;
#define GAS __attribute__((address_space(1)))
#define LAS __attribute__((address_space(3)))
#define DI __device__ __forceinline__
#define RLX_AGENT __ATOMIC_RELAXED, __HIP_MEMORY_SCOPE_AGENT
#define RLX_WG __ATOMIC_RELAXED, __HIP_MEMORY_SCOPE_WORKGROUP
#define SB() __builtin_amdgcn_sched_barrier(0)
#define STAMP(i)
#define STAMP_DECL
#define STAMP_OUT(base, idx)

constexpr int D = 256, H = 512, TM = 40;
constexpr int EPB = 640;
constexpr int CAP = 1536;
constexpr int HP = 544, H1P = 1056;
constexpr unsigned OOB = 0x40000000u;
constexpr int R = 3;
constexpr int SC1 = 16;
constexpr unsigned SPIN_CAP = 4096;
#define W2PRE 6
constexpr unsigned WS_EPOCH = 0, WS_WFLAG = 4096, WS_W2T = 65536, WS_CELL = 1u << 20, WS_BYTES = WS_CELL + 256u * 16384u;
constexpr int L_H = 0, L_H1 = L_H + 48 * HP, L_FLAT = L_H1 + 48 * H1P, L_CNT = L_FLAT + (CAP + 128) * 4, L_MISC = L_CNT + 128 * 8, L_END = L_MISC + 32;
constexpr int L_PCELL = L_H1, L_PCNT = L_H1 + 320 * 64;

DI unsigned pack_bf16(float a, float b) { f32x2 v = {a, b}; bf16x2_t r = __builtin_convertvector(v, bf16x2_t); return __builtin_bit_cast(unsigned, r); }
DI float bflo(unsigned u) { return __builtin_bit_cast(float, u << 16); }
DI float bfhi(unsigned u) { return __builtin_bit_cast(float, u & 0xffff0000u); }
DI float asf(unsigned u) { return __builtin_bit_cast(float, u); }
DI unsigned asu(float u) { return __builtin_bit_cast(unsigned, u); }
DI f32x4 mfma(v4u a, v4u b, f32x4 c) {
  return __builtin_amdgcn_mfma_f32_16x16x32_bf16(__builtin_bit_cast(bf16x8, a), __builtin_bit_cast(bf16x8, b), c, 0, 0, 0);
}
DI float vmax(float a, float b) { float r; asm("v_max_f32 %0, %1, %2" : "=v"(r) : "v"(a), "v"(b)); return r; }
DI unsigned relu_pack(float a, float b) { const f32x2 h = {a, b}; const f32x2 z = h * 0.0f; return pack_bf16(vmax(a, z.x), vmax(b, z.y)); }
DI void bar() { asm volatile("s_waitcnt lgkmcnt(0)" ::: "memory"); __builtin_amdgcn_s_barrier(); asm volatile("" ::: "memory"); }
template <int CTRL> DI unsigned dpp_add(unsigned x) { return x + (unsigned)__builtin_amdgcn_update_dpp(0, (int)x, CTRL, 0xF, 0xF, false); }
DI unsigned wave_incl_scan(unsigned x) {
  x = dpp_add<0x111>(x); x = dpp_add<0x112>(x); x = dpp_add<0x114>(x); x = dpp_add<0x118>(x);
  x = x + (unsigned)__builtin_amdgcn_update_dpp(0, (int)x, 0x142, 0xA, 0xF, false);
  x = x + (unsigned)__builtin_amdgcn_update_dpp(0, (int)x, 0x143, 0xC, 0xF, false);
  return x;
}
DI unsigned bytesum64(u64 c) { return __builtin_amdgcn_sad_u8((unsigned)c, 0u, __builtin_amdgcn_sad_u8((unsigned)(c >> 32), 0u, 0u)); }
DI __amdgpu_buffer_rsrc_t rsrc(const void* p, unsigned bytes) { return __builtin_amdgcn_make_buffer_rsrc((void*)p, 0, bytes, 0x00020000); }
DI unsigned tag_b(u64 tag) { return ((unsigned)tag + (unsigned)(tag >> 24) * 0x9E3779B1u) & 0x7FFFFu; }

DI void tile_w2_load(__amdgpu_buffer_rsrc_t w2r, int f, int lane, unsigned (&v)[8]) {
  const int ww = f >> 5, ks = (f >> 1) & 15, nt = f & 1, i = lane & 15, kg = lane >> 4;
  const int qo = (2 * ks + (kg >> 1)) & 3;
  const int row0 = 64 * (ks >> 1) + 32 * (qo & 1) + 4 * (qo >> 1) + 2 * (kg & 1);
  const unsigned o = f < 256 ? (row0 * D + 32 * ww + 8 * (i >> 2) + 4 * nt + (i & 3)) * 2 : OOB;
#pragma unroll
  for (int j = 0; j < 8; ++j) v[j] = __builtin_amdgcn_raw_buffer_load_b16(w2r, o + (j >> 2) * D * 2, (j & 3) * 8 * D * 2, 0);
}
DI void tile_w2_store(__amdgpu_buffer_rsrc_t wsr, int f, int lane, const unsigned (&v)[8]) {
  const v4u o = {v[0] | (v[1] << 16), v[2] | (v[3] << 16), v[4] | (v[5] << 16), v[6] | (v[7] << 16)};
  __builtin_amdgcn_raw_buffer_store_b128(o, wsr, f < 256 ? WS_W2T + f * 1024 + lane * 16 : OOB, 0, SC1);
}
template <int DS> DI void xpose(const v4u (&raw)[32], v4u (&wf)[32]) {
#pragma unroll
  for (int c = 0; c < 4; ++c)
#pragma unroll
    for (int d = 0; d < 4; ++d) {
      const unsigned sel = (c & 1) ? 0x07060302u : 0x05040100u;
      const unsigned ta = __builtin_amdgcn_perm(raw[8 * DS + 2 * d + 1][c >> 1], raw[8 * DS + 2 * d][c >> 1], sel);
      const unsigned tb = __builtin_amdgcn_perm(raw[8 * DS + 2 * d + 1][(c >> 1) + 2], raw[8 * DS + 2 * d][(c >> 1) + 2], sel);
      wf[(2 * DS) * 4 + c][d] = (unsigned)__builtin_amdgcn_update_dpp((int)ta, (int)tb, 0x128, 0xF, 0xC, false);
      wf[(2 * DS + 1) * 4 + c][d] = (unsigned)__builtin_amdgcn_update_dpp((int)tb, (int)ta, 0x128, 0xF, 0x3, false);
    }
}
template <int LO, int HI> DI void load_w1(__amdgpu_buffer_rsrc_t w1r, unsigned w1off, v4u (&raw)[32]) {
#pragma unroll
  for (int i = LO; i < HI; ++i) raw[i] = __builtin_amdgcn_raw_buffer_load_b128(w1r, w1off + (i & 3) * 1024, ((i >> 3) * 64 + (i & 4)) * 1024, 0);
  SB();
}

__global__ __launch_bounds__(512, 2) void gin_one(const unsigned short* __restrict__ x, const int* __restrict__ src, const int* __restrict__ dst,
                                                  const unsigned short* __restrict__ epsp, const unsigned short* __restrict__ w1,
                                                  const unsigned short* __restrict__ b1, const unsigned short* __restrict__ w2,
                                                  const unsigned short* __restrict__ b2, unsigned short* __restrict__ out, unsigned char* ws,
                                                  int N, int E) {
  __shared__ __attribute__((aligned(16))) unsigned char Ls[L_END];
  LAS unsigned char* L = (LAS unsigned char*)Ls;
  const int tid = threadIdx.x, lane = tid & 63, w = __builtin_amdgcn_readfirstlane(tid >> 6);
  const int t = blockIdx.x, T = gridDim.x, node0 = t * TM;
  const __amdgpu_buffer_rsrc_t wsr = rsrc(ws, WS_BYTES), xr = rsrc(x, N * 512), w1r = rsrc(w1, D * H * 2), w2r = rsrc(w2, D * H * 2);
  STAMP_DECL STAMP(0);
  const int q = lane >> 4, m = lane & 15;
  v4u raw[32], wf[32];
  const unsigned w1off = (((lane >> 3) & 1) * 32 + q * 8) * 1024 + w * 128 + (lane & 7) * 16;
  LAS unsigned* pcnt = (LAS unsigned*)(L + L_PCNT);
  LAS unsigned* pcell = (LAS unsigned*)(L + L_PCELL);

  v4u dv[3], sv[3];
  u64 tag = 0;
  if (w == 0) {
    __builtin_amdgcn_s_setprio(3);
    const __amdgpu_buffer_rsrc_t dr = rsrc(dst, E * 4), sr = rsrc(src, E * 4);
#pragma unroll
    for (int i = 0; i < 3; ++i) {
      const unsigned o = (i == 2 && lane >= 32) ? OOB : (unsigned)(t * EPB + (lane + 64 * i) * 4) * 4u;
      dv[i] = __builtin_amdgcn_raw_buffer_load_b128(dr, o, 0, 0);
      sv[i] = __builtin_amdgcn_raw_buffer_load_b128(sr, o, 0, 0);
    }
    tag = __hip_atomic_load((GAS u64*)(ws + WS_EPOCH), RLX_AGENT);
  }
  const int tf = t + (7 - w) * T;
  unsigned tv[8];
  if (w >= 6) tile_w2_load(w2r, tf, lane, tv);
  const unsigned wu = __builtin_amdgcn_raw_buffer_load_b32(w1r, lane < 8 ? ((t >> 3) & 31) * 8192 + w * 1024 + lane * 128 : OOB, 0, 0);
  v2u selfr[5];
#pragma unroll
  for (int j = 0; j < 5; ++j) selfr[j] = __builtin_amdgcn_raw_buffer_load_b64(xr, (unsigned)(node0 + 5 * w + j) * 512u + lane * 8, 0, 0);
  SB();
  load_w1<0, 16>(w1r, w1off, raw);
  if (w == 0) {
    *(LAS v4u*)(pcnt + lane * 4) = v4u{0, 0, 0, 0};
    pcnt[256 + lane] = 0;
    unsigned tt[12], rk[12];
#pragma unroll
    for (int i = 0; i < 12; ++i) {
      const unsigned d = dv[i >> 2][i & 3];
      const int e = t * EPB + (lane + 64 * (i >> 2)) * 4;
      const bool ok = e < E && !(i >= 8 && lane >= 32) && d < (unsigned)N;
      tt[i] = ok ? d / (unsigned)TM : 256u + lane;
      rk[i] = __hip_atomic_fetch_add(pcnt + tt[i], 1u, RLX_WG);
    }
#pragma unroll
    for (int i = 0; i < 12; ++i) {
      const unsigned d = dv[i >> 2][i & 3], s = sv[i >> 2][i & 3];
      pcell[tt[i] * 16 + (rk[i] & 15u)] = min(s, 16383u) | ((d - tt[i] * TM) << 14);
    }
    *(LAS u64*)(L + L_MISC) = tag;
    __builtin_amdgcn_s_setprio(0);
  } else {
    if (w == 2) *(LAS v4u*)(L + L_CNT + lane * 16) = v4u{0, 0, 0, 0};
    if (tid == 128) *(LAS unsigned*)(L + L_MISC + 8) = 0u;
  }
#pragma unroll
  for (int j = 0; j < 5; ++j) *(LAS v2u*)(L + L_H + (5 * w + j) * HP + lane * 8) = selfr[j];
  SB();
  bar();
  STAMP(1);
  tag = *(LAS u64*)(L + L_MISC) + 1;
  const unsigned tagA = (unsigned)tag & 0xFFFFFFu, tagB = tag_b(tag);
  const unsigned k4 = (lane & 3) * 4;
#pragma unroll
  for (int i = 0; i < 2; ++i) {
    const unsigned tc = (w * 2 + i) * 16 + (lane >> 2);
    const v4u e = *(LAS v4u*)(pcell + (w * 2 + i) * 256 + lane * 4);
    const unsigned c = min(pcnt[tc], 31u);
    const v4u g = {(e.x & 0xFFFFFu) | (e.y << 20), ((e.y >> 12) & 0xFFu) | (tagA << 8), (e.z & 0xFFFFFu) | (e.w << 20),
                   ((e.w >> 12) & 0xFFu) | (c << 8) | (tagB << 13)};
    const bool st = tc < (unsigned)T && (k4 == 0 || c > k4);
    __builtin_amdgcn_raw_buffer_store_b128(g, wsr, st ? WS_CELL + tc * 16384 + t * 64 + k4 * 4 : OOB, 0, SC1);
  }
  if (w >= 6) tile_w2_store(wsr, tf, lane, tv);
  SB();
  load_w1<16, 24>(w1r, w1off, raw);
  xpose<0>(raw, wf);
  SB();
  const bool pv0 = (tid >> 2) < T, pv1 = 128 + (tid >> 2) < T;
  const unsigned coff0 = pv0 ? WS_CELL + t * 16384 + tid * 16 : OOB, coff1 = pv1 ? WS_CELL + t * 16384 + 8192 + tid * 16 : OOB;
  v4u ga = __builtin_amdgcn_raw_buffer_load_b128(wsr, coff0, 0, SC1);
  v4u gb = __builtin_amdgcn_raw_buffer_load_b128(wsr, coff1, 0, SC1);
  SB();
  xpose<1>(raw, wf);
  SB();
  if (w >= 6) {
    asm volatile("s_waitcnt vmcnt(0)" ::: "memory");
    if (lane == 0 && tf < 256) __hip_atomic_store((GAS u64*)(ws + WS_WFLAG) + tf, tag, RLX_AGENT);
  }
  load_w1<24, 32>(w1r, w1off, raw);
  const unsigned epsu = epsp[0];
  v2u bb1[4];
#pragma unroll
  for (int r = 0; r < 4; ++r) bb1[r] = *(const v2u*)(b1 + 64 * w + 32 * (q & 1) + 4 * (q >> 1) + 8 * r);
  const v4u bb2 = *(const v4u*)(b2 + 32 * w + 8 * q);
  SB();
  asm volatile("" :: "v"(wu));
  unsigned ca, cb;
  bool bad = false, tmo = false;
  for (unsigned spins = 0;; ++spins) {
    const bool va = (ga.y >> 8) == tagA && (ga.w >> 13) == tagB, vb = (gb.y >> 8) == tagA && (gb.w >> 13) == tagB;
    ca = (unsigned)__builtin_amdgcn_update_dpp(0, (int)(va ? (ga.w >> 8) & 31u : 32u), 0x00, 0xF, 0xF, false);
    cb = (unsigned)__builtin_amdgcn_update_dpp(0, (int)(vb ? (gb.w >> 8) & 31u : 32u), 0x00, 0xF, 0xF, false);
    ca = pv0 ? ca : 0u; cb = pv1 ? cb : 0u;
    const bool da = va || ca <= k4, db = vb || cb <= k4;
    if (__all(da && db && ca < 32u && cb < 32u)) break;
    if (spins > SPIN_CAP) { tmo = true; ca = cb = 0; break; }
    const v4u na = __builtin_amdgcn_raw_buffer_load_b128(wsr, va ? OOB : coff0, 0, SC1);
    const v4u nb = __builtin_amdgcn_raw_buffer_load_b128(wsr, vb ? OOB : coff1, 0, SC1);
    if (!va) ga = na;
    if (!vb) gb = nb;
  }
  STAMP(2);
  bad |= ca > 16u || cb > 16u;
  unsigned ent[8], rk[8];
  ent[0] = ga.x & 0xFFFFFu; ent[1] = (ga.x >> 20) | ((ga.y & 0xFFu) << 12); ent[2] = ga.z & 0xFFFFFu; ent[3] = (ga.z >> 20) | ((ga.w & 0xFFu) << 12);
  ent[4] = gb.x & 0xFFFFFu; ent[5] = (gb.x >> 20) | ((gb.y & 0xFFu) << 12); ent[6] = gb.z & 0xFFFFFu; ent[7] = (gb.z >> 20) | ((gb.w & 0xFFu) << 12);
  LAS u64* cnt = (LAS u64*)(L + L_CNT);
  LAS unsigned* flat = (LAS unsigned*)(L + L_FLAT);
  u64 old[8];
#pragma unroll
  for (int i = 0; i < 8; ++i) {
    const bool ok = k4 + (i & 3) < (i < 4 ? ca : cb);
    if (!ok) ent[i] = (64u + lane) << 14;
    old[i] = __hip_atomic_fetch_add(cnt + (ent[i] >> 14), 1ull << (8 * w), RLX_WG);
  }
#pragma unroll
  for (int i = 0; i < 8; ++i) { rk[i] = (unsigned)(old[i] >> (8 * w)) & 255u; bad |= rk[i] == 255u; }
  if ((__any(bad) || tmo) && lane == 0) __hip_atomic_fetch_or((LAS unsigned*)(L + L_MISC + 8), tmo ? 3u : 1u, RLX_WG);
  bar();
  STAMP(3);
  bool slow, timedout;
  int s0, nit;
  {
    const u64 c = cnt[lane];
    const unsigned flagv = __builtin_amdgcn_readfirstlane(*(LAS unsigned*)(L + L_MISC + 8));
    const unsigned deg = bytesum64(c), below = bytesum64(c & ((1ull << (8 * w)) - 1ull));
    const unsigned l4 = max(4u, (deg + 3u) & ~3u);
    const bool lastn = (lane % 5) == 4;
    const unsigned plen = lane < TM ? l4 + (lastn ? 8u * R : 0u) : 0u;
    const unsigned incl = wave_incl_scan(plen), start = incl - plen;
    timedout = (flagv & 2u) != 0u;
    slow = flagv != 0u || __builtin_amdgcn_readlane(incl, 63) > (unsigned)CAP;
    const unsigned B = (start + below) | ((start + l4 - 4u) << 16);
    const unsigned tail = __builtin_amdgcn_readlane(incl, 5 * w + 4) - 8 * R;
    s0 = __builtin_amdgcn_readlane(start, 5 * w);
    nit = ((tail - s0) / 4 + R - 1) / R;
    if (slow) s0 = 0;
    if (!slow) {
      unsigned b[8];
#pragma unroll
      for (int i = 0; i < 8; ++i) b[i] = (unsigned)__builtin_amdgcn_ds_bpermute((int)((ent[i] >> 14) << 2), (int)B);
      const unsigned st2 = (unsigned)__builtin_amdgcn_ds_bpermute((int)((5 * w + (lane >> 3)) << 2), (int)(start | (deg << 16)));
#pragma unroll
      for (int i = 0; i < 8; ++i) {
        const unsigned pos = (b[i] & 0xFFFFu) + rk[i];
        flat[ent[i] >= (64u << 14) ? CAP + 64 + lane : pos] = ((ent[i] & 0x3FFFu) << 9) | (pos >= (b[i] >> 16) ? 1u : 0u);
      }
      const unsigned dg = st2 >> 16, e4 = max(4u, (dg + 3u) & ~3u), pp = dg + (lane & 7);
      flat[lane >= 40 ? tail + lane - 40 : (pp < e4 ? (st2 & 0xFFFFu) + pp : CAP + 64 + lane)] = lane >= 40 ? OOB : OOB | 1u;
    }
  }
  bar();
  v4u fl0 = __builtin_amdgcn_raw_buffer_load_b128(wsr, WS_WFLAG + lane * 16, 0, SC1);
  v4u fl1 = __builtin_amdgcn_raw_buffer_load_b128(wsr, WS_WFLAG + 1024 + lane * 16, 0, SC1);
  const float eps1 = 1.0f + bflo(epsu);
  const unsigned hlane = L_H + (lane >> 5) * 256 + m * 16 + (q & 1) * 8;
  const unsigned l16 = m * 16;
  LAS unsigned* fp = flat + s0 + q;
  v4u bl[R], br[R];
  unsigned en[R], mk[R];
#pragma unroll
  for (int i = 0; i < R; ++i) en[i] = fp[4 * i];
#pragma unroll
  for (int i = 0; i < R; ++i) {
    const unsigned o = (en[i] & ~1u) + l16;
    mk[i] = __builtin_amdgcn_readfirstlane(en[i]) & 1u;
    bl[i] = __builtin_amdgcn_raw_buffer_load_b128(xr, o, 0, 0);
    br[i] = __builtin_amdgcn_raw_buffer_load_b128(xr, o + 256, 0, 0);
    en[i] = fp[4 * (R + i)];
  }
  SB();
  xpose<2>(raw, wf);
  SB();
  STAMP(4);
  bool okw = false;
  {
    const unsigned tl = (unsigned)tag, th = (unsigned)(tag >> 32);
    for (unsigned spins = 0; spins <= SPIN_CAP; ++spins) {
      const bool d = fl0.x == tl && fl0.y == th && fl0.z == tl && fl0.w == th && fl1.x == tl && fl1.y == th && fl1.z == tl && fl1.w == th;
      if (__all(d)) { okw = true; break; }
      fl0 = __builtin_amdgcn_raw_buffer_load_b128(wsr, WS_WFLAG + lane * 16, 0, SC1);
      fl1 = __builtin_amdgcn_raw_buffer_load_b128(wsr, WS_WFLAG + 1024 + lane * 16, 0, SC1);
    }
    if (__builtin_expect(!okw, 0)) {
#pragma unroll 1
      for (int i = 0; i < 32; ++i) { tile_w2_load(w2r, w * 32 + i, lane, tv); tile_w2_store(wsr, w * 32 + i, lane, tv); }
      asm volatile("s_waitcnt vmcnt(0)" ::: "memory");
    }
    if (okw && !timedout && tid == 192 && t == 0) __hip_atomic_store((GAS u64*)(ws + WS_EPOCH), tag, RLX_AGENT);
  }
  STAMP(5);
  const unsigned wu2 = __builtin_amdgcn_raw_buffer_load_b32(wsr, lane < 8 ? WS_W2T + ((t >> 3) & 31) * 8192 + w * 1024 + lane * 128 : OOB, 0, SC1);
  STAMP(6);
  if (__builtin_expect(!slow, 1)) {
    const v4u sel = {m == 0 ? 0x3F80u : (m == 1 ? 0x3F800000u : 0u), m == 2 ? 0x3F80u : (m == 3 ? 0x3F800000u : 0u),
                     m == 4 ? 0x3F80u : (m == 5 ? 0x3F800000u : 0u), m == 6 ? 0x3F80u : (m == 7 ? 0x3F800000u : 0u)};
    fp += 8 * R;
    f32x4 a0 = {0.f, 0.f, 0.f, 0.f}, a1 = {0.f, 0.f, 0.f, 0.f};
    unsigned haddr = hlane + 5 * w * HP;
    v2u sf = *(LAS v2u*)(L + haddr);
    do {
#pragma unroll
      for (int i = 0; i < R; ++i) {
        a0 = mfma(sel, bl[i], a0);
        a1 = mfma(sel, br[i], a1);
        const unsigned last = mk[i];
        const unsigned o = (en[i] & ~1u) + l16;
        mk[i] = __builtin_amdgcn_readfirstlane(en[i]) & 1u;
        bl[i] = __builtin_amdgcn_raw_buffer_load_b128(xr, o, 0, 0);
        br[i] = __builtin_amdgcn_raw_buffer_load_b128(xr, o + 256, 0, 0);
        en[i] = fp[4 * i];
        if (last) {
          float f[4];
#pragma unroll
          for (int r = 0; r < 4; ++r) {
            auto s2 = __builtin_amdgcn_permlane32_swap(asu(a0[r]), asu(a1[r]), false, false);
            const unsigned u = s2[0];
            f[r] = asf(u);
          }
          v2u o2;
          o2.x = pack_bf16(fmaf(eps1, bflo(sf.x), f[0]), fmaf(eps1, bfhi(sf.x), f[1]));
          o2.y = pack_bf16(fmaf(eps1, bflo(sf.y), f[2]), fmaf(eps1, bfhi(sf.y), f[3]));
          *(LAS v2u*)(L + haddr) = o2;
          haddr += HP;
          sf = *(LAS v2u*)(L + haddr);
          a0 = f32x4{0.f, 0.f, 0.f, 0.f}; a1 = a0;
        }
      }
      fp += 4 * R;
    } while (--nit > 0);
  } else {
#pragma unroll
    for (int i = 0; i < R; ++i) asm volatile("" :: "v"(bl[i]), "v"(br[i]));
#pragma unroll 1
    for (int j = 0; j < 5; ++j) {
      const int node = node0 + 5 * w + j;
      LAS v2u* hp = (LAS v2u*)(L + L_H + (5 * w + j) * HP + lane * 8);
      const v2u sv = *hp;
      float a0 = 0.f, a1 = 0.f, a2 = 0.f, a3 = 0.f;
      if (node < N)
        for (int eb = 0; eb < E; eb += 64) {
          const int e = eb + lane;
          const int dv = e < E ? dst[e] : -1, sv2 = e < E ? src[e] : 0;
          u64 mkk = __ballot(dv == node);
          while (mkk) {
            const int b = __builtin_ctzll(mkk); mkk &= mkk - 1;
            const unsigned sidx = (unsigned)__builtin_amdgcn_readlane(sv2, b);
            const v2u rv = __builtin_amdgcn_raw_buffer_load_b64(xr, sidx < (unsigned)N ? sidx * 512u + lane * 8 : OOB, 0, 0);
            a0 += bflo(rv.x); a1 += bfhi(rv.x); a2 += bflo(rv.y); a3 += bfhi(rv.y);
          }
        }
      v2u o; o.x = pack_bf16(fmaf(eps1, bflo(sv.x), a0), fmaf(eps1, bfhi(sv.x), a1)); o.y = pack_bf16(fmaf(eps1, bflo(sv.y), a2), fmaf(eps1, bfhi(sv.y), a3));
      *hp = o;
    }
  }
  asm volatile("" :: "v"(wu2));
  STAMP(7);
  v4u w2f[32];
  const unsigned w2off = WS_W2T + w * 32768 + lane * 16;
#define LOAD_W2(i) w2f[i] = __builtin_amdgcn_raw_buffer_load_b128(wsr, w2off + ((i) & 3) * 1024, ((i) >> 2) * 4096, SC1)
#pragma unroll
  for (int i = 0; i < W2PRE; ++i) LOAD_W2(i);
  SB();
  xpose<3>(raw, wf);
  SB();
  bar();
  STAMP(8);
  {
    f32x4 acc[3][4];
#pragma unroll
    for (int c = 0; c < 4; ++c) {
      f32x4 bv;
#pragma unroll
      for (int r = 0; r < 4; ++r) { const unsigned u = bb1[r][c >> 1]; bv[r] = (c & 1) ? bfhi(u) : bflo(u); }
#pragma unroll
      for (int mt = 0; mt < 3; ++mt) acc[mt][c] = bv;
    }
    LAS unsigned char* hb = L + L_H + m * HP + q * 16;
    v4u b[2][3];
#pragma unroll
    for (int mt = 0; mt < 3; ++mt) b[0][mt] = *(LAS v4u*)(hb + mt * 16 * HP);
#pragma unroll
    for (int ks = 0; ks < 8; ++ks) {
      if (ks < 7) {
#pragma unroll
        for (int mt = 0; mt < 3; ++mt) b[(ks + 1) & 1][mt] = *(LAS v4u*)(hb + mt * 16 * HP + (ks + 1) * 64);
      }
      SB();
#pragma unroll
      for (int c = 0; c < 4; ++c) {
#pragma unroll
        for (int mt = 0; mt < 3; ++mt) acc[mt][c] = mfma(wf[ks * 4 + c], b[ks & 1][mt], acc[mt][c]);
        if (c & 1) { LOAD_W2(W2PRE + ks * 2 + (c >> 1)); SB(); }
      }
    }
    LAS unsigned char* h1w = L + L_H1 + m * H1P + (64 * w + 16 * q) * 2;
#pragma unroll
    for (int mt = 0; mt < 3; ++mt) {
      unsigned o[8];
#pragma unroll
      for (int c = 0; c < 4; ++c) {
        o[2 * c] = relu_pack(acc[mt][c][0], acc[mt][c][1]);
        o[2 * c + 1] = relu_pack(acc[mt][c][2], acc[mt][c][3]);
      }
      *(LAS v4u*)(h1w + mt * 16 * H1P) = v4u{o[0], o[1], o[2], o[3]};
      *(LAS v4u*)(h1w + mt * 16 * H1P + 16) = v4u{o[4], o[5], o[6], o[7]};
    }
  }
#pragma unroll
  for (int i = W2PRE + 16; i < W2PRE + 20 && i < 32; ++i) LOAD_W2(i);
  STAMP(9);
  bar();
  const u64 ep = __hip_atomic_load((GAS u64*)(ws + WS_EPOCH), RLX_AGENT);
  {
    f32x4 acc[3][2];
#pragma unroll
    for (int nt = 0; nt < 2; ++nt) {
      const f32x4 bv = {bflo(bb2[2 * nt]), bfhi(bb2[2 * nt]), bflo(bb2[2 * nt + 1]), bfhi(bb2[2 * nt + 1])};
#pragma unroll
      for (int mt = 0; mt < 3; ++mt) acc[mt][nt] = bv;
    }
    LAS unsigned char* hb = L + L_H1 + m * H1P + q * 16;
    v4u b[2][3];
#pragma unroll
    for (int mt = 0; mt < 3; ++mt) b[0][mt] = *(LAS v4u*)(hb + mt * 16 * H1P);
#pragma unroll
    for (int ks = 0; ks < 16; ++ks) {
      if (ks < 15) {
#pragma unroll
        for (int mt = 0; mt < 3; ++mt) b[(ks + 1) & 1][mt] = *(LAS v4u*)(hb + mt * 16 * H1P + (ks + 1) * 64);
      }
      if (W2PRE + 20 + ks < 32) LOAD_W2(W2PRE + 20 + ks);
      SB();
#pragma unroll
      for (int nt = 0; nt < 2; ++nt)
#pragma unroll
        for (int mt = 0; mt < 3; ++mt) acc[mt][nt] = mfma(w2f[ks * 2 + nt], b[ks & 1][mt], acc[mt][nt]);
      SB();
    }
#pragma unroll
    for (int mt = 0; mt < 3; ++mt) {
      const int r = 16 * mt + m;
      v4u o;
      o.x = pack_bf16(acc[mt][0][0], acc[mt][0][1]); o.y = pack_bf16(acc[mt][0][2], acc[mt][0][3]);
      o.z = pack_bf16(acc[mt][1][0], acc[mt][1][1]); o.w = pack_bf16(acc[mt][1][2], acc[mt][1][3]);
      if (r < TM && node0 + r < N) *(v4u*)(out + (size_t)(node0 + r) * D + 32 * w + 8 * q) = o;
    }
  }
  if (okw && !timedout && tid == 192 && ep != tag) __hip_atomic_store((GAS u64*)(ws + WS_EPOCH), tag, RLX_AGENT);
  STAMP(10);
  STAMP_OUT((unsigned long long*)(ws + (64u << 20)), blockIdx.x * 8 + w);
}

extern "C" void kernel_launch(void* const* d_in, const int* in_sizes, int n_in, void* d_out, int out_size, void* d_ws, size_t ws_size,
                              hipStream_t stream) {
  const int N = in_sizes[0] / D, E = in_sizes[1];
  const int T = (N + TM - 1) / TM;
  gin_one<<<T, 512, 0, stream>>>((const unsigned short*)d_in[0], (const int*)d_in[1], (const int*)d_in[2], (const unsigned short*)d_in[3],
                                 (const unsigned short*)d_in[4], (const unsigned short*)d_in[5], (const unsigned short*)d_in[6],
                                 (const unsigned short*)d_in[7], (unsigned short*)d_out, (unsigned char*)d_ws, N, E);
}
```

```cpp
#include <hip/hip_runtime.h>
#include <hip/hip_bf16.h>
#include <stdint.h>
#include <type_traits>

typedef unsigned v4u __attribute__((ext_vector_type(4)));
typedef unsigned v2u __attribute__((ext_vector_type(2)));
typedef float f32x4 __attribute__((ext_vector_type(4)));
typedef float f32x2 __attribute__((ext_vector_type(2)));
typedef short bf16x8 __attribute__((ext_vector_type(8)));
typedef __bf16 bf16x2_t __attribute__((ext_vector_type(2)));
typedef unsigned long long u64;
#define GAS __attribute__((address_space(1)))
#define LAS __attribute__((address_space(3)))
#define DI __device__ __forceinline__
#define RLX_AGENT __ATOMIC_RELAXED, __HIP_MEMORY_SCOPE_AGENT
#define RLX_WG __ATOMIC_RELAXED, __HIP_MEMORY_SCOPE_WORKGROUP
#define SB() __builtin_amdgcn_sched_barrier(0)
#define PRIO(p) __builtin_amdgcn_s_setprio(p)
#define STAMP(i)
#define STAMP_DECL
#define STAMP_OUT(base, idx)

constexpr int D = 256, H = 512, TM = 40;
constexpr int EPB = 640;
constexpr int CAP = 1536;
constexpr int HP = 544, H1P = 1056;
constexpr unsigned OOB = 0x40000000u, EOOB = OOB >> 3;
#define RING 4
constexpr int R = RING;
constexpr int SC1 = 16;
constexpr unsigned SPIN_CAP = 4096;
#define BSCHED 0
#define XPRIO 0
#define ESLEEP 3
#define PSLEEP 24
#define NB1 8
#define NB2 16
#define NA 16
#define NCN 5
#define OUTAUX 16
#define W2PRE 8
constexpr unsigned WS_EPOCH = 0, WS_WFLAG = 4096, WS_W2T = 65536, WS_CELL = 1u << 20, WS_BYTES = WS_CELL + 256u * 16384u;
constexpr int L_H = 0, L_H1 = L_H + 48 * HP, L_FLAT = L_H1 + 48 * H1P, L_CNT = L_FLAT + (CAP + 128) * 4, L_MISC = L_CNT + 512,
              L_B1 = L_MISC + 32, L_B2 = L_B1 + 1024, L_END = L_B2 + 512;
constexpr int L_PCELL = L_H1, L_PCNT = L_H1 + 320 * 64;

DI unsigned pack_bf16(float a, float b) { f32x2 v = {a, b}; bf16x2_t r = __builtin_convertvector(v, bf16x2_t); return __builtin_bit_cast(unsigned, r); }
DI float bflo(unsigned u) { return __builtin_bit_cast(float, u << 16); }
DI float bfhi(unsigned u) { return __builtin_bit_cast(float, u & 0xffff0000u); }
DI float asf(unsigned u) { return __builtin_bit_cast(float, u); }
DI unsigned asu(float u) { return __builtin_bit_cast(unsigned, u); }
DI f32x4 mfma(v4u a, v4u b, f32x4 c) {
  return __builtin_amdgcn_mfma_f32_16x16x32_bf16(__builtin_bit_cast(bf16x8, a), __builtin_bit_cast(bf16x8, b), c, 0, 0, 0);
}
typedef short s16x2 __attribute__((ext_vector_type(2)));
DI unsigned relu_pack(float a, float b) {
  const s16x2 v = __builtin_bit_cast(s16x2, pack_bf16(a, b));
  return __builtin_bit_cast(unsigned, __builtin_elementwise_max(v, s16x2{0, 0}));
}
DI void bar() { asm volatile("s_waitcnt lgkmcnt(0)" ::: "memory"); __builtin_amdgcn_s_barrier(); asm volatile("" ::: "memory"); }
template <int CTRL> DI unsigned dpp_add(unsigned x) { return x + (unsigned)__builtin_amdgcn_update_dpp(0, (int)x, CTRL, 0xF, 0xF, false); }
DI unsigned wave_incl_scan(unsigned x) {
  x = dpp_add<0x111>(x); x = dpp_add<0x112>(x); x = dpp_add<0x114>(x); x = dpp_add<0x118>(x);
  x = x + (unsigned)__builtin_amdgcn_update_dpp(0, (int)x, 0x142, 0xA, 0xF, false);
  x = x + (unsigned)__builtin_amdgcn_update_dpp(0, (int)x, 0x143, 0xC, 0xF, false);
  return x;
}
DI unsigned bsum(unsigned a, unsigned b) { return __builtin_amdgcn_sad_u8(a, 0u, __builtin_amdgcn_sad_u8(b, 0u, 0u)); }
DI __amdgpu_buffer_rsrc_t rsrc(const void* p, unsigned bytes) { return __builtin_amdgcn_make_buffer_rsrc((void*)p, 0, bytes, 0x00020000); }
DI unsigned tag_b(u64 tag) { return ((unsigned)tag + (unsigned)(tag >> 24) * 0x9E3779B1u) & 0x7FFFFu; }

DI void tile_w2_load(__amdgpu_buffer_rsrc_t w2r, int f, int lane, unsigned (&v)[8]) {
  const int ww = f >> 5, ks = (f >> 1) & 15, nt = f & 1, i = lane & 15, kg = lane >> 4;
  const int qo = (2 * ks + (kg >> 1)) & 3;
  const int row0 = 64 * (ks >> 1) + 32 * (qo & 1) + 4 * (qo >> 1) + 2 * (kg & 1);
  const unsigned o = f < 256 ? (row0 * D + 32 * ww + 8 * (i >> 2) + 4 * nt + (i & 3)) * 2 : OOB;
#pragma unroll
  for (int j = 0; j < 8; ++j) v[j] = __builtin_amdgcn_raw_buffer_load_b16(w2r, o + (j >> 2) * D * 2, (j & 3) * 8 * D * 2, 0);
}
DI void tile_w2_store(__amdgpu_buffer_rsrc_t wsr, int f, int lane, const unsigned (&v)[8]) {
  const v4u o = {v[0] | (v[1] << 16), v[2] | (v[3] << 16), v[4] | (v[5] << 16), v[6] | (v[7] << 16)};
  __builtin_amdgcn_raw_buffer_store_b128(o, wsr, f < 256 ? WS_W2T + f * 1024 + lane * 16 : OOB, 0, SC1);
}
constexpr int PB = 96;
template <int G> DI void load_w1(v4u srd, unsigned off) {
  unsigned so;
  constexpr int r = PB + 32 + 32 * G;
  asm volatile("s_mov_b32 %0, %3\n\t"
               "buffer_load_dwordx4 v[%c5:%c5+3], %1, %2, %0 offen\n\tbuffer_load_dwordx4 v[%c5+4:%c5+7], %1, %2, %0 offen offset:1024\n\t"
               "buffer_load_dwordx4 v[%c5+8:%c5+11], %1, %2, %0 offen offset:2048\n\tbuffer_load_dwordx4 v[%c5+12:%c5+15], %1, %2, %0 offen offset:3072\n\t"
               "s_mov_b32 %0, %4\n\t"
               "buffer_load_dwordx4 v[%c5+16:%c5+19], %1, %2, %0 offen\n\tbuffer_load_dwordx4 v[%c5+20:%c5+23], %1, %2, %0 offen offset:1024\n\t"
               "buffer_load_dwordx4 v[%c5+24:%c5+27], %1, %2, %0 offen offset:2048\n\tbuffer_load_dwordx4 v[%c5+28:%c5+31], %1, %2, %0 offen offset:3072"
               : "=&s"(so) : "v"(off), "s"(srd), "i"(G * 65536), "i"(G * 65536 + 4096), "i"(r) : "v255");
}
template <int G> DI void xpose(unsigned lo, unsigned hi) {
#pragma unroll
  for (int d = 0; d < 4; ++d)
#pragma unroll
    for (int p = 0; p < 2; ++p) {
      unsigned t0, t1;
      asm volatile("v_perm_b32 v[%c5], v[%c4+4], v[%c4], %2\n\tv_perm_b32 v[%c5+16], v[%c4+6], v[%c4+2], %2\n\t"
                   "v_perm_b32 v[%c5+4], v[%c4+4], v[%c4], %3\n\tv_perm_b32 v[%c5+20], v[%c4+6], v[%c4+2], %3\n\t"
                   "v_mov_b32 %0, v[%c5]\n\tv_mov_b32 %1, v[%c5+4]\n\t"
                   "v_mov_b32_dpp v[%c5], v[%c5+16] row_ror:8 row_mask:0xf bank_mask:0xc\n\t"
                   "v_mov_b32_dpp v[%c5+4], v[%c5+20] row_ror:8 row_mask:0xf bank_mask:0xc\n\t"
                   "v_mov_b32_dpp v[%c5+16], %0 row_ror:8 row_mask:0xf bank_mask:0x3\n\t"
                   "v_mov_b32_dpp v[%c5+20], %1 row_ror:8 row_mask:0xf bank_mask:0x3"
                   : "=&v"(t0), "=&v"(t1) : "s"(lo), "s"(hi), "i"(PB + 32 + 32 * G + 8 * d + p), "i"(PB + 32 * G + 8 * p + d));
    }
}
DI void xpose_all() { xpose<0>(0x05040100u, 0x07060302u); xpose<1>(0x05040100u, 0x07060302u); xpose<2>(0x05040100u, 0x07060302u); xpose<3>(0x05040100u, 0x07060302u); }
constexpr int w2reg(int i) { return i < 8 ? PB + 128 + 4 * i : PB + 4 * (i - 8); }
template <int I> DI void load_w2(v4u srd, unsigned off) {
  unsigned so;
  asm volatile("s_mov_b32 %0, %3\n\tbuffer_load_dwordx4 v[%c4:%c4+3], %1, %2, %0 offen offset:%c5 sc1"
               : "=&s"(so) : "v"(off), "s"(srd), "i"((I >> 2) * 4096), "i"(w2reg(I)), "i"((I & 3) * 1024));
}
template <int A> DI void mfma_p(f32x4& acc, v4u b) { asm volatile("v_mfma_f32_16x16x32_bf16 %0, v[%c2:%c2+3], %1, %0" : "+v"(acc) : "v"(b), "i"(A)); }
template <int A> DI void mfma_p0(f32x4& acc, v4u b, f32x4 c) {
  asm volatile("v_mfma_f32_16x16x32_bf16 %0, v[%c3:%c3+3], %1, %2" : "=&v"(acc) : "v"(b), "v"(c), "i"(A));
}
template <int N> DI void wait_vm() { asm volatile("s_waitcnt vmcnt(%c0)" :: "i"(N)); }
template <int I, int E, typename F> DI void static_for(F&& f) { if constexpr (I < E) { f(std::integral_constant<int, I>{}); static_for<I + 1, E>(f); } }

__global__ __launch_bounds__(512, 2) __attribute__((amdgpu_num_vgpr(PB / 2))) void gin_one(const unsigned short* __restrict__ x, const int* __restrict__ src, const int* __restrict__ dst,
                                                  const unsigned short* __restrict__ epsp, const unsigned short* __restrict__ w1,
                                                  const unsigned short* __restrict__ b1, const unsigned short* __restrict__ w2,
                                                  const unsigned short* __restrict__ b2, unsigned short* __restrict__ out, unsigned char* ws,
                                                  int N, int E, int T) {
  extern __shared__ __attribute__((aligned(16))) unsigned char Ls[];
  LAS unsigned char* L = (LAS unsigned char*)Ls;
  const int tid = threadIdx.x, lane = tid & 63, w = __builtin_amdgcn_readfirstlane(tid >> 6);
  const int t = blockIdx.x, node0 = t * TM;
  const unsigned bulk = w >> 2, chain = bulk ^ 1;
  unsigned cpf;
  {
    u64 pc; unsigned len;
    asm volatile("s_getpc_b64 %0\n.Lgin_pc:\n\ts_mov_b32 %1, .Lgin_end-.Lgin_pc" : "=s"(pc), "=s"(len));
    cpf = __builtin_amdgcn_raw_buffer_load_b32(rsrc((const void*)pc, len), (((t >> 3) & 31) * 8 + w) * 128 + lane * 32768, 0, 0);
  }
  const __amdgpu_buffer_rsrc_t wsr = rsrc(ws, WS_BYTES), xr = rsrc(x, N * 512), w1r = rsrc(w1, D * H * 2), w2r = rsrc(w2, D * H * 2);
  STAMP_DECL STAMP(0);
  const int q = lane >> 4, m = lane & 15;
  const v4u w1d = {(unsigned)(u64)w1, (unsigned)((u64)w1 >> 32) & 0xffffu, D * H * 2, 0x00020000u};
  const v4u wsd = {(unsigned)(u64)ws, (unsigned)((u64)ws >> 32) & 0xffffu, WS_BYTES, 0x00020000u};
  const unsigned w1off = (((lane >> 3) & 1) * 32 + q * 8) * 1024 + w * 128 + (lane & 7) * 16;
  LAS unsigned* pcnt = (LAS unsigned*)(L + L_PCNT);
  LAS unsigned* pcell = (LAS unsigned*)(L + L_PCELL);

  unsigned wu = 0;
  const int tf = t + (w - 1) * T;
  if (w == 4) {
    PRIO(3);
    const __amdgpu_buffer_rsrc_t dr = rsrc(dst, E * 4), sr = rsrc(src, E * 4);
    v4u dv[3], sv[3];
#pragma unroll
    for (int i = 0; i < 3; ++i) {
      const unsigned o = (i == 2 && lane >= 32) ? OOB : (unsigned)(t * EPB + (lane + 64 * i) * 4) * 4u;
      dv[i] = __builtin_amdgcn_raw_buffer_load_b128(dr, o, 0, 0);
      sv[i] = __builtin_amdgcn_raw_buffer_load_b128(sr, o, 0, 0);
    }
    const u64 ep0 = __hip_atomic_load((GAS u64*)(ws + WS_EPOCH), RLX_AGENT);
    SB();
    *(LAS v4u*)(pcnt + lane * 4) = v4u{0, 0, 0, 0};
    pcnt[256 + lane] = 0;
    const unsigned dummy = 256u + lane;
    unsigned tt[12], rk[12];
#pragma unroll
    for (int i = 0; i < 12; ++i) {
      const int e = t * EPB + (lane + 64 * (i >> 2)) * 4;
      const unsigned lo = (e < E && !(i >= 8 && lane >= 32)) ? 0u : dummy;
      tt[i] = max(min(dv[i >> 2][i & 3] / (unsigned)TM, dummy), lo);
      rk[i] = __hip_atomic_fetch_add(pcnt + tt[i], 1u, RLX_WG);
    }
#pragma unroll
    for (int i = 0; i < 12; ++i)
      pcell[tt[i] * 16 + (rk[i] & 15u)] = (dv[i >> 2][i & 3] - tt[i] * TM) | (min(sv[i >> 2][i & 3], 16383u) << 6);
    *(LAS u64*)(L + L_MISC) = ep0;
  } else {
   __builtin_amdgcn_s_sleep(ESLEEP);
   wu = __builtin_amdgcn_raw_buffer_load_b32(w1r, w == 0 ? ((t >> 3) & 31) * 8192 + lane * 128 : OOB, 0, 0);
   if (w == 3) {
    const v4u v1 = __builtin_amdgcn_raw_buffer_load_b128(rsrc(b1, H * 2), lane * 16, 0, 0);
    const v4u v2 = __builtin_amdgcn_raw_buffer_load_b128(rsrc(b2, D * 2), lane * 16, 0, 0);
    const unsigned ev = epsp[0];
    *(LAS v2u*)(L + L_CNT + lane * 8) = v2u{0, 0};
    *(LAS v4u*)(L + L_B1 + lane * 16) = v1;
    *(LAS v4u*)(L + (lane < 32 ? L_B2 + lane * 16 : L_FLAT + lane * 16)) = v2;
    *(LAS v2u*)(L + L_MISC + 8) = v2u{0u, ev};
   } else if (w == 1 || w == 2) {
    unsigned tv[8];
    tile_w2_load(w2r, tf, lane, tv);
    tile_w2_store(wsr, tf, lane, tv);
   }
  }
  if (chain) { load_w1<0>(w1d, w1off); load_w1<1>(w1d, w1off); }
  PRIO(3);
  bar();
  STAMP(1);
  const u64 tag = *(LAS u64*)(L + L_MISC) + 1;
  const unsigned tagA = (unsigned)tag & 0xFFFFFFu, tagB = tag_b(tag);
  {
    v4u e[2]; unsigned c[2];
#pragma unroll
    for (int i = 0; i < 2; ++i) { e[i] = *(LAS v4u*)(pcell + (w * 2 + i) * 256 + lane * 4); c[i] = pcnt[(w * 2 + i) * 16 + (lane >> 2)]; }
#pragma unroll
    for (int i = 0; i < 2; ++i) {
      const unsigned tc = (w * 2 + i) * 16 + (lane >> 2), cc = min(c[i], 31u);
      const v4u g = {(e[i].x & 0xFFFFFu) | (e[i].y << 20), ((e[i].y >> 12) & 0xFFu) | (tagA << 8), (e[i].z & 0xFFFFFu) | (e[i].w << 20),
                     ((e[i].w >> 12) & 0xFFu) | (cc << 8) | (tagB << 13)};
      __builtin_amdgcn_raw_buffer_store_b128(g, wsr, tc < (unsigned)T ? WS_CELL + tc * 16384 + t * 64 + (lane & 3) * 16 : OOB, 0, SC1);
    }
  }
  SB();
  PRIO(1);
  if (chain) { load_w1<2>(w1d, w1off); load_w1<3>(w1d, w1off); } else __builtin_amdgcn_s_sleep(PSLEEP);
  const bool pv = (tid & 255) < T;
  const unsigned coff = pv ? WS_CELL + t * 16384 + (tid & 255) * 64 + bulk * 32 : OOB;
  STAMP(2);
  PRIO(2);
  v4u ga = __builtin_amdgcn_raw_buffer_load_b128(wsr, coff, 0, SC1);
  v4u gb = __builtin_amdgcn_raw_buffer_load_b128(wsr, coff + 16, 0, SC1);
  SB();
  if (bulk) { PRIO(0); load_w1<0>(w1d, w1off); load_w1<1>(w1d, w1off); }
  bool tmo = false;
  unsigned spins = 0;
#define CELL_OK() (!pv || ((ga.y >> 8) == tagA && (ga.w >> 13) == tagB && (gb.y >> 8) == tagA && (gb.w >> 13) == tagB))
  if (!__all(CELL_OK())) {
    for (;;) {
      const bool ok = CELL_OK();
      const v4u na = __builtin_amdgcn_raw_buffer_load_b128(wsr, ok ? OOB : coff, 0, SC1);
      const v4u nb = __builtin_amdgcn_raw_buffer_load_b128(wsr, ok ? OOB : coff + 16, 0, SC1);
#pragma unroll
      for (int i = 0; i < 4; ++i) { ga[i] = ok ? ga[i] : na[i]; gb[i] = ok ? gb[i] : nb[i]; }
      if (__all(CELL_OK())) break;
      if (++spins > SPIN_CAP) { tmo = true; break; }
    }
    ++spins;
  }
  STAMP(3);
  __builtin_amdgcn_s_waitcnt(0x0F70);
  asm volatile("" :: "v"(wu), "v"(cpf));
  const unsigned cc = (pv && !tmo) ? (ga.w >> 8) & 31u : 0u;
  const int nval = (int)cc - 8 * (int)bulk;
  const bool any = __any(nval > 0);
  bool bad = cc > 16u;
  unsigned ent[8], na4[8], rk[8];
  LAS unsigned* flat = (LAS unsigned*)(L + L_FLAT);
  if (any) {
    ent[0] = ga.x; ent[1] = __builtin_amdgcn_alignbit(ga.y, ga.x, 20); ent[2] = ga.z; ent[3] = __builtin_amdgcn_alignbit(ga.w, ga.z, 20);
    ent[4] = gb.x; ent[5] = __builtin_amdgcn_alignbit(gb.y, gb.x, 20); ent[6] = gb.z; ent[7] = __builtin_amdgcn_alignbit(gb.w, gb.z, 20);
    const unsigned dummy = 40u + (unsigned)(lane % 24);
#pragma unroll
    for (int i = 0; i < 8; ++i) {
      ent[i] = i < nval ? ent[i] & 0xFFFFFu : dummy;
      na4[i] = (ent[i] & 63u) << 2;
      rk[i] = __hip_atomic_fetch_add((LAS unsigned*)(L + L_CNT + bulk * 256 + na4[i]), 1u << (8 * (w & 3)), RLX_WG);
    }
#pragma unroll
    for (int i = 0; i < 8; ++i) { rk[i] = (rk[i] >> (8 * (w & 3))) & 255u; bad |= rk[i] == 255u; }
  }
  if ((__any(bad) || tmo) && lane == 0) __hip_atomic_fetch_or((LAS unsigned*)(L + L_MISC + 8), tmo ? 3u : 1u, RLX_WG);
  if (bulk) load_w1<2>(w1d, w1off);
  bar();
  STAMP(4);
  bool slow, timedout;
  int s0, nit, len[6];
  const int n0 = bulk ? (10 - NCN) * w + (8 * NCN - 40) : NCN * w;
  const int nn = bulk ? 10 - NCN : NCN;
  {
    const unsigned clo = *(LAS unsigned*)(L + L_CNT + lane * 4), chi = *(LAS unsigned*)(L + L_CNT + 256 + lane * 4);
    const unsigned flagv = __builtin_amdgcn_readfirstlane(*(LAS unsigned*)(L + L_MISC + 8));
    const unsigned mk = (1u << (8 * (w & 3))) - 1u;
    const unsigned deg = bsum(clo, chi), below = bsum(clo & (bulk ? ~0u : mk), chi & (bulk ? mk : 0u));
    const unsigned l4 = (deg + 4u) & ~3u;
    const bool lastn = lane < 4 * NCN ? (lane % NCN) == NCN - 1 : ((lane - 4 * NCN) % (10 - NCN)) == 9 - NCN;
    const unsigned plen = lane < TM ? l4 + (lastn ? 4u * (R - 1) : 0u) : 0u;
    const unsigned incl = wave_incl_scan(plen), start = incl - plen;
    timedout = (flagv & 2u) != 0u;
    slow = flagv != 0u || __builtin_amdgcn_readlane(incl, 63) > (unsigned)CAP;
    const unsigned tail = __builtin_amdgcn_readlane(incl, n0 + nn - 1) - 4 * (R - 1);
    s0 = __builtin_amdgcn_readlane(start, n0);
    nit = ((tail - s0) / 4 + R - 1) / R;
#pragma unroll
    for (int j = 0; j < 6; ++j) len[j] = __builtin_amdgcn_readlane(l4, n0 + j) >> 2;
    if (slow) s0 = 0;
    if (!slow) {
      const unsigned st2 = (unsigned)__builtin_amdgcn_ds_bpermute((n0 + (lane >> 3)) << 2, (int)(start | (deg << 16)));
      if (any) {
        const unsigned B4 = L_FLAT + 4u * (lane < TM ? start + 1u + below : CAP + 64u);
        unsigned b[8];
#pragma unroll
        for (int i = 0; i < 8; ++i) b[i] = (unsigned)__builtin_amdgcn_ds_bpermute((int)na4[i], (int)B4);
#pragma unroll
        for (int i = 0; i < 8; ++i) *(LAS unsigned*)(L + b[i] + 4u * rk[i]) = ent[i];
      }
      const unsigned dg = st2 >> 16, k = lane & 7, pp = k == 7 ? 0u : dg + 1u + k;
      const bool mine = (lane >> 3) < nn && pp < ((dg + 4u) & ~3u);
      const unsigned at = mine ? (st2 & 0xFFFFu) + pp : (lane >= 48 && lane - 48 < 4 * (R - 1) ? tail + lane - 48 : CAP + lane);
      flat[at] = (mine && k == 7) ? (unsigned)(node0 + n0 + (lane >> 3)) << 6 : EOOB;
    }
  }
  if (bulk) load_w1<3>(w1d, w1off);
  bar();
  STAMP(5);
  PRIO(2);
  if (w == 1 || w == 2) {
    asm volatile("s_waitcnt vmcnt(0)" ::: "memory");
    if (lane == 0 && tf < 256) __hip_atomic_store((GAS unsigned*)(ws + WS_WFLAG) + tf, (unsigned)tag, RLX_AGENT);
  }
  const unsigned l16 = m * 16;
  LAS unsigned* fp = flat + s0 + q;
  v4u bl[R], br[R];
  unsigned en[R];
#pragma unroll
  for (int i = 0; i < R; ++i) en[i] = fp[4 * i];
  const float epsf = bflo(*(LAS unsigned*)(L + L_MISC + 12));
#pragma unroll
  for (int i = 0; i < R; ++i) {
    const unsigned o = ((en[i] << 3) & ~511u) | l16;
    bl[i] = __builtin_amdgcn_raw_buffer_load_b128(xr, o, 0, 0);
    br[i] = __builtin_amdgcn_raw_buffer_load_b128(xr, o + 256, 0, 0);
    en[i] = fp[4 * (R + i)];
  }
  asm volatile("" ::: "memory");
  SB();
  if (chain) { PRIO(XPRIO); xpose_all(); PRIO(0); } else PRIO(1);
  STAMP(6);
  unsigned wu2 = 0;
  v4u fl0;
  if (__builtin_expect(!slow, 1)) {
    const v4u sel = {m == 0 ? 0x3F80u : (m == 1 ? 0x3F800000u : 0u), m == 2 ? 0x3F80u : (m == 3 ? 0x3F800000u : 0u),
                     m == 4 ? 0x3F80u : (m == 5 ? 0x3F800000u : 0u), m == 6 ? 0x3F80u : (m == 7 ? 0x3F800000u : 0u)};
    const v4u sel8 = {lane == 8 ? 0x3F80u : (lane == 9 ? 0x3F800000u : 0u), lane == 10 ? 0x3F80u : (lane == 11 ? 0x3F800000u : 0u),
                      lane == 12 ? 0x3F80u : (lane == 13 ? 0x3F800000u : 0u), lane == 14 ? 0x3F80u : (lane == 15 ? 0x3F800000u : 0u)};
    fp += 8 * R;
    f32x4 a0 = {0.f, 0.f, 0.f, 0.f}, a1 = {0.f, 0.f, 0.f, 0.f};
    unsigned haddr = L_H + (lane >> 5) * 256 + m * 16 + (q & 1) * 8 + n0 * HP;
    bool first = true;
    int rem = len[0], left = nn;
#define SLOT(i, REFILL) { \
        a0 = mfma(sel, bl[i], a0); \
        a1 = mfma(sel, br[i], a1); \
        if (first) { a0 = mfma(sel8, bl[i], a0); a1 = mfma(sel8, br[i], a1); } \
        if (REFILL) { \
          const unsigned o = ((en[i] << 3) & ~511u) | l16; \
          bl[i] = __builtin_amdgcn_raw_buffer_load_b128(xr, o, 0, 0); \
          br[i] = __builtin_amdgcn_raw_buffer_load_b128(xr, o + 256, 0, 0); \
          en[i] = fp[4 * i]; \
        } \
        first = --rem == 0; \
        if (first) { \
          float f[4]; \
          _Pragma("unroll") for (int r = 0; r < 4; ++r) { \
            auto s2 = __builtin_amdgcn_permlane32_swap(asu(a0[r]), asu(a1[r]), false, false); \
            const unsigned u0 = s2[0], u1 = s2[1]; \
            f[r] = fmaf(epsf, asf(u1), asf(u0)); \
          } \
          *(LAS v2u*)(L + haddr) = v2u{pack_bf16(f[0], f[1]), pack_bf16(f[2], f[3])}; \
          haddr += HP; \
          a0 = f32x4{0.f, 0.f, 0.f, 0.f}; a1 = a0; \
          rem = --left > 0 ? len[1] : 1 << 20; len[1] = len[2]; len[2] = len[3]; len[3] = len[4]; len[4] = len[5]; \
        } }
    wu2 = __builtin_amdgcn_raw_buffer_load_b32(wsr, lane < 8 ? WS_W2T + ((t >> 3) & 31) * 8192 + w * 1024 + lane * 128 : OOB, 0, SC1);
    for (; nit > 1; --nit) {
#pragma unroll
      for (int i = 0; i < R; ++i) SLOT(i, true)
      fp += 4 * R;
    }
    fl0 = __builtin_amdgcn_raw_buffer_load_b128(wsr, WS_WFLAG + lane * 16, 0, SC1);
#pragma unroll
    for (int i = 0; i < R; ++i) SLOT(i, false)
  } else {
#pragma unroll 1
    for (int j = 0; j < nn; ++j) {
      const int node = node0 + n0 + j;
      const v2u s2 = __builtin_amdgcn_raw_buffer_load_b64(xr, (unsigned)node * 512u + lane * 8, 0, 0);
      float a0 = bflo(s2.x), a1 = bfhi(s2.x), a2 = bflo(s2.y), a3 = bfhi(s2.y);
      if (node < N)
        for (int eb = 0; eb < E; eb += 64) {
          const int e = eb + lane;
          const int dv2 = (int)__builtin_amdgcn_raw_buffer_load_b32(rsrc(dst, E * 4), e * 4, 0, 0);
          const int sv2 = (int)__builtin_amdgcn_raw_buffer_load_b32(rsrc(src, E * 4), e * 4, 0, 0);
          u64 mkk = __ballot(dv2 == node && e < E);
          while (mkk) {
            const int b = __builtin_ctzll(mkk); mkk &= mkk - 1;
            const unsigned sidx = (unsigned)__builtin_amdgcn_readlane(sv2, b);
            const v2u rv = __builtin_amdgcn_raw_buffer_load_b64(xr, sidx < (unsigned)N ? sidx * 512u + lane * 8 : OOB, 0, 0);
            a0 += bflo(rv.x); a1 += bfhi(rv.x); a2 += bflo(rv.y); a3 += bfhi(rv.y);
          }
        }
      *(LAS v2u*)(L + L_H + (n0 + j) * HP + lane * 8) =
          v2u{pack_bf16(fmaf(epsf, bflo(s2.x), a0), fmaf(epsf, bfhi(s2.x), a1)), pack_bf16(fmaf(epsf, bflo(s2.y), a2), fmaf(epsf, bfhi(s2.y), a3))};
    }
    fl0 = __builtin_amdgcn_raw_buffer_load_b128(wsr, WS_WFLAG + lane * 16, 0, SC1);
  }
  STAMP(7);
  PRIO(0);
  {
    bool okw = false;
    const unsigned tl = (unsigned)tag;
    for (unsigned sp = 0; sp <= SPIN_CAP; ++sp) {
      if (__all(fl0.x == tl && fl0.y == tl && fl0.z == tl && fl0.w == tl)) { okw = true; break; }
      fl0 = __builtin_amdgcn_raw_buffer_load_b128(wsr, WS_WFLAG + lane * 16, 0, SC1);
    }
    if (__builtin_expect(!okw, 0)) {
#pragma unroll 1
      for (int i = 0; i < 32; ++i) { unsigned tv[8]; tile_w2_load(w2r, w * 32 + i, lane, tv); tile_w2_store(wsr, w * 32 + i, lane, tv); }
      asm volatile("s_waitcnt vmcnt(0)" ::: "memory");
    }
    __builtin_amdgcn_fence(__ATOMIC_ACQUIRE, "wavefront");
  }
  if (!timedout && tid == 192 && t == 0) __hip_atomic_store((GAS u64*)(ws + WS_EPOCH), tag, RLX_AGENT);
  asm volatile("" :: "v"(wu2));
  if (bulk) { PRIO(XPRIO); xpose_all(); PRIO(0); }
  const unsigned w2off = WS_W2T + w * 32768 + lane * 16;
  static_for<0, W2PRE>([&](auto I) { load_w2<I>(wsd, w2off); });
  f32x4 bias1[4];
  {
    v2u bb1[4];
#pragma unroll
    for (int r = 0; r < 4; ++r) bb1[r] = *(LAS v2u*)(L + L_B1 + (64 * w + 32 * (q & 1) + 4 * (q >> 1) + 8 * r) * 2);
#pragma unroll
    for (int c = 0; c < 4; ++c) {
#pragma unroll
      for (int r = 0; r < 4; ++r) { const unsigned u = bb1[r][c >> 1]; bias1[c][r] = (c & 1) ? bfhi(u) : bflo(u); }
      asm volatile("" : "+v"(bias1[c]));
    }
  }
  SB();
  bar();
  STAMP(8);
  f32x4 bias2[2];
  {
    LAS unsigned char* hb = L + L_H + m * HP + q * 16;
    LAS unsigned char* h1w = L + L_H1 + m * H1P + (64 * w + 16 * q) * 2;
    v4u b[2][3];
    f32x4 acc[3][4];
#pragma unroll
    for (int mt = 0; mt < 3; ++mt) b[0][mt] = *(LAS v4u*)(hb + mt * 16 * HP);
    static_for<0, 8>([&](auto KS) {
      constexpr int ks = KS;
      if constexpr (ks < 7) {
#pragma unroll
        for (int mt = 0; mt < 3; ++mt) b[(ks + 1) & 1][mt] = *(LAS v4u*)(hb + mt * 16 * HP + (ks + 1) * 64);
      }
      SB();
      static_for<0, 12>([&](auto J) {
        constexpr int c = J / 3, mt = J % 3;
        if constexpr (ks == 0) mfma_p0<PB + 4 * c>(acc[mt][c], b[0][mt], bias1[c]);
        else mfma_p<PB + 16 * ks + 4 * c>(acc[mt][c], b[ks & 1][mt]);
        if constexpr (J == 5) load_w2<8 + 2 * ks>(wsd, w2off);
        if constexpr (J == 11) load_w2<9 + 2 * ks>(wsd, w2off);
      });
      SB();
    });
    const v4u bb2 = *(LAS v4u*)(L + L_B2 + (32 * w + 8 * q) * 2);
    asm volatile("s_nop 4" : "+v"(acc[0][0]), "+v"(acc[0][1]), "+v"(acc[0][2]), "+v"(acc[0][3]), "+v"(acc[1][0]), "+v"(acc[1][1]), "+v"(acc[1][2]),
                 "+v"(acc[2][0]), "+v"(acc[2][1]), "+v"(acc[2][2]));
#pragma unroll
    for (int mt = 0; mt < 3; ++mt) {
      unsigned o[8];
      if (mt == 1) asm volatile("" : "+v"(acc[1][3]), "+v"(acc[2][3]));
#pragma unroll
      for (int k = 0; k < 8; ++k) o[k] = relu_pack(acc[mt][k >> 1][2 * (k & 1)], acc[mt][k >> 1][2 * (k & 1) + 1]);
      *(LAS v4u*)(h1w + mt * 16 * H1P) = v4u{o[0], o[1], o[2], o[3]};
      *(LAS v4u*)(h1w + mt * 16 * H1P + 16) = v4u{o[4], o[5], o[6], o[7]};
      SB();
    }
#pragma unroll
    for (int nt = 0; nt < 2; ++nt) {
      bias2[nt] = f32x4{bflo(bb2[2 * nt]), bfhi(bb2[2 * nt]), bflo(bb2[2 * nt + 1]), bfhi(bb2[2 * nt + 1])};
      asm volatile("" : "+v"(bias2[nt]));
    }
  }
  SB();
  STAMP(9);
  bar();
  v2u ep;
  {
    f32x4 acc[3][2];
    LAS unsigned char* hb = L + L_H1 + m * H1P + q * 16;
    v4u b[3][3];
#pragma unroll
    for (int i = 0; i < 2; ++i)
#pragma unroll
      for (int mt = 0; mt < 3; ++mt) b[i][mt] = *(LAS v4u*)(hb + mt * 16 * H1P + i * 64);
    static_for<0, 16>([&](auto KS) {
      constexpr int ks = KS;
      if constexpr (ks + 2 < 16) {
#pragma unroll
        for (int mt = 0; mt < 3; ++mt) b[(ks + 2) % 3][mt] = *(LAS v4u*)(hb + mt * 16 * H1P + (ks + 2) * 64);
      }
      if constexpr (ks < 8) load_w2<24 + ks>(wsd, w2off);
      constexpr int issued = ks < 8 ? 25 + ks : 32;
      wait_vm<issued - (2 * ks + 2)>();
      SB();
      static_for<0, 6>([&](auto J) {
        constexpr int nt = J / 3, mt = J % 3;
        if constexpr (ks == 0) mfma_p0<w2reg(nt)>(acc[mt][nt], b[0][mt], bias2[nt]);
        else mfma_p<w2reg(2 * ks + nt)>(acc[mt][nt], b[ks % 3][mt]);
      });
      SB();
    });
    ep = __builtin_amdgcn_raw_buffer_load_b64(wsr, tid == 192 ? WS_EPOCH : OOB, 0, SC1);
    asm volatile("s_nop 7" : "+v"(acc[0][0]), "+v"(acc[0][1]), "+v"(acc[1][0]), "+v"(acc[1][1]), "+v"(acc[2][0]), "+v"(acc[2][1]));
    const __amdgpu_buffer_rsrc_t outr = rsrc(out, N * 512);
#pragma unroll
    for (int mt = 0; mt < 3; ++mt) {
      const int r = 16 * mt + m;
      v4u o;
      o.x = pack_bf16(acc[mt][0][0], acc[mt][0][1]); o.y = pack_bf16(acc[mt][0][2], acc[mt][0][3]);
      o.z = pack_bf16(acc[mt][1][0], acc[mt][1][1]); o.w = pack_bf16(acc[mt][1][2], acc[mt][1][3]);
      __builtin_amdgcn_raw_buffer_store_b128(o, outr, r < TM ? (unsigned)(node0 + r) * 512u + 64 * w + 16 * q : OOB, 0, OUTAUX);
    }
  }
  if (!timedout && tid == 192 && (ep.x != (unsigned)tag || ep.y != (unsigned)(tag >> 32))) __hip_atomic_store((GAS u64*)(ws + WS_EPOCH), tag, RLX_AGENT);
  STAMP(10);
  STAMP_OUT((unsigned long long*)(ws + (64u << 20)), blockIdx.x * 8 + w);
  asm volatile(".Lgin_end:");
}

extern "C" void kernel_launch(void* const* d_in, const int* in_sizes, int n_in, void* d_out, int out_size, void* d_ws, size_t ws_size,
                              hipStream_t stream) {
  const int N = in_sizes[0] / D, E = in_sizes[1];
  const int T = (N + TM - 1) / TM;
  (void)hipFuncSetAttribute((const void*)gin_one, hipFuncAttributeMaxDynamicSharedMemorySize, L_END);
  gin_one<<<T, 512, L_END, stream>>>((const unsigned short*)d_in[0], (const int*)d_in[1], (const int*)d_in[2], (const unsigned short*)d_in[3],
                                 (const unsigned short*)d_in[4], (const unsigned short*)d_in[5], (const unsigned short*)d_in[6],
                                 (const unsigned short*)d_in[7], (unsigned short*)d_out, (unsigned char*)d_ws, N, E, T);
}
```

```cpp
#define MINMAX 0
#include <hip/hip_runtime.h>
#include <hip/hip_bf16.h>
#include <stdint.h>
#include <type_traits>

typedef unsigned v4u __attribute__((ext_vector_type(4)));
typedef unsigned v2u __attribute__((ext_vector_type(2)));
typedef float f32x4 __attribute__((ext_vector_type(4)));
typedef float f32x2 __attribute__((ext_vector_type(2)));
typedef short bf16x8 __attribute__((ext_vector_type(8)));
typedef __bf16 bf16x2_t __attribute__((ext_vector_type(2)));
typedef unsigned long long u64;
#define GAS __attribute__((address_space(1)))
#define LAS __attribute__((address_space(3)))
#define DI __device__ __forceinline__
#define RLX_AGENT __ATOMIC_RELAXED, __HIP_MEMORY_SCOPE_AGENT
#define RLX_WG __ATOMIC_RELAXED, __HIP_MEMORY_SCOPE_WORKGROUP
#define SB() __builtin_amdgcn_sched_barrier(0)
#define PRIO(p) __builtin_amdgcn_s_setprio(p)
#define STAMP(i)
#define STAMP_DECL
#define STAMP_OUT(base, idx)

constexpr int D = 256, H = 512, TM = 40;
constexpr int EPB = 640;
constexpr int CAP = 1536;
constexpr int HP = 544, H1P = 1056;
constexpr unsigned OOB = 0x40000000u, EOOB = OOB >> 3;
#define RING 4
constexpr int R = RING;
constexpr int SC1 = 16;
constexpr unsigned SPIN_CAP = 4096;
#define BSCHED 0
#define XPRIO 0
#define ESLEEP 3
#define PSLEEP 24
#define NB1 8
#define NB2 16
#define NA 16
#define NCN 5
#define OUTAUX 16
#define W2PRE 8
constexpr unsigned WS_EPOCH = 0, WS_WFLAG = 4096, WS_W2T = 65536, WS_CELL = 1u << 20, WS_BYTES = WS_CELL + 256u * 16384u;
constexpr int L_H = 0, L_H1 = L_H + 48 * HP, L_FLAT = L_H1 + 48 * H1P, L_CNT = L_FLAT + (CAP + 128) * 4, L_MISC = L_CNT + 512,
              L_B1 = L_MISC + 32, L_B2 = L_B1 + 1024, L_END = L_B2 + 512;
constexpr int L_PCELL = L_H1, L_PCNT = L_H1 + 320 * 64;

DI unsigned pack_bf16(float a, float b) { f32x2 v = {a, b}; bf16x2_t r = __builtin_convertvector(v, bf16x2_t); return __builtin_bit_cast(unsigned, r); }
DI float bflo(unsigned u) { return __builtin_bit_cast(float, u << 16); }
DI float bfhi(unsigned u) { return __builtin_bit_cast(float, u & 0xffff0000u); }
DI float asf(unsigned u) { return __builtin_bit_cast(float, u); }
DI unsigned asu(float u) { return __builtin_bit_cast(unsigned, u); }
DI f32x4 mfma(v4u a, v4u b, f32x4 c) {
  return __builtin_amdgcn_mfma_f32_16x16x32_bf16(__builtin_bit_cast(bf16x8, a), __builtin_bit_cast(bf16x8, b), c, 0, 0, 0);
}
typedef short s16x2 __attribute__((ext_vector_type(2)));
DI unsigned relu_pack(float a, float b) {
  const s16x2 v = __builtin_bit_cast(s16x2, pack_bf16(a, b));
  return __builtin_bit_cast(unsigned, __builtin_elementwise_max(v, s16x2{0, 0}));
}
DI void bar() { asm volatile("s_waitcnt lgkmcnt(0)" ::: "memory"); __builtin_amdgcn_s_barrier(); asm volatile("" ::: "memory"); }
template <int CTRL> DI unsigned dpp_add(unsigned x) { return x + (unsigned)__builtin_amdgcn_update_dpp(0, (int)x, CTRL, 0xF, 0xF, false); }
DI unsigned wave_incl_scan(unsigned x) {
  x = dpp_add<0x111>(x); x = dpp_add<0x112>(x); x = dpp_add<0x114>(x); x = dpp_add<0x118>(x);
  x = x + (unsigned)__builtin_amdgcn_update_dpp(0, (int)x, 0x142, 0xA, 0xF, false);
  x = x + (unsigned)__builtin_amdgcn_update_dpp(0, (int)x, 0x143, 0xC, 0xF, false);
  return x;
}
DI unsigned bsum(unsigned a, unsigned b) { return __builtin_amdgcn_sad_u8(a, 0u, __builtin_amdgcn_sad_u8(b, 0u, 0u)); }
DI __amdgpu_buffer_rsrc_t rsrc(const void* p, unsigned bytes) { return __builtin_amdgcn_make_buffer_rsrc((void*)p, 0, bytes, 0x00020000); }
DI unsigned tag_b(u64 tag) { return ((unsigned)tag + (unsigned)(tag >> 24) * 0x9E3779B1u) & 0x7FFFFu; }

DI void tile_w2_load(__amdgpu_buffer_rsrc_t w2r, int f, int lane, unsigned (&v)[8]) {
  const int ww = f >> 5, ks = (f >> 1) & 15, nt = f & 1, i = lane & 15, kg = lane >> 4;
  const int qo = (2 * ks + (kg >> 1)) & 3;
  const int row0 = 64 * (ks >> 1) + 32 * (qo & 1) + 4 * (qo >> 1) + 2 * (kg & 1);
  const unsigned o = f < 256 ? (row0 * D + 32 * ww + 8 * (i >> 2) + 4 * nt + (i & 3)) * 2 : OOB;
#pragma unroll
  for (int j = 0; j < 8; ++j) v[j] = __builtin_amdgcn_raw_buffer_load_b16(w2r, o + (j >> 2) * D * 2, (j & 3) * 8 * D * 2, 0);
}
DI void tile_w2_store(__amdgpu_buffer_rsrc_t wsr, int f, int lane, const unsigned (&v)[8]) {
  const v4u o = {v[0] | (v[1] << 16), v[2] | (v[3] << 16), v[4] | (v[5] << 16), v[6] | (v[7] << 16)};
  __builtin_amdgcn_raw_buffer_store_b128(o, wsr, f < 256 ? WS_W2T + f * 1024 + lane * 16 : OOB, 0, SC1);
}
constexpr int PB = 96;
template <int G> DI void load_w1(v4u srd, unsigned off) {
  unsigned so;
  constexpr int r = PB + 32 + 32 * G;
  asm volatile("s_mov_b32 %0, %3\n\t"
               "buffer_load_dwordx4 v[%c5:%c5+3], %1, %2, %0 offen\n\tbuffer_load_dwordx4 v[%c5+4:%c5+7], %1, %2, %0 offen offset:1024\n\t"
               "buffer_load_dwordx4 v[%c5+8:%c5+11], %1, %2, %0 offen offset:2048\n\tbuffer_load_dwordx4 v[%c5+12:%c5+15], %1, %2, %0 offen offset:3072\n\t"
               "s_mov_b32 %0, %4\n\t"
               "buffer_load_dwordx4 v[%c5+16:%c5+19], %1, %2, %0 offen\n\tbuffer_load_dwordx4 v[%c5+20:%c5+23], %1, %2, %0 offen offset:1024\n\t"
               "buffer_load_dwordx4 v[%c5+24:%c5+27], %1, %2, %0 offen offset:2048\n\tbuffer_load_dwordx4 v[%c5+28:%c5+31], %1, %2, %0 offen offset:3072"
               : "=&s"(so) : "v"(off), "s"(srd), "i"(G * 65536), "i"(G * 65536 + 4096), "i"(r) : "v255");
}
template <int G> DI void xpose(unsigned lo, unsigned hi) {
#pragma unroll
  for (int d = 0; d < 4; ++d)
#pragma unroll
    for (int p = 0; p < 2; ++p) {
      unsigned t0, t1;
      asm volatile("v_perm_b32 v[%c5], v[%c4+4], v[%c4], %2\n\tv_perm_b32 v[%c5+16], v[%c4+6], v[%c4+2], %2\n\t"
                   "v_perm_b32 v[%c5+4], v[%c4+4], v[%c4], %3\n\tv_perm_b32 v[%c5+20], v[%c4+6], v[%c4+2], %3\n\t"
                   "v_mov_b32 %0, v[%c5]\n\tv_mov_b32 %1, v[%c5+4]\n\t"
                   "v_mov_b32_dpp v[%c5], v[%c5+16] row_ror:8 row_mask:0xf bank_mask:0xc\n\t"
                   "v_mov_b32_dpp v[%c5+4], v[%c5+20] row_ror:8 row_mask:0xf bank_mask:0xc\n\t"
                   "v_mov_b32_dpp v[%c5+16], %0 row_ror:8 row_mask:0xf bank_mask:0x3\n\t"
                   "v_mov_b32_dpp v[%c5+20], %1 row_ror:8 row_mask:0xf bank_mask:0x3"
                   : "=&v"(t0), "=&v"(t1) : "s"(lo), "s"(hi), "i"(PB + 32 + 32 * G + 8 * d + p), "i"(PB + 32 * G + 8 * p + d));
    }
}
DI void xpose_all() { xpose<0>(0x05040100u, 0x07060302u); xpose<1>(0x05040100u, 0x07060302u); xpose<2>(0x05040100u, 0x07060302u); xpose<3>(0x05040100u, 0x07060302u); }
constexpr int w2reg(int i) { return i < 8 ? PB + 128 + 4 * i : PB + 4 * (i - 8); }
template <int I> DI void load_w2(v4u srd, unsigned off) {
  unsigned so;
  asm volatile("s_mov_b32 %0, %3\n\tbuffer_load_dwordx4 v[%c4:%c4+3], %1, %2, %0 offen offset:%c5 sc1"
               : "=&s"(so) : "v"(off), "s"(srd), "i"((I >> 2) * 4096), "i"(w2reg(I)), "i"((I & 3) * 1024));
}
template <int A> DI void mfma_p(f32x4& acc, v4u b) { asm volatile("v_mfma_f32_16x16x32_bf16 %0, v[%c2:%c2+3], %1, %0" : "+v"(acc) : "v"(b), "i"(A)); }
template <int A> DI void mfma_p0(f32x4& acc, v4u b, f32x4 c) {
  asm volatile("v_mfma_f32_16x16x32_bf16 %0, v[%c3:%c3+3], %1, %2" : "=&v"(acc) : "v"(b), "v"(c), "i"(A));
}
template <int N> DI void wait_vm() { asm volatile("s_waitcnt vmcnt(%c0)" :: "i"(N)); }
template <int I, int E, typename F> DI void static_for(F&& f) { if constexpr (I < E) { f(std::integral_constant<int, I>{}); static_for<I + 1, E>(f); } }

__global__ __launch_bounds__(512, 2) __attribute__((amdgpu_num_vgpr(PB / 2))) void gin_one(const unsigned short* __restrict__ x, const int* __restrict__ src, const int* __restrict__ dst,
                                                  const unsigned short* __restrict__ epsp, const unsigned short* __restrict__ w1,
                                                  const unsigned short* __restrict__ b1, const unsigned short* __restrict__ w2,
                                                  const unsigned short* __restrict__ b2, unsigned short* __restrict__ out, unsigned char* ws,
                                                  int N, int E, int T) {
  extern __shared__ __attribute__((aligned(16))) unsigned char Ls[];
  LAS unsigned char* L = (LAS unsigned char*)Ls;
  const int tid = threadIdx.x, lane = tid & 63, w = __builtin_amdgcn_readfirstlane(tid >> 6);
  const int t = blockIdx.x, node0 = t * TM;
  const u64 t_begin = __builtin_amdgcn_s_memrealtime();
  if (tid == 0) __hip_atomic_store((GAS u64*)(ws + 16384) + t, t_begin, RLX_AGENT);
  const unsigned bulk = w >> 2, chain = bulk ^ 1;
  unsigned cpf;
  {
    u64 pc; unsigned len;
    asm volatile("s_getpc_b64 %0\n.Lgin_pc:\n\ts_mov_b32 %1, .Lgin_end-.Lgin_pc" : "=s"(pc), "=s"(len));
    cpf = __builtin_amdgcn_raw_buffer_load_b32(rsrc((const void*)pc, len), (((t >> 3) & 31) * 8 + w) * 128 + lane * 32768, 0, 0);
  }
  const __amdgpu_buffer_rsrc_t wsr = rsrc(ws, WS_BYTES), xr = rsrc(x, N * 512), w1r = rsrc(w1, D * H * 2), w2r = rsrc(w2, D * H * 2);
  STAMP_DECL STAMP(0);
  const int q = lane >> 4, m = lane & 15;
  const v4u w1d = {(unsigned)(u64)w1, (unsigned)((u64)w1 >> 32) & 0xffffu, D * H * 2, 0x00020000u};
  const v4u wsd = {(unsigned)(u64)ws, (unsigned)((u64)ws >> 32) & 0xffffu, WS_BYTES, 0x00020000u};
  const unsigned w1off = (((lane >> 3) & 1) * 32 + q * 8) * 1024 + w * 128 + (lane & 7) * 16;
  LAS unsigned* pcnt = (LAS unsigned*)(L + L_PCNT);
  LAS unsigned* pcell = (LAS unsigned*)(L + L_PCELL);

  unsigned wu = 0;
  const int tf = t + (w - 1) * T;
  if (w == 4) {
    PRIO(3);
    const __amdgpu_buffer_rsrc_t dr = rsrc(dst, E * 4), sr = rsrc(src, E * 4);
    v4u dv[3], sv[3];
#pragma unroll
    for (int i = 0; i < 3; ++i) {
      const unsigned o = (i == 2 && lane >= 32) ? OOB : (unsigned)(t * EPB + (lane + 64 * i) * 4) * 4u;
      dv[i] = __builtin_amdgcn_raw_buffer_load_b128(dr, o, 0, 0);
      sv[i] = __builtin_amdgcn_raw_buffer_load_b128(sr, o, 0, 0);
    }
    const u64 ep0 = __hip_atomic_load((GAS u64*)(ws + WS_EPOCH), RLX_AGENT);
    SB();
    *(LAS v4u*)(pcnt + lane * 4) = v4u{0, 0, 0, 0};
    pcnt[256 + lane] = 0;
    const unsigned dummy = 256u + lane;
    unsigned tt[12], rk[12];
#pragma unroll
    for (int i = 0; i < 12; ++i) {
      const int e = t * EPB + (lane + 64 * (i >> 2)) * 4;
      const unsigned lo = (e < E && !(i >= 8 && lane >= 32)) ? 0u : dummy;
      tt[i] = max(min(dv[i >> 2][i & 3] / (unsigned)TM, dummy), lo);
      rk[i] = __hip_atomic_fetch_add(pcnt + tt[i], 1u, RLX_WG);
    }
#pragma unroll
    for (int i = 0; i < 12; ++i)
      pcell[tt[i] * 16 + (rk[i] & 15u)] = (dv[i >> 2][i & 3] - tt[i] * TM) | (min(sv[i >> 2][i & 3], 16383u) << 6);
    *(LAS u64*)(L + L_MISC) = ep0;
  } else {
   __builtin_amdgcn_s_sleep(ESLEEP);
   wu = __builtin_amdgcn_raw_buffer_load_b32(w1r, w == 0 ? ((t >> 3) & 31) * 8192 + lane * 128 : OOB, 0, 0);
   if (w == 3) {
    const v4u v1 = __builtin_amdgcn_raw_buffer_load_b128(rsrc(b1, H * 2), lane * 16, 0, 0);
    const v4u v2 = __builtin_amdgcn_raw_buffer_load_b128(rsrc(b2, D * 2), lane * 16, 0, 0);
    const unsigned ev = epsp[0];
    *(LAS v2u*)(L + L_CNT + lane * 8) = v2u{0, 0};
    *(LAS v4u*)(L + L_B1 + lane * 16) = v1;
    *(LAS v4u*)(L + (lane < 32 ? L_B2 + lane * 16 : L_FLAT + lane * 16)) = v2;
    *(LAS v2u*)(L + L_MISC + 8) = v2u{0u, ev};
   } else if (w == 1 || w == 2) {
    unsigned tv[8];
    tile_w2_load(w2r, tf, lane, tv);
    tile_w2_store(wsr, tf, lane, tv);
   }
  }
  if (chain) { load_w1<0>(w1d, w1off); load_w1<1>(w1d, w1off); }
  PRIO(3);
  bar();
  STAMP(1);
  const u64 tag = *(LAS u64*)(L + L_MISC) + 1;
  const unsigned tagA = (unsigned)tag & 0xFFFFFFu, tagB = tag_b(tag);
  {
    v4u e[2]; unsigned c[2];
#pragma unroll
    for (int i = 0; i < 2; ++i) { e[i] = *(LAS v4u*)(pcell + (w * 2 + i) * 256 + lane * 4); c[i] = pcnt[(w * 2 + i) * 16 + (lane >> 2)]; }
#pragma unroll
    for (int i = 0; i < 2; ++i) {
      const unsigned tc = (w * 2 + i) * 16 + (lane >> 2), cc = min(c[i], 31u);
      const v4u g = {(e[i].x & 0xFFFFFu) | (e[i].y << 20), ((e[i].y >> 12) & 0xFFu) | (tagA << 8), (e[i].z & 0xFFFFFu) | (e[i].w << 20),
                     ((e[i].w >> 12) & 0xFFu) | (cc << 8) | (tagB << 13)};
      __builtin_amdgcn_raw_buffer_store_b128(g, wsr, tc < (unsigned)T ? WS_CELL + tc * 16384 + t * 64 + (lane & 3) * 16 : OOB, 0, SC1);
    }
  }
  SB();
  PRIO(1);
  if (chain) { load_w1<2>(w1d, w1off); load_w1<3>(w1d, w1off); } else __builtin_amdgcn_s_sleep(PSLEEP);
  const bool pv = (tid & 255) < T;
  const unsigned coff = pv ? WS_CELL + t * 16384 + (tid & 255) * 64 + bulk * 32 : OOB;
  STAMP(2);
  PRIO(2);
  v4u ga = __builtin_amdgcn_raw_buffer_load_b128(wsr, coff, 0, SC1);
  v4u gb = __builtin_amdgcn_raw_buffer_load_b128(wsr, coff + 16, 0, SC1);
  SB();
  if (bulk) { PRIO(0); load_w1<0>(w1d, w1off); load_w1<1>(w1d, w1off); }
  bool tmo = false;
  unsigned spins = 0;
#define CELL_OK() (!pv || ((ga.y >> 8) == tagA && (ga.w >> 13) == tagB && (gb.y >> 8) == tagA && (gb.w >> 13) == tagB))
  if (!__all(CELL_OK())) {
    for (;;) {
      const bool ok = CELL_OK();
      const v4u na = __builtin_amdgcn_raw_buffer_load_b128(wsr, ok ? OOB : coff, 0, SC1);
      const v4u nb = __builtin_amdgcn_raw_buffer_load_b128(wsr, ok ? OOB : coff + 16, 0, SC1);
#pragma unroll
      for (int i = 0; i < 4; ++i) { ga[i] = ok ? ga[i] : na[i]; gb[i] = ok ? gb[i] : nb[i]; }
      if (__all(CELL_OK())) break;
      if (++spins > SPIN_CAP) { tmo = true; break; }
    }
    ++spins;
  }
  STAMP(3);
  __builtin_amdgcn_s_waitcnt(0x0F70);
  asm volatile("" :: "v"(wu), "v"(cpf));
  const unsigned cc = (pv && !tmo) ? (ga.w >> 8) & 31u : 0u;
  const int nval = (int)cc - 8 * (int)bulk;
  const bool any = __any(nval > 0);
  bool bad = cc > 16u;
  unsigned ent[8], na4[8], rk[8];
  LAS unsigned* flat = (LAS unsigned*)(L + L_FLAT);
  if (any) {
    ent[0] = ga.x; ent[1] = __builtin_amdgcn_alignbit(ga.y, ga.x, 20); ent[2] = ga.z; ent[3] = __builtin_amdgcn_alignbit(ga.w, ga.z, 20);
    ent[4] = gb.x; ent[5] = __builtin_amdgcn_alignbit(gb.y, gb.x, 20); ent[6] = gb.z; ent[7] = __builtin_amdgcn_alignbit(gb.w, gb.z, 20);
    const unsigned dummy = 40u + (unsigned)(lane % 24);
#pragma unroll
    for (int i = 0; i < 8; ++i) {
      ent[i] = i < nval ? ent[i] & 0xFFFFFu : dummy;
      na4[i] = (ent[i] & 63u) << 2;
      rk[i] = __hip_atomic_fetch_add((LAS unsigned*)(L + L_CNT + bulk * 256 + na4[i]), 1u << (8 * (w & 3)), RLX_WG);
    }
#pragma unroll
    for (int i = 0; i < 8; ++i) { rk[i] = (rk[i] >> (8 * (w & 3))) & 255u; bad |= rk[i] == 255u; }
  }
  if ((__any(bad) || tmo) && lane == 0) __hip_atomic_fetch_or((LAS unsigned*)(L + L_MISC + 8), tmo ? 3u : 1u, RLX_WG);
  if (bulk) load_w1<2>(w1d, w1off);
  bar();
  STAMP(4);
  bool slow, timedout;
  int s0, nit, len[6];
  const int n0 = bulk ? (10 - NCN) * w + (8 * NCN - 40) : NCN * w;
  const int nn = bulk ? 10 - NCN : NCN;
  {
    const unsigned clo = *(LAS unsigned*)(L + L_CNT + lane * 4), chi = *(LAS unsigned*)(L + L_CNT + 256 + lane * 4);
    const unsigned flagv = __builtin_amdgcn_readfirstlane(*(LAS unsigned*)(L + L_MISC + 8));
    const unsigned mk = (1u << (8 * (w & 3))) - 1u;
    const unsigned deg = bsum(clo, chi), below = bsum(clo & (bulk ? ~0u : mk), chi & (bulk ? mk : 0u));
    const unsigned l4 = (deg + 4u) & ~3u;
    const bool lastn = lane < 4 * NCN ? (lane % NCN) == NCN - 1 : ((lane - 4 * NCN) % (10 - NCN)) == 9 - NCN;
    const unsigned plen = lane < TM ? l4 + (lastn ? 4u * (R - 1) : 0u) : 0u;
    const unsigned incl = wave_incl_scan(plen), start = incl - plen;
    timedout = (flagv & 2u) != 0u;
    slow = flagv != 0u || __builtin_amdgcn_readlane(incl, 63) > (unsigned)CAP;
    const unsigned tail = __builtin_amdgcn_readlane(incl, n0 + nn - 1) - 4 * (R - 1);
    s0 = __builtin_amdgcn_readlane(start, n0);
    nit = ((tail - s0) / 4 + R - 1) / R;
#pragma unroll
    for (int j = 0; j < 6; ++j) len[j] = __builtin_amdgcn_readlane(l4, n0 + j) >> 2;
    if (slow) s0 = 0;
    if (!slow) {
      const unsigned st2 = (unsigned)__builtin_amdgcn_ds_bpermute((n0 + (lane >> 3)) << 2, (int)(start | (deg << 16)));
      if (any) {
        const unsigned B4 = L_FLAT + 4u * (lane < TM ? start + 1u + below : CAP + 64u);
        unsigned b[8];
#pragma unroll
        for (int i = 0; i < 8; ++i) b[i] = (unsigned)__builtin_amdgcn_ds_bpermute((int)na4[i], (int)B4);
#pragma unroll
        for (int i = 0; i < 8; ++i) *(LAS unsigned*)(L + b[i] + 4u * rk[i]) = ent[i];
      }
      const unsigned dg = st2 >> 16, k = lane & 7, pp = k == 7 ? 0u : dg + 1u + k;
      const bool mine = (lane >> 3) < nn && pp < ((dg + 4u) & ~3u);
      const unsigned at = mine ? (st2 & 0xFFFFu) + pp : (lane >= 48 && lane - 48 < 4 * (R - 1) ? tail + lane - 48 : CAP + lane);
      flat[at] = (mine && k == 7) ? (unsigned)(node0 + n0 + (lane >> 3)) << 6 : EOOB;
    }
  }
  if (bulk) load_w1<3>(w1d, w1off);
  bar();
  STAMP(5);
  PRIO(2);
  if (w == 1 || w == 2) {
    asm volatile("s_waitcnt vmcnt(0)" ::: "memory");
    if (lane == 0 && tf < 256) __hip_atomic_store((GAS unsigned*)(ws + WS_WFLAG) + tf, (unsigned)tag, RLX_AGENT);
  }
  const unsigned l16 = m * 16;
  LAS unsigned* fp = flat + s0 + q;
  v4u bl[R], br[R];
  unsigned en[R];
#pragma unroll
  for (int i = 0; i < R; ++i) en[i] = fp[4 * i];
  const float epsf = bflo(*(LAS unsigned*)(L + L_MISC + 12));
#pragma unroll
  for (int i = 0; i < R; ++i) {
    const unsigned o = ((en[i] << 3) & ~511u) | l16;
    bl[i] = __builtin_amdgcn_raw_buffer_load_b128(xr, o, 0, 0);
    br[i] = __builtin_amdgcn_raw_buffer_load_b128(xr, o + 256, 0, 0);
    en[i] = fp[4 * (R + i)];
  }
  asm volatile("" ::: "memory");
  SB();
  if (chain) { PRIO(XPRIO); xpose_all(); PRIO(0); } else PRIO(1);
  STAMP(6);
  unsigned wu2 = 0;
  v4u fl0;
  if (__builtin_expect(!slow, 1)) {
    const v4u sel = {m == 0 ? 0x3F80u : (m == 1 ? 0x3F800000u : 0u), m == 2 ? 0x3F80u : (m == 3 ? 0x3F800000u : 0u),
                     m == 4 ? 0x3F80u : (m == 5 ? 0x3F800000u : 0u), m == 6 ? 0x3F80u : (m == 7 ? 0x3F800000u : 0u)};
    const v4u sel8 = {lane == 8 ? 0x3F80u : (lane == 9 ? 0x3F800000u : 0u), lane == 10 ? 0x3F80u : (lane == 11 ? 0x3F800000u : 0u),
                      lane == 12 ? 0x3F80u : (lane == 13 ? 0x3F800000u : 0u), lane == 14 ? 0x3F80u : (lane == 15 ? 0x3F800000u : 0u)};
    fp += 8 * R;
    f32x4 a0 = {0.f, 0.f, 0.f, 0.f}, a1 = {0.f, 0.f, 0.f, 0.f};
    unsigned haddr = L_H + (lane >> 5) * 256 + m * 16 + (q & 1) * 8 + n0 * HP;
    bool first = true;
    int rem = len[0], left = nn;
#define SLOT(i, REFILL) { \
        a0 = mfma(sel, bl[i], a0); \
        a1 = mfma(sel, br[i], a1); \
        if (first) { a0 = mfma(sel8, bl[i], a0); a1 = mfma(sel8, br[i], a1); } \
        if (REFILL) { \
          const unsigned o = ((en[i] << 3) & ~511u) | l16; \
          bl[i] = __builtin_amdgcn_raw_buffer_load_b128(xr, o, 0, 0); \
          br[i] = __builtin_amdgcn_raw_buffer_load_b128(xr, o + 256, 0, 0); \
          en[i] = fp[4 * i]; \
        } \
        first = --rem == 0; \
        if (first) { \
          float f[4]; \
          _Pragma("unroll") for (int r = 0; r < 4; ++r) { \
            auto s2 = __builtin_amdgcn_permlane32_swap(asu(a0[r]), asu(a1[r]), false, false); \
            const unsigned u0 = s2[0], u1 = s2[1]; \
            f[r] = fmaf(epsf, asf(u1), asf(u0)); \
          } \
          *(LAS v2u*)(L + haddr) = v2u{pack_bf16(f[0], f[1]), pack_bf16(f[2], f[3])}; \
          haddr += HP; \
          a0 = f32x4{0.f, 0.f, 0.f, 0.f}; a1 = a0; \
          rem = --left > 0 ? len[1] : 1 << 20; len[1] = len[2]; len[2] = len[3]; len[3] = len[4]; len[4] = len[5]; \
        } }
    wu2 = __builtin_amdgcn_raw_buffer_load_b32(wsr, lane < 8 ? WS_W2T + ((t >> 3) & 31) * 8192 + w * 1024 + lane * 128 : OOB, 0, SC1);
    for (; nit > 1; --nit) {
#pragma unroll
      for (int i = 0; i < R; ++i) SLOT(i, true)
      fp += 4 * R;
    }
    fl0 = __builtin_amdgcn_raw_buffer_load_b128(wsr, WS_WFLAG + lane * 16, 0, SC1);
#pragma unroll
    for (int i = 0; i < R; ++i) SLOT(i, false)
  } else {
#pragma unroll 1
    for (int j = 0; j < nn; ++j) {
      const int node = node0 + n0 + j;
      const v2u s2 = __builtin_amdgcn_raw_buffer_load_b64(xr, (unsigned)node * 512u + lane * 8, 0, 0);
      float a0 = bflo(s2.x), a1 = bfhi(s2.x), a2 = bflo(s2.y), a3 = bfhi(s2.y);
      if (node < N)
        for (int eb = 0; eb < E; eb += 64) {
          const int e = eb + lane;
          const int dv2 = (int)__builtin_amdgcn_raw_buffer_load_b32(rsrc(dst, E * 4), e * 4, 0, 0);
          const int sv2 = (int)__builtin_amdgcn_raw_buffer_load_b32(rsrc(src, E * 4), e * 4, 0, 0);
          u64 mkk = __ballot(dv2 == node && e < E);
          while (mkk) {
            const int b = __builtin_ctzll(mkk); mkk &= mkk - 1;
            const unsigned sidx = (unsigned)__builtin_amdgcn_readlane(sv2, b);
            const v2u rv = __builtin_amdgcn_raw_buffer_load_b64(xr, sidx < (unsigned)N ? sidx * 512u + lane * 8 : OOB, 0, 0);
            a0 += bflo(rv.x); a1 += bfhi(rv.x); a2 += bflo(rv.y); a3 += bfhi(rv.y);
          }
        }
      *(LAS v2u*)(L + L_H + (n0 + j) * HP + lane * 8) =
          v2u{pack_bf16(fmaf(epsf, bflo(s2.x), a0), fmaf(epsf, bfhi(s2.x), a1)), pack_bf16(fmaf(epsf, bflo(s2.y), a2), fmaf(epsf, bfhi(s2.y), a3))};
    }
    fl0 = __builtin_amdgcn_raw_buffer_load_b128(wsr, WS_WFLAG + lane * 16, 0, SC1);
  }
  STAMP(7);
  PRIO(0);
  {
    bool okw = false;
    const unsigned tl = (unsigned)tag;
    for (unsigned sp = 0; sp <= SPIN_CAP; ++sp) {
      if (__all(fl0.x == tl && fl0.y == tl && fl0.z == tl && fl0.w == tl)) { okw = true; break; }
      fl0 = __builtin_amdgcn_raw_buffer_load_b128(wsr, WS_WFLAG + lane * 16, 0, SC1);
    }
    if (__builtin_expect(!okw, 0)) {
#pragma unroll 1
      for (int i = 0; i < 32; ++i) { unsigned tv[8]; tile_w2_load(w2r, w * 32 + i, lane, tv); tile_w2_store(wsr, w * 32 + i, lane, tv); }
      asm volatile("s_waitcnt vmcnt(0)" ::: "memory");
    }
    __builtin_amdgcn_fence(__ATOMIC_ACQUIRE, "wavefront");
  }
  if (!timedout && tid == 192 && t == 0) __hip_atomic_store((GAS u64*)(ws + WS_EPOCH), tag, RLX_AGENT);
  asm volatile("" :: "v"(wu2));
  if (bulk) { PRIO(XPRIO); xpose_all(); PRIO(0); }
  const unsigned w2off = WS_W2T + w * 32768 + lane * 16;
  static_for<0, W2PRE>([&](auto I) { load_w2<I>(wsd, w2off); });
  f32x4 bias1[4];
  {
    v2u bb1[4];
#pragma unroll
    for (int r = 0; r < 4; ++r) bb1[r] = *(LAS v2u*)(L + L_B1 + (64 * w + 32 * (q & 1) + 4 * (q >> 1) + 8 * r) * 2);
#pragma unroll
    for (int c = 0; c < 4; ++c) {
#pragma unroll
      for (int r = 0; r < 4; ++r) { const unsigned u = bb1[r][c >> 1]; bias1[c][r] = (c & 1) ? bfhi(u) : bflo(u); }
      asm volatile("" : "+v"(bias1[c]));
    }
  }
  SB();
  bar();
  STAMP(8);
  f32x4 bias2[2];
  {
    LAS unsigned char* hb = L + L_H + m * HP + q * 16;
    LAS unsigned char* h1w = L + L_H1 + m * H1P + (64 * w + 16 * q) * 2;
    v4u b[2][3];
    f32x4 acc[3][4];
#pragma unroll
    for (int mt = 0; mt < 3; ++mt) b[0][mt] = *(LAS v4u*)(hb + mt * 16 * HP);
    static_for<0, 8>([&](auto KS) {
      constexpr int ks = KS;
      if constexpr (ks < 7) {
#pragma unroll
        for (int mt = 0; mt < 3; ++mt) b[(ks + 1) & 1][mt] = *(LAS v4u*)(hb + mt * 16 * HP + (ks + 1) * 64);
      }
      SB();
      static_for<0, 12>([&](auto J) {
        constexpr int c = J / 3, mt = J % 3;
        if constexpr (ks == 0) mfma_p0<PB + 4 * c>(acc[mt][c], b[0][mt], bias1[c]);
        else mfma_p<PB + 16 * ks + 4 * c>(acc[mt][c], b[ks & 1][mt]);
        if constexpr (J == 5) load_w2<8 + 2 * ks>(wsd, w2off);
        if constexpr (J == 11) load_w2<9 + 2 * ks>(wsd, w2off);
      });
      SB();
    });
    const v4u bb2 = *(LAS v4u*)(L + L_B2 + (32 * w + 8 * q) * 2);
    asm volatile("s_nop 4" : "+v"(acc[0][0]), "+v"(acc[0][1]), "+v"(acc[0][2]), "+v"(acc[0][3]), "+v"(acc[1][0]), "+v"(acc[1][1]), "+v"(acc[1][2]),
                 "+v"(acc[2][0]), "+v"(acc[2][1]), "+v"(acc[2][2]));
#pragma unroll
    for (int mt = 0; mt < 3; ++mt) {
      unsigned o[8];
      if (mt == 1) asm volatile("" : "+v"(acc[1][3]), "+v"(acc[2][3]));
#pragma unroll
      for (int k = 0; k < 8; ++k) o[k] = relu_pack(acc[mt][k >> 1][2 * (k & 1)], acc[mt][k >> 1][2 * (k & 1) + 1]);
      *(LAS v4u*)(h1w + mt * 16 * H1P) = v4u{o[0], o[1], o[2], o[3]};
      *(LAS v4u*)(h1w + mt * 16 * H1P + 16) = v4u{o[4], o[5], o[6], o[7]};
      SB();
    }
#pragma unroll
    for (int nt = 0; nt < 2; ++nt) {
      bias2[nt] = f32x4{bflo(bb2[2 * nt]), bfhi(bb2[2 * nt]), bflo(bb2[2 * nt + 1]), bfhi(bb2[2 * nt + 1])};
      asm volatile("" : "+v"(bias2[nt]));
    }
  }
  SB();
  STAMP(9);
  bar();
  v2u ep;
  {
    f32x4 acc[3][2];
    LAS unsigned char* hb = L + L_H1 + m * H1P + q * 16;
    v4u b[3][3];
#pragma unroll
    for (int i = 0; i < 2; ++i)
#pragma unroll
      for (int mt = 0; mt < 3; ++mt) b[i][mt] = *(LAS v4u*)(hb + mt * 16 * H1P + i * 64);
    static_for<0, 16>([&](auto KS) {
      constexpr int ks = KS;
      if constexpr (ks + 2 < 16) {
#pragma unroll
        for (int mt = 0; mt < 3; ++mt) b[(ks + 2) % 3][mt] = *(LAS v4u*)(hb + mt * 16 * H1P + (ks + 2) * 64);
      }
      if constexpr (ks < 8) load_w2<24 + ks>(wsd, w2off);
      constexpr int issued = ks < 8 ? 25 + ks : 32;
      wait_vm<issued - (2 * ks + 2)>();
      SB();
      static_for<0, 6>([&](auto J) {
        constexpr int nt = J / 3, mt = J % 3;
        if constexpr (ks == 0) mfma_p0<w2reg(nt)>(acc[mt][nt], b[0][mt], bias2[nt]);
        else mfma_p<w2reg(2 * ks + nt)>(acc[mt][nt], b[ks % 3][mt]);
      });
      SB();
    });
    ep = __builtin_amdgcn_raw_buffer_load_b64(wsr, tid == 192 ? WS_EPOCH : OOB, 0, SC1);
    asm volatile("s_nop 7" : "+v"(acc[0][0]), "+v"(acc[0][1]), "+v"(acc[1][0]), "+v"(acc[1][1]), "+v"(acc[2][0]), "+v"(acc[2][1]));
    const __amdgpu_buffer_rsrc_t outr = rsrc(out, N * 512);
#pragma unroll
    for (int mt = 0; mt < 3; ++mt) {
      const int r = 16 * mt + m;
      v4u o;
      o.x = pack_bf16(acc[mt][0][0], acc[mt][0][1]); o.y = pack_bf16(acc[mt][0][2], acc[mt][0][3]);
      o.z = pack_bf16(acc[mt][1][0], acc[mt][1][1]); o.w = pack_bf16(acc[mt][1][2], acc[mt][1][3]);
      __builtin_amdgcn_raw_buffer_store_b128(o, outr, r < TM ? (unsigned)(node0 + r) * 512u + 64 * w + 16 * q : OOB, 0, OUTAUX);
    }
  }
  if (!timedout && tid == 192 && (ep.x != (unsigned)tag || ep.y != (unsigned)(tag >> 32))) __hip_atomic_store((GAS u64*)(ws + WS_EPOCH), tag, RLX_AGENT);
  STAMP(10);
  STAMP_OUT((unsigned long long*)(ws + (64u << 20)), blockIdx.x * 8 + w);
  while (__builtin_amdgcn_s_memrealtime() - t_begin < 2500) __builtin_amdgcn_s_sleep(1);
  {
    u64 mn = MINMAX ? 0ull : ~0ull;
    for (int i = 0; i < 4; ++i) { const int b = lane + 64 * i; if (b < T) { const u64 v = __hip_atomic_load((GAS u64*)(ws + 16384) + b, RLX_AGENT); mn = MINMAX ? (v > mn ? v : mn) : (v < mn ? v : mn); } }
    for (int o = 32; o; o >>= 1) { const u64 v = __shfl_xor(mn, o); mn = MINMAX ? (v > mn ? v : mn) : (v < mn ? v : mn); }
    while (__builtin_amdgcn_s_memrealtime() - mn < 4000) __builtin_amdgcn_s_sleep(1);
  }
  asm volatile(".Lgin_end:");
}

extern "C" void kernel_launch(void* const* d_in, const int* in_sizes, int n_in, void* d_out, int out_size, void* d_ws, size_t ws_size,
                              hipStream_t stream) {
  const int N = in_sizes[0] / D, E = in_sizes[1];
  const int T = (N + TM - 1) / TM;
  (void)hipFuncSetAttribute((const void*)gin_one, hipFuncAttributeMaxDynamicSharedMemorySize, L_END);
  gin_one<<<T, 512, L_END, stream>>>((const unsigned short*)d_in[0], (const int*)d_in[1], (const int*)d_in[2], (const unsigned short*)d_in[3],
                                 (const unsigned short*)d_in[4], (const unsigned short*)d_in[5], (const unsigned short*)d_in[6],
                                 (const unsigned short*)d_in[7], (unsigned short*)d_out, (unsigned char*)d_ws, N, E, T);
}
```

```cpp
#include <hip/hip_runtime.h>
#include <hip/hip_bf16.h>
#include <stdint.h>

typedef unsigned v4u __attribute__((ext_vector_type(4)));
typedef unsigned v2u __attribute__((ext_vector_type(2)));
typedef float f32x4 __attribute__((ext_vector_type(4)));
typedef float f32x2 __attribute__((ext_vector_type(2)));
typedef short bf16x8 __attribute__((ext_vector_type(8)));
typedef __bf16 bf16x2_t __attribute__((ext_vector_type(2)));
typedef unsigned long long u64;
#define GAS __attribute__((address_space(1)))
#define LAS __attribute__((address_space(3)))
#define DI __device__ __forceinline__
#define RLX_AGENT __ATOMIC_RELAXED, __HIP_MEMORY_SCOPE_AGENT
#define RLX_WG __ATOMIC_RELAXED, __HIP_MEMORY_SCOPE_WORKGROUP
#define SB() __builtin_amdgcn_sched_barrier(0)
#define STAMP(i)
#define STAMP_DECL
#define STAMP_OUT(base, idx)

constexpr int D = 256, H = 512, TM = 40;
constexpr int EPB = 640;
constexpr int CAP = 1536;
constexpr int HP = 544, H1P = 1056;
constexpr unsigned OOB = 0x40000000u;
#define RING 3
constexpr int R = RING;
constexpr int SC1 = 16;
constexpr unsigned SPIN_CAP = 4096;
#define W2AUX 16
#define CODE_PF 1
#define NA 16
#define NB 24
#define PSLEEP 0
#define OUTAUX 0
#define CELL_MODE 1
#define W2PRE 6
constexpr unsigned WS_EPOCH = 0, WS_WFLAG = 4096, WS_W2T = 65536, WS_CELL = 1u << 20, WS_BYTES = WS_CELL + 256u * 16384u;
constexpr int L_H = 0, L_H1 = L_H + 48 * HP, L_FLAT = L_H1 + 48 * H1P, L_CNT = L_FLAT + (CAP + 128) * 4, L_MISC = L_CNT + 128 * 8, L_B1 = L_MISC + 32, L_B2 = L_B1 + 1024, L_END = L_B2 + 512;
constexpr int L_PCELL = L_H1, L_PCNT = L_H1 + 320 * 64;

DI unsigned pack_bf16(float a, float b) { f32x2 v = {a, b}; bf16x2_t r = __builtin_convertvector(v, bf16x2_t); return __builtin_bit_cast(unsigned, r); }
DI float bflo(unsigned u) { return __builtin_bit_cast(float, u << 16); }
DI float bfhi(unsigned u) { return __builtin_bit_cast(float, u & 0xffff0000u); }
DI float asf(unsigned u) { return __builtin_bit_cast(float, u); }
DI unsigned asu(float u) { return __builtin_bit_cast(unsigned, u); }
DI f32x4 mfma(v4u a, v4u b, f32x4 c) {
  return __builtin_amdgcn_mfma_f32_16x16x32_bf16(__builtin_bit_cast(bf16x8, a), __builtin_bit_cast(bf16x8, b), c, 0, 0, 0);
}
DI float vmax(float a, float b) { float r; asm("v_max_f32 %0, %1, %2" : "=v"(r) : "v"(a), "v"(b)); return r; }
DI unsigned relu_pack(float a, float b) { const f32x2 h = {a, b}; const f32x2 z = h * 0.0f; return pack_bf16(vmax(a, z.x), vmax(b, z.y)); }
DI void bar() { asm volatile("s_waitcnt lgkmcnt(0)" ::: "memory"); __builtin_amdgcn_s_barrier(); asm volatile("" ::: "memory"); }
template <int CTRL> DI unsigned dpp_add(unsigned x) { return x + (unsigned)__builtin_amdgcn_update_dpp(0, (int)x, CTRL, 0xF, 0xF, false); }
DI unsigned wave_incl_scan(unsigned x) {
  x = dpp_add<0x111>(x); x = dpp_add<0x112>(x); x = dpp_add<0x114>(x); x = dpp_add<0x118>(x);
  x = x + (unsigned)__builtin_amdgcn_update_dpp(0, (int)x, 0x142, 0xA, 0xF, false);
  x = x + (unsigned)__builtin_amdgcn_update_dpp(0, (int)x, 0x143, 0xC, 0xF, false);
  return x;
}
DI unsigned bytesum64(u64 c) { return __builtin_amdgcn_sad_u8((unsigned)c, 0u, __builtin_amdgcn_sad_u8((unsigned)(c >> 32), 0u, 0u)); }
DI __amdgpu_buffer_rsrc_t rsrc(const void* p, unsigned bytes) { return __builtin_amdgcn_make_buffer_rsrc((void*)p, 0, bytes, 0x00020000); }
DI unsigned tag_b(u64 tag) { return ((unsigned)tag + (unsigned)(tag >> 24) * 0x9E3779B1u) & 0x7FFFFu; }

DI void tile_w2_load(__amdgpu_buffer_rsrc_t w2r, int f, int lane, unsigned (&v)[8]) {
  const int ww = f >> 5, ks = (f >> 1) & 15, nt = f & 1, i = lane & 15, kg = lane >> 4;
  const int qo = (2 * ks + (kg >> 1)) & 3;
  const int row0 = 64 * (ks >> 1) + 32 * (qo & 1) + 4 * (qo >> 1) + 2 * (kg & 1);
  const unsigned o = f < 256 ? (row0 * D + 32 * ww + 8 * (i >> 2) + 4 * nt + (i & 3)) * 2 : OOB;
#pragma unroll
  for (int j = 0; j < 8; ++j) v[j] = __builtin_amdgcn_raw_buffer_load_b16(w2r, o + (j >> 2) * D * 2, (j & 3) * 8 * D * 2, 0);
}
DI void tile_w2_store(__amdgpu_buffer_rsrc_t wsr, int f, int lane, const unsigned (&v)[8]) {
  const v4u o = {v[0] | (v[1] << 16), v[2] | (v[3] << 16), v[4] | (v[5] << 16), v[6] | (v[7] << 16)};
  __builtin_amdgcn_raw_buffer_store_b128(o, wsr, f < 256 ? WS_W2T + f * 1024 + lane * 16 : OOB, 0, SC1);
}
template <int DS, int C0 = 0, int C1 = 4> DI void xpose(const v4u (&raw)[32], v4u (&wf)[32]) {
#pragma unroll
  for (int c = C0; c < C1; ++c)
#pragma unroll
    for (int d = 0; d < 4; ++d) {
      const unsigned sel = (c & 1) ? 0x07060302u : 0x05040100u;
      const unsigned ta = __builtin_amdgcn_perm(raw[8 * DS + 2 * d + 1][c >> 1], raw[8 * DS + 2 * d][c >> 1], sel);
      const unsigned tb = __builtin_amdgcn_perm(raw[8 * DS + 2 * d + 1][(c >> 1) + 2], raw[8 * DS + 2 * d][(c >> 1) + 2], sel);
      wf[(2 * DS) * 4 + c][d] = (unsigned)__builtin_amdgcn_update_dpp((int)ta, (int)tb, 0x128, 0xF, 0xC, false);
      wf[(2 * DS + 1) * 4 + c][d] = (unsigned)__builtin_amdgcn_update_dpp((int)tb, (int)ta, 0x128, 0xF, 0x3, false);
    }
}
template <int LO, int HI> DI void load_w1(__amdgpu_buffer_rsrc_t w1r, unsigned w1off, v4u (&raw)[32]) {
#pragma unroll
  for (int i = LO; i < HI; ++i) raw[i] = __builtin_amdgcn_raw_buffer_load_b128(w1r, w1off + (i & 3) * 1024, ((i >> 3) * 64 + (i & 4)) * 1024, 0);
  SB();
}

__global__ __launch_bounds__(512, 2) void gin_one(const unsigned short* __restrict__ x, const int* __restrict__ src, const int* __restrict__ dst,
                                                  const unsigned short* __restrict__ epsp, const unsigned short* __restrict__ w1,
                                                  const unsigned short* __restrict__ b1, const unsigned short* __restrict__ w2,
                                                  const unsigned short* __restrict__ b2, unsigned short* __restrict__ out, unsigned char* ws,
                                                  int N, int E, int T) {
  __shared__ __attribute__((aligned(16))) unsigned char Ls[L_END];
  LAS unsigned char* L = (LAS unsigned char*)Ls;
  const int tid = threadIdx.x, lane = tid & 63, w = __builtin_amdgcn_readfirstlane(tid >> 6);
  const int t = blockIdx.x, node0 = t * TM;
  const u64 t_begin = __builtin_amdgcn_s_memrealtime();
  unsigned cpf = 0;
  {
    u64 pc; unsigned len;
    asm volatile("s_getpc_b64 %0\n.Lgin_pc:\n\ts_mov_b32 %1, .Lgin_end-.Lgin_pc" : "=s"(pc), "=s"(len));
    const unsigned o = (((t >> 3) & 31) * 8 + ((tid >> 6) & 7)) * 128 + lane * 32768;
    cpf = __builtin_amdgcn_raw_buffer_load_b32(rsrc((const void*)pc, len), o, 0, 0);
  }
  const __amdgpu_buffer_rsrc_t wsr = rsrc(ws, WS_BYTES), xr = rsrc(x, N * 512), w1r = rsrc(w1, D * H * 2), w2r = rsrc(w2, D * H * 2);
  STAMP_DECL STAMP(0);
  const int q = lane >> 4, m = lane & 15;
  v4u raw[32], wf[32];
  const unsigned w1off = (((lane >> 3) & 1) * 32 + q * 8) * 1024 + w * 128 + (lane & 7) * 16;
  LAS unsigned* pcnt = (LAS unsigned*)(L + L_PCNT);
  LAS unsigned* pcell = (LAS unsigned*)(L + L_PCELL);

  v4u dv[3], sv[3];
  u64 tag = 0;
  const int tf = t + (w - 1) * T;
  unsigned tv[8];
  if (w == 0) {
    __builtin_amdgcn_s_setprio(3);
    const __amdgpu_buffer_rsrc_t dr = rsrc(dst, E * 4), sr = rsrc(src, E * 4);
#pragma unroll
    for (int i = 0; i < 3; ++i) {
      const unsigned o = (i == 2 && lane >= 32) ? OOB : (unsigned)(t * EPB + (lane + 64 * i) * 4) * 4u;
      dv[i] = __builtin_amdgcn_raw_buffer_load_b128(dr, o, 0, 0);
      sv[i] = __builtin_amdgcn_raw_buffer_load_b128(sr, o, 0, 0);
    }
    tag = __hip_atomic_load((GAS u64*)(ws + WS_EPOCH), RLX_AGENT);
  } else if (w == 3) {
    dv[0] = __builtin_amdgcn_raw_buffer_load_b128(rsrc(b1, H * 2), lane * 16, 0, 0);
    dv[1] = __builtin_amdgcn_raw_buffer_load_b128(rsrc(b2, D * 2), lane * 16, 0, 0);
    tv[0] = epsp[0];
  } else if (w <= 2) {
    __builtin_amdgcn_s_setprio(3);
    tile_w2_load(w2r, tf, lane, tv);
  }
  const unsigned wu = __builtin_amdgcn_raw_buffer_load_b32(w1r, lane < 8 ? ((t >> 3) & 31) * 8192 + w * 1024 + lane * 128 : OOB, 0, 0);
  SB();
  if (w == 0) {
    *(LAS v4u*)(pcnt + lane * 4) = v4u{0, 0, 0, 0};
    pcnt[256 + lane] = 0;
    const unsigned dummy = 256u + lane;
    unsigned tt[12], rk[12];
#pragma unroll
    for (int i = 0; i < 12; ++i) {
      const int e = t * EPB + (lane + 64 * (i >> 2)) * 4;
      const unsigned lo = (e < E && !(i >= 8 && lane >= 32)) ? 0u : dummy;
      tt[i] = max(min(dv[i >> 2][i & 3] / (unsigned)TM, dummy), lo);
      rk[i] = __hip_atomic_fetch_add(pcnt + tt[i], 1u, RLX_WG);
    }
#pragma unroll
    for (int i = 0; i < 12; ++i)
      pcell[tt[i] * 16 + (rk[i] & 15u)] = min(sv[i >> 2][i & 3], 16383u) | ((dv[i >> 2][i & 3] - tt[i] * TM) << 14);
    *(LAS u64*)(L + L_MISC) = tag;
  } else {
    if (w == 4) *(LAS v4u*)(L + L_CNT + lane * 16) = v4u{0, 0, 0, 0};
    if (tid == 256) *(LAS unsigned*)(L + L_MISC + 8) = 0u;
    if (w <= 2) {
      tile_w2_store(wsr, tf, lane, tv);
      asm volatile("s_waitcnt vmcnt(0)" ::: "memory");
    } else {
      load_w1<0, NA>(w1r, w1off, raw);
    }
  }
  __builtin_amdgcn_s_setprio(3);
  bar();
  STAMP(1);
  tag = *(LAS u64*)(L + L_MISC) + 1;
  const unsigned tagA = (unsigned)tag & 0xFFFFFFu, tagB = tag_b(tag);
  const unsigned k4 = (lane & 3) * 4;
  {
    v4u e[2]; unsigned c[2];
#pragma unroll
    for (int i = 0; i < 2; ++i) { e[i] = *(LAS v4u*)(pcell + (w * 2 + i) * 256 + lane * 4); c[i] = pcnt[(w * 2 + i) * 16 + (lane >> 2)]; }
#pragma unroll
    for (int i = 0; i < 2; ++i) {
      const unsigned tc = (w * 2 + i) * 16 + (lane >> 2), cc = min(c[i], 31u);
      const v4u g = {(e[i].x & 0xFFFFFu) | (e[i].y << 20), ((e[i].y >> 12) & 0xFFu) | (tagA << 8), (e[i].z & 0xFFFFFu) | (e[i].w << 20),
                     ((e[i].w >> 12) & 0xFFu) | (cc << 8) | (tagB << 13)};
      __builtin_amdgcn_raw_buffer_store_b128(g, wsr, tc < (unsigned)T ? WS_CELL + tc * 16384 + t * 64 + k4 * 4 : OOB, 0, SC1);
    }
  }
  SB();
  __builtin_amdgcn_s_setprio(1);
  if (w <= 2) {
    load_w1<0, NA>(w1r, w1off, raw);
  } else if (w == 3) {
    *(LAS v4u*)(L + L_B1 + lane * 16) = dv[0];
    if (lane < 32) *(LAS v4u*)(L + L_B2 + lane * 16) = dv[1];
    *(LAS unsigned*)(L + L_MISC + 12) = tv[0];
  }
  SB();
  load_w1<NA, NB>(w1r, w1off, raw);
  const bool pv0 = (tid >> 2) < T, pv1 = 128 + (tid >> 2) < T;
  const unsigned coff0 = pv0 ? WS_CELL + t * 16384 + tid * 16 : OOB, coff1 = pv1 ? WS_CELL + t * 16384 + 8192 + tid * 16 : OOB;
  STAMP(2);
  v4u ga = __builtin_amdgcn_raw_buffer_load_b128(wsr, coff0, 0, SC1);
  v4u gb = __builtin_amdgcn_raw_buffer_load_b128(wsr, coff1, 0, SC1);
  SB();
  __builtin_amdgcn_s_setprio(0);
  load_w1<NB, 32>(w1r, w1off, raw);
  __builtin_amdgcn_s_setprio(2);
  asm volatile("" :: "v"(wu), "v"(cpf));
  unsigned ca, cb, spins_out = 0, nspin = 0;
  bool bad = false, tmo = false;
  for (unsigned spins = 0;; ++spins) {
    const bool va = (ga.y >> 8) == tagA && (ga.w >> 13) == tagB, vb = (gb.y >> 8) == tagA && (gb.w >> 13) == tagB;
    ca = (unsigned)__builtin_amdgcn_update_dpp(0, (int)(va ? (ga.w >> 8) & 31u : 32u), 0x00, 0xF, 0xF, false);
    cb = (unsigned)__builtin_amdgcn_update_dpp(0, (int)(vb ? (gb.w >> 8) & 31u : 32u), 0x00, 0xF, 0xF, false);
    ca = pv0 ? ca : 0u; cb = pv1 ? cb : 0u;
    const bool da = va || ca <= k4, db = vb || cb <= k4;
    spins_out = spins;
    if (__all(da && db && ca < 32u && cb < 32u)) break;
    if (spins > SPIN_CAP) { tmo = true; ca = cb = 0; break; }
    const v4u na = __builtin_amdgcn_raw_buffer_load_b128(wsr, va ? OOB : coff0, 0, SC1);
    const v4u nb = __builtin_amdgcn_raw_buffer_load_b128(wsr, vb ? OOB : coff1, 0, SC1);
    if (!va) ga = na;
    if (!vb) gb = nb;
  }
  STAMP(3);
  bad |= ca > 16u || cb > 16u;
  unsigned ent[8], rk[8];
  ent[0] = ga.x & 0xFFFFFu; ent[1] = (ga.x >> 20) | ((ga.y & 0xFFu) << 12); ent[2] = ga.z & 0xFFFFFu; ent[3] = (ga.z >> 20) | ((ga.w & 0xFFu) << 12);
  ent[4] = gb.x & 0xFFFFFu; ent[5] = (gb.x >> 20) | ((gb.y & 0xFFu) << 12); ent[6] = gb.z & 0xFFFFFu; ent[7] = (gb.z >> 20) | ((gb.w & 0xFFu) << 12);
  LAS u64* cnt = (LAS u64*)(L + L_CNT);
  LAS unsigned* flat = (LAS unsigned*)(L + L_FLAT);
  u64 old[8];
#pragma unroll
  for (int i = 0; i < 8; ++i) {
    const bool ok = k4 + (i & 3) < (i < 4 ? ca : cb);
    if (!ok) ent[i] = (64u + lane) << 14;
    old[i] = __hip_atomic_fetch_add(cnt + (ent[i] >> 14), 1ull << (8 * w), RLX_WG);
  }
#pragma unroll
  for (int i = 0; i < 8; ++i) { rk[i] = (unsigned)(old[i] >> (8 * w)) & 255u; bad |= rk[i] == 255u; }
  if ((__any(bad) || tmo) && lane == 0) __hip_atomic_fetch_or((LAS unsigned*)(L + L_MISC + 8), tmo ? 3u : 1u, RLX_WG);
  bar();
  STAMP(4);
  bool slow, timedout;
  int s0, nit;
  {
    const u64 c = cnt[lane];
    const unsigned flagv = __builtin_amdgcn_readfirstlane(*(LAS unsigned*)(L + L_MISC + 8));
    const unsigned deg = bytesum64(c), below = bytesum64(c & ((1ull << (8 * w)) - 1ull));
    const unsigned l4 = (deg + 4u) & ~3u;
    const bool lastn = (lane % 5) == 4;
    const unsigned plen = lane < TM ? l4 + (lastn ? 4u * (R - 1) : 0u) : 0u;
    const unsigned incl = wave_incl_scan(plen), start = incl - plen;
    timedout = (flagv & 2u) != 0u;
    slow = flagv != 0u || __builtin_amdgcn_readlane(incl, 63) > (unsigned)CAP;
    const unsigned B = (start + 1u + below) | ((start + l4 - 4u) << 16);
    const unsigned tail = __builtin_amdgcn_readlane(incl, 5 * w + 4) - 4 * (R - 1);
    s0 = __builtin_amdgcn_readlane(start, 5 * w);
    nit = ((tail - s0) / 4 + R - 1) / R;
    if (slow) s0 = 0;
    if (!slow) {
      unsigned b[8];
#pragma unroll
      for (int i = 0; i < 8; ++i) b[i] = (unsigned)__builtin_amdgcn_ds_bpermute((int)((ent[i] >> 14) << 2), (int)B);
      const unsigned st2 = (unsigned)__builtin_amdgcn_ds_bpermute((int)((5 * w + (lane >> 3)) << 2), (int)(start | (deg << 16)));
#pragma unroll
      for (int i = 0; i < 8; ++i) {
        const unsigned pos = (b[i] & 0xFFFFu) + rk[i];
        flat[ent[i] >= (64u << 14) ? CAP + 64 + lane : pos] = ((ent[i] & 0x3FFFu) << 9) | (pos >= (b[i] >> 16) ? 1u : 0u);
      }
      const unsigned dg = st2 >> 16, e4 = (dg + 4u) & ~3u, k = lane & 7, pp = k == 7 ? 0u : dg + 1u + k;
      const unsigned selfe = (unsigned)(node0 + 5 * w + (lane >> 3)) * 512u | (e4 == 4u ? 1u : 0u);
      const unsigned at = lane >= 40 ? (lane - 40 < 4 * (R - 1) ? tail + lane - 40 : CAP + 64 + lane) : (pp < e4 ? (st2 & 0xFFFFu) + pp : CAP + 64 + lane);
      flat[at] = lane >= 40 ? OOB : (k == 7 ? selfe : OOB | 1u);
    }
  }
  bar();
  STAMP(5);
  __builtin_amdgcn_s_setprio(0);
  const unsigned l16 = m * 16;
  LAS unsigned* fp = flat + s0 + q;
  v4u bl[R], br[R];
  unsigned en[R], mk[R];
#pragma unroll
  for (int i = 0; i < R; ++i) en[i] = fp[4 * i];
  const float epsf = bflo(*(LAS unsigned*)(L + L_MISC + 12));
#pragma unroll
  for (int i = 0; i < R; ++i) {
    const unsigned o = (en[i] & ~1u) + l16;
    mk[i] = __builtin_amdgcn_readfirstlane(en[i]) & 1u;
    bl[i] = __builtin_amdgcn_raw_buffer_load_b128(xr, o, 0, 0);
    br[i] = __builtin_amdgcn_raw_buffer_load_b128(xr, o + 256, 0, 0);
    en[i] = fp[4 * (R + i)];
  }
  SB();
  xpose<0>(raw, wf);
  xpose<1>(raw, wf);
  SB();
  STAMP(6);
  unsigned wu2 = 0;
  auto gather = [&]() __attribute__((always_inline)) {
  if (__builtin_expect(!slow, 1)) {
    const v4u sel = {m == 0 ? 0x3F80u : (m == 1 ? 0x3F800000u : 0u), m == 2 ? 0x3F80u : (m == 3 ? 0x3F800000u : 0u),
                     m == 4 ? 0x3F80u : (m == 5 ? 0x3F800000u : 0u), m == 6 ? 0x3F80u : (m == 7 ? 0x3F800000u : 0u)};
    const v4u sel8 = {lane == 8 ? 0x3F80u : (lane == 9 ? 0x3F800000u : 0u), lane == 10 ? 0x3F80u : (lane == 11 ? 0x3F800000u : 0u),
                      lane == 12 ? 0x3F80u : (lane == 13 ? 0x3F800000u : 0u), lane == 14 ? 0x3F80u : (lane == 15 ? 0x3F800000u : 0u)};
    fp += 8 * R;
    f32x4 a0 = {0.f, 0.f, 0.f, 0.f}, a1 = {0.f, 0.f, 0.f, 0.f};
    unsigned haddr = L_H + (lane >> 5) * 256 + m * 16 + (q & 1) * 8 + 5 * w * HP;
    unsigned first = 1;
#define SLOT(i, REFILL) { \
        a0 = mfma(sel, bl[i], a0); \
        a1 = mfma(sel, br[i], a1); \
        if (first) { a0 = mfma(sel8, bl[i], a0); a1 = mfma(sel8, br[i], a1); } \
        first = mk[i]; \
        if (REFILL) { \
          const unsigned o = (en[i] & ~1u) + l16; \
          mk[i] = __builtin_amdgcn_readfirstlane(en[i]) & 1u; \
          bl[i] = __builtin_amdgcn_raw_buffer_load_b128(xr, o, 0, 0); \
          br[i] = __builtin_amdgcn_raw_buffer_load_b128(xr, o + 256, 0, 0); \
          en[i] = fp[4 * i]; \
        } \
        if (first) { \
          float f[4]; \
          _Pragma("unroll") for (int r = 0; r < 4; ++r) { \
            auto s2 = __builtin_amdgcn_permlane32_swap(asu(a0[r]), asu(a1[r]), false, false); \
            const unsigned u0 = s2[0], u1 = s2[1]; \
            f[r] = fmaf(epsf, asf(u1), asf(u0)); \
          } \
          *(LAS v2u*)(L + haddr) = v2u{pack_bf16(f[0], f[1]), pack_bf16(f[2], f[3])}; \
          haddr += HP; \
          a0 = f32x4{0.f, 0.f, 0.f, 0.f}; a1 = a0; \
        } }
    wu2 = __builtin_amdgcn_raw_buffer_load_b32(wsr, lane < 8 ? WS_W2T + ((t >> 3) & 31) * 8192 + w * 1024 + lane * 128 : OOB, 0, SC1);
    for (; nit > 1; --nit) {
#pragma unroll
      for (int i = 0; i < R; ++i) SLOT(i, true)
      fp += 4 * R;
    }
#pragma unroll
    for (int i = 0; i < R; ++i) SLOT(i, false)
  } else {
#pragma unroll
    for (int i = 0; i < R; ++i) asm volatile("" :: "v"(bl[i]), "v"(br[i]));
#pragma unroll 1
    for (int j = 0; j < 5; ++j) {
      const int node = node0 + 5 * w + j;
      const v2u s2 = __builtin_amdgcn_raw_buffer_load_b64(xr, (unsigned)node * 512u + lane * 8, 0, 0);
      float a0 = bflo(s2.x), a1 = bfhi(s2.x), a2 = bflo(s2.y), a3 = bfhi(s2.y);
      if (node < N)
        for (int eb = 0; eb < E; eb += 64) {
          const int e = eb + lane;
          const int dv2 = (int)__builtin_amdgcn_raw_buffer_load_b32(rsrc(dst, E * 4), e * 4, 0, 0), sv2 = (int)__builtin_amdgcn_raw_buffer_load_b32(rsrc(src, E * 4), e * 4, 0, 0);
          u64 mkk = __ballot(dv2 == node && e < E);
          while (mkk) {
            const int b = __builtin_ctzll(mkk); mkk &= mkk - 1;
            const unsigned sidx = (unsigned)__builtin_amdgcn_readlane(sv2, b);
            const v2u rv = __builtin_amdgcn_raw_buffer_load_b64(xr, sidx < (unsigned)N ? sidx * 512u + lane * 8 : OOB, 0, 0);
            a0 += bflo(rv.x); a1 += bfhi(rv.x); a2 += bflo(rv.y); a3 += bfhi(rv.y);
          }
        }
      *(LAS v2u*)(L + L_H + (5 * w + j) * HP + lane * 8) =
          v2u{pack_bf16(fmaf(epsf, bflo(s2.x), a0), fmaf(epsf, bfhi(s2.x), a1)), pack_bf16(fmaf(epsf, bflo(s2.y), a2), fmaf(epsf, bfhi(s2.y), a3))};
    }
  }
  };
  gather();
  STAMP(7);
  if (__builtin_expect(timedout, 0)) {
#pragma unroll 1
    for (int i = 0; i < 32; ++i) { tile_w2_load(w2r, w * 32 + i, lane, tv); tile_w2_store(wsr, w * 32 + i, lane, tv); }
    asm volatile("s_waitcnt vmcnt(0)" ::: "memory");
  } else if (tid == 192 && t == 0) {
    __hip_atomic_store((GAS u64*)(ws + WS_EPOCH), tag, RLX_AGENT);
  }
  asm volatile("" :: "v"(wu2));
  v4u w2f[32];
  const unsigned w2off = WS_W2T + w * 32768 + lane * 16;
#define LOAD_W2(i) w2f[i] = __builtin_amdgcn_raw_buffer_load_b128(wsr, w2off + ((i) & 3) * 1024, ((i) >> 2) * 4096, SC1)
#pragma unroll
  for (int i = 0; i < W2PRE; ++i) LOAD_W2(i);
  SB();
  xpose<2>(raw, wf);
  f32x4 bias1[4];
  {
    v2u bb1[4];
#pragma unroll
    for (int r = 0; r < 4; ++r) bb1[r] = *(LAS v2u*)(L + L_B1 + (64 * w + 32 * (q & 1) + 4 * (q >> 1) + 8 * r) * 2);
#pragma unroll
    for (int c = 0; c < 4; ++c)
#pragma unroll
      for (int r = 0; r < 4; ++r) { const unsigned u = bb1[r][c >> 1]; bias1[c][r] = (c & 1) ? bfhi(u) : bflo(u); }
  }
  SB();
  bar();
  STAMP(8);
  {
    constexpr int PD = 4;
    LAS unsigned char* hb = L + L_H + m * HP + q * 16;
    LAS unsigned char* h1w = L + L_H1 + m * H1P + (64 * w + 16 * q) * 2;
    v4u b[PD];
    f32x4 acc[2][4];
    unsigned o[8];
#pragma unroll
    for (int i = 0; i < PD; ++i) b[i] = *(LAS v4u*)(hb + i * 64);
#pragma unroll
    for (int st = 0; st < 24; ++st) {
      const int mt = st >> 3, ks = st & 7;
      const v4u bc = b[st % PD];
#pragma unroll
      for (int c = 0; c < 4; ++c) acc[mt & 1][c] = mfma(wf[ks * 4 + c], bc, ks == 0 ? bias1[c] : acc[mt & 1][c]);
      if (st + PD < 24) b[st % PD] = *(LAS v4u*)(hb + ((st + PD) >> 3) * 16 * HP + ((st + PD) & 7) * 64);
      if (mt < 2) { if (ks & 1) LOAD_W2(W2PRE + mt * 4 + (ks >> 1)); } else LOAD_W2(W2PRE + 8 + ks);
      if (st == 0) xpose<3, 0, 1>(raw, wf);
      if (st == 1) xpose<3, 1, 2>(raw, wf);
      if (st == 2) xpose<3, 2, 3>(raw, wf);
      if (st == 3) xpose<3, 3, 4>(raw, wf);
      if (mt > 0) {
        o[ks] = relu_pack(acc[(mt - 1) & 1][ks >> 1][2 * (ks & 1)], acc[(mt - 1) & 1][ks >> 1][2 * (ks & 1) + 1]);
        if ((ks & 3) == 3) *(LAS v4u*)(h1w + (mt - 1) * 16 * H1P + (ks >> 2) * 16) = v4u{o[ks - 3], o[ks - 2], o[ks - 1], o[ks]};
      }
      SB();
    }
#pragma unroll
    for (int ks = 0; ks < 8; ++ks) o[ks] = relu_pack(acc[0][ks >> 1][2 * (ks & 1)], acc[0][ks >> 1][2 * (ks & 1) + 1]);
    *(LAS v4u*)(h1w + 32 * H1P) = v4u{o[0], o[1], o[2], o[3]};
    *(LAS v4u*)(h1w + 32 * H1P + 16) = v4u{o[4], o[5], o[6], o[7]};
  }
  const v4u bb2 = *(LAS v4u*)(L + L_B2 + (32 * w + 8 * q) * 2);
  STAMP(9);
  bar();
  const v2u ep = __builtin_amdgcn_raw_buffer_load_b64(wsr, tid == 192 ? WS_EPOCH : OOB, 0, SC1);
  {
    f32x4 acc[3][2];
#pragma unroll
    for (int nt = 0; nt < 2; ++nt) {
      const f32x4 bv = {bflo(bb2[2 * nt]), bfhi(bb2[2 * nt]), bflo(bb2[2 * nt + 1]), bfhi(bb2[2 * nt + 1])};
#pragma unroll
      for (int mt = 0; mt < 3; ++mt) acc[mt][nt] = bv;
    }
    LAS unsigned char* hb = L + L_H1 + m * H1P + q * 16;
    v4u b[3][3];
#pragma unroll
    for (int i = 0; i < 2; ++i)
#pragma unroll
      for (int mt = 0; mt < 3; ++mt) b[i][mt] = *(LAS v4u*)(hb + mt * 16 * H1P + i * 64);
#pragma unroll
    for (int ks = 0; ks < 16; ++ks) {
      if (ks + 2 < 16) {
#pragma unroll
        for (int mt = 0; mt < 3; ++mt) b[(ks + 2) % 3][mt] = *(LAS v4u*)(hb + mt * 16 * H1P + (ks + 2) * 64);
      }
      if (W2PRE + 16 + ks < 32) LOAD_W2(W2PRE + 16 + ks);
      SB();
#pragma unroll
      for (int nt = 0; nt < 2; ++nt)
#pragma unroll
        for (int mt = 0; mt < 3; ++mt) acc[mt][nt] = mfma(w2f[ks * 2 + nt], b[ks % 3][mt], acc[mt][nt]);
      SB();
    }
    const __amdgpu_buffer_rsrc_t outr = rsrc(out, N * 512);
#pragma unroll
    for (int mt = 0; mt < 3; ++mt) {
      const int r = 16 * mt + m;
      v4u o;
      o.x = pack_bf16(acc[mt][0][0], acc[mt][0][1]); o.y = pack_bf16(acc[mt][0][2], acc[mt][0][3]);
      o.z = pack_bf16(acc[mt][1][0], acc[mt][1][1]); o.w = pack_bf16(acc[mt][1][2], acc[mt][1][3]);
      __builtin_amdgcn_raw_buffer_store_b128(o, outr, r < TM ? (unsigned)(node0 + r) * 512u + 64 * w + 16 * q : OOB, 0, OUTAUX);
    }
  }
  if (!timedout && tid == 192 && (ep.x != (unsigned)tag || ep.y != (unsigned)(tag >> 32))) __hip_atomic_store((GAS u64*)(ws + WS_EPOCH), tag, RLX_AGENT);
  STAMP(10);
  STAMP_OUT((unsigned long long*)(ws + (64u << 20)), blockIdx.x * 8 + w);
  if (t == 0 && w == 0) while (__builtin_amdgcn_s_memrealtime() - t_begin < 4000) __builtin_amdgcn_s_sleep(1);
  asm volatile(".Lgin_end:");
}

extern "C" void kernel_launch(void* const* d_in, const int* in_sizes, int n_in, void* d_out, int out_size, void* d_ws, size_t ws_size,
                              hipStream_t stream) {
  const int N = in_sizes[0] / D, E = in_sizes[1];
  const int T = (N + TM - 1) / TM;
  gin_one<<<T, 512, 0, stream>>>((const unsigned short*)d_in[0], (const int*)d_in[1], (const int*)d_in[2], (const unsigned short*)d_in[3],
                                 (const unsigned short*)d_in[4], (const unsigned short*)d_in[5], (const unsigned short*)d_in[6],
                                 (const unsigned short*)d_in[7], (unsigned short*)d_out, (unsigned char*)d_ws, N, E, T);
}
```

```cpp
#include <hip/hip_runtime.h>
#include <hip/hip_bf16.h>
#include <stdint.h>
#include <type_traits>

typedef unsigned v4u __attribute__((ext_vector_type(4)));
typedef unsigned v2u __attribute__((ext_vector_type(2)));
typedef float f32x4 __attribute__((ext_vector_type(4)));
typedef float f32x2 __attribute__((ext_vector_type(2)));
typedef short bf16x8 __attribute__((ext_vector_type(8)));
typedef __bf16 bf16x2_t __attribute__((ext_vector_type(2)));
typedef unsigned long long u64;
#define GAS __attribute__((address_space(1)))
#define LAS __attribute__((address_space(3)))
#define DI __device__ __forceinline__
#define RLX_AGENT __ATOMIC_RELAXED, __HIP_MEMORY_SCOPE_AGENT
#define RLX_WG __ATOMIC_RELAXED, __HIP_MEMORY_SCOPE_WORKGROUP
#define SB() __builtin_amdgcn_sched_barrier(0)
#define PRIO(p) __builtin_amdgcn_s_setprio(p)
#define STAMP(i)
#define STAMP_DECL
#define STAMP_OUT(base, idx)

constexpr int D = 256, H = 512, TM = 40;
constexpr int EPB = 640;
constexpr int CAP = 1536;
constexpr int HP = 544, H1P = 1056;
constexpr unsigned OOB = 0x40000000u, EOOB = OOB >> 3;
#define RING 4
constexpr int R = RING;
constexpr int SC1 = 16;
constexpr unsigned SPIN_CAP = 4096;
#define FAIR 1
#define XWARM 0
#define BPRIO 0
#define BSCHED 0
#define XPRIO 0
#define ESLEEP 3
#define PSLEEP 32
#define NB1 8
#define NB2 16
#define NA 16
#define NCN 5
#define OUTAUX 16
#define W2PRE 8
constexpr unsigned WS_EPOCH = 0, WS_WFLAG = 4096, WS_W2T = 65536, WS_CELL = 1u << 20, WS_BYTES = WS_CELL + 256u * 16384u;
constexpr int L_H = 0, L_H1 = L_H + 48 * HP, L_FLAT = L_H1 + 48 * H1P, L_CNT = L_FLAT + (CAP + 128) * 4, L_MISC = L_CNT + 256,
              L_B1 = L_MISC + 32, L_B2 = L_B1 + 2048, L_END = L_B2 + 512;
constexpr int L_PSTG = L_H;
constexpr int L_PCELL = L_H1, L_PCNT = L_H1 + 320 * 64;

DI unsigned pack_bf16(float a, float b) { f32x2 v = {a, b}; bf16x2_t r = __builtin_convertvector(v, bf16x2_t); return __builtin_bit_cast(unsigned, r); }
DI float bflo(unsigned u) { return __builtin_bit_cast(float, u << 16); }
DI float bfhi(unsigned u) { return __builtin_bit_cast(float, u & 0xffff0000u); }
DI float asf(unsigned u) { return __builtin_bit_cast(float, u); }
DI unsigned asu(float u) { return __builtin_bit_cast(unsigned, u); }
DI f32x4 mfma(v4u a, v4u b, f32x4 c) {
  return __builtin_amdgcn_mfma_f32_16x16x32_bf16(__builtin_bit_cast(bf16x8, a), __builtin_bit_cast(bf16x8, b), c, 0, 0, 0);
}
typedef short s16x2 __attribute__((ext_vector_type(2)));
DI unsigned relu_pack(float a, float b) {
  const s16x2 v = __builtin_bit_cast(s16x2, pack_bf16(a, b));
  return __builtin_bit_cast(unsigned, __builtin_elementwise_max(v, s16x2{0, 0}));
}
DI void bar() { asm volatile("s_waitcnt lgkmcnt(0)" ::: "memory"); __builtin_amdgcn_s_barrier(); asm volatile("" ::: "memory"); }
template <int CTRL> DI unsigned dpp_add(unsigned x) { return x + (unsigned)__builtin_amdgcn_update_dpp(0, (int)x, CTRL, 0xF, 0xF, false); }
DI unsigned wave_incl_scan(unsigned x) {
  x = dpp_add<0x111>(x); x = dpp_add<0x112>(x); x = dpp_add<0x114>(x); x = dpp_add<0x118>(x);
  x = x + (unsigned)__builtin_amdgcn_update_dpp(0, (int)x, 0x142, 0xA, 0xF, false);
  x = x + (unsigned)__builtin_amdgcn_update_dpp(0, (int)x, 0x143, 0xC, 0xF, false);
  return x;
}
DI unsigned bsum(unsigned a, unsigned b) { return __builtin_amdgcn_sad_u8(a, 0u, __builtin_amdgcn_sad_u8(b, 0u, 0u)); }
DI __amdgpu_buffer_rsrc_t rsrc(const void* p, unsigned bytes) { return __builtin_amdgcn_make_buffer_rsrc((void*)p, 0, bytes, 0x00020000); }
DI unsigned tag_b(u64 tag) { return ((unsigned)tag + (unsigned)(tag >> 24) * 0x9E3779B1u) & 0x7FFFFu; }

DI void tile_w2_load(__amdgpu_buffer_rsrc_t w2r, int f, int lane, unsigned (&v)[8]) {
  const int ww = f >> 5, ks = (f >> 1) & 15, nt = f & 1, i = lane & 15, kg = lane >> 4;
  const int qo = (2 * ks + (kg >> 1)) & 3;
  const int row0 = 64 * (ks >> 1) + 32 * (qo & 1) + 4 * (qo >> 1) + 2 * (kg & 1);
  const unsigned o = f < 256 ? (row0 * D + 32 * ww + 8 * (i >> 2) + 4 * nt + (i & 3)) * 2 : OOB;
#pragma unroll
  for (int j = 0; j < 8; ++j) v[j] = __builtin_amdgcn_raw_buffer_load_b16(w2r, o + (j >> 2) * D * 2, (j & 3) * 8 * D * 2, 0);
}
DI void tile_w2_store(__amdgpu_buffer_rsrc_t wsr, int f, int lane, const unsigned (&v)[8]) {
  const v4u o = {v[0] | (v[1] << 16), v[2] | (v[3] << 16), v[4] | (v[5] << 16), v[6] | (v[7] << 16)};
  __builtin_amdgcn_raw_buffer_store_b128(o, wsr, f < 256 ? WS_W2T + f * 1024 + lane * 16 : OOB, 0, SC1);
}
constexpr int PB = 96;
template <int G> DI void load_w1(v4u srd, unsigned off) {
  unsigned so;
  constexpr int r = PB + 32 + 32 * G;
  asm volatile("s_mov_b32 %0, %3\n\t"
               "buffer_load_dwordx4 v[%c5:%c5+3], %1, %2, %0 offen\n\tbuffer_load_dwordx4 v[%c5+4:%c5+7], %1, %2, %0 offen offset:1024\n\t"
               "buffer_load_dwordx4 v[%c5+8:%c5+11], %1, %2, %0 offen offset:2048\n\tbuffer_load_dwordx4 v[%c5+12:%c5+15], %1, %2, %0 offen offset:3072\n\t"
               "s_mov_b32 %0, %4\n\t"
               "buffer_load_dwordx4 v[%c5+16:%c5+19], %1, %2, %0 offen\n\tbuffer_load_dwordx4 v[%c5+20:%c5+23], %1, %2, %0 offen offset:1024\n\t"
               "buffer_load_dwordx4 v[%c5+24:%c5+27], %1, %2, %0 offen offset:2048\n\tbuffer_load_dwordx4 v[%c5+28:%c5+31], %1, %2, %0 offen offset:3072"
               : "=&s"(so) : "v"(off), "s"(srd), "i"(G * 65536), "i"(G * 65536 + 4096), "i"(r) : "v255");
}
template <int G> DI void xpose(unsigned lo, unsigned hi) {
#pragma unroll
  for (int d = 0; d < 4; ++d)
#pragma unroll
    for (int p = 0; p < 2; ++p) {
      unsigned t0, t1;
      asm volatile("v_perm_b32 v[%c5], v[%c4+4], v[%c4], %2\n\tv_perm_b32 v[%c5+16], v[%c4+6], v[%c4+2], %2\n\t"
                   "v_perm_b32 v[%c5+4], v[%c4+4], v[%c4], %3\n\tv_perm_b32 v[%c5+20], v[%c4+6], v[%c4+2], %3\n\t"
                   "v_mov_b32 %0, v[%c5]\n\tv_mov_b32 %1, v[%c5+4]\n\t"
                   "v_mov_b32_dpp v[%c5], v[%c5+16] row_ror:8 row_mask:0xf bank_mask:0xc\n\t"
                   "v_mov_b32_dpp v[%c5+4], v[%c5+20] row_ror:8 row_mask:0xf bank_mask:0xc\n\t"
                   "v_mov_b32_dpp v[%c5+16], %0 row_ror:8 row_mask:0xf bank_mask:0x3\n\t"
                   "v_mov_b32_dpp v[%c5+20], %1 row_ror:8 row_mask:0xf bank_mask:0x3"
                   : "=&v"(t0), "=&v"(t1) : "s"(lo), "s"(hi), "i"(PB + 32 + 32 * G + 8 * d + p), "i"(PB + 32 * G + 8 * p + d));
    }
}
DI void xpose_all() { xpose<0>(0x05040100u, 0x07060302u); xpose<1>(0x05040100u, 0x07060302u); xpose<2>(0x05040100u, 0x07060302u); xpose<3>(0x05040100u, 0x07060302u); }
constexpr int w2reg(int i) { return i < 8 ? PB + 128 + 4 * i : PB + 4 * (i - 8); }
template <int I> DI void load_w2(v4u srd, unsigned off) {
  unsigned so;
  asm volatile("s_mov_b32 %0, %3\n\tbuffer_load_dwordx4 v[%c4:%c4+3], %1, %2, %0 offen offset:%c5 sc1"
               : "=&s"(so) : "v"(off), "s"(srd), "i"((I >> 2) * 4096), "i"(w2reg(I)), "i"((I & 3) * 1024));
}
template <int A> DI void mfma_p(f32x4& acc, v4u b) { asm volatile("v_mfma_f32_16x16x32_bf16 %0, v[%c2:%c2+3], %1, %0" : "+v"(acc) : "v"(b), "i"(A)); }
template <int A> DI void mfma_p0(f32x4& acc, v4u b, f32x4 c) {
  asm volatile("v_mfma_f32_16x16x32_bf16 %0, v[%c3:%c3+3], %1, %2" : "=&v"(acc) : "v"(b), "v"(c), "i"(A));
}
template <int N> DI void wait_vm() { asm volatile("s_waitcnt vmcnt(%c0)" :: "i"(N)); }
template <int I, int E, typename F> DI void static_for(F&& f) { if constexpr (I < E) { f(std::integral_constant<int, I>{}); static_for<I + 1, E>(f); } }

__global__ __launch_bounds__(512, 2) __attribute__((amdgpu_num_vgpr(PB / 2))) void gin_one(const unsigned short* __restrict__ x, const int* __restrict__ src, const int* __restrict__ dst,
                                                  const unsigned short* __restrict__ epsp, const unsigned short* __restrict__ w1,
                                                  const unsigned short* __restrict__ b1, const unsigned short* __restrict__ w2,
                                                  const unsigned short* __restrict__ b2, unsigned short* __restrict__ out, unsigned char* ws,
                                                  int N, int E, int T) {
  extern __shared__ __attribute__((aligned(16))) unsigned char Ls[];
  LAS unsigned char* L = (LAS unsigned char*)Ls;
  const int tid = threadIdx.x, lane = tid & 63, w = __builtin_amdgcn_readfirstlane(tid >> 6);
  const int t = blockIdx.x, node0 = t * TM;
  const unsigned bulk = w >> 2, chain = bulk ^ 1;
  unsigned cpf;
  {
    u64 pc; unsigned len;
    asm volatile("s_getpc_b64 %0\n.Lgin_pc:\n\ts_mov_b32 %1, .Lgin_end-.Lgin_pc" : "=s"(pc), "=s"(len));
    cpf = __builtin_amdgcn_raw_buffer_load_b32(rsrc((const void*)pc, len), (((t >> 3) & 31) * 8 + w) * 128 + lane * 32768, 0, 0);
  }
  const __amdgpu_buffer_rsrc_t wsr = rsrc(ws, WS_BYTES), xr = rsrc(x, N * 512), w1r = rsrc(w1, D * H * 2), w2r = rsrc(w2, D * H * 2);
  STAMP_DECL STAMP(0);
  const int q = lane >> 4, m = lane & 15;
  const v4u w1d = {(unsigned)(u64)w1, (unsigned)((u64)w1 >> 32) & 0xffffu, D * H * 2, 0x00020000u};
  const v4u wsd = {(unsigned)(u64)ws, (unsigned)((u64)ws >> 32) & 0xffffu, WS_BYTES, 0x00020000u};
  const unsigned w1off = (((lane >> 3) & 1) * 32 + q * 8) * 1024 + w * 128 + (lane & 7) * 16;
  LAS unsigned* pcnt = (LAS unsigned*)(L + L_PCNT);
  LAS unsigned* pcell = (LAS unsigned*)(L + L_PCELL);

  if (w != 4) __builtin_amdgcn_s_sleep(ESLEEP);
  const unsigned wu = __builtin_amdgcn_raw_buffer_load_b32(w1r, w == 0 ? ((t >> 3) & 31) * 8192 + lane * 128 : OOB, 0, 0);
  const unsigned wx = 0;
  const int tf = t + (w - 1) * T;
  if (w == 4) {
    PRIO(3);
    const __amdgpu_buffer_rsrc_t dr = rsrc(dst, E * 4), sr = rsrc(src, E * 4);
    v4u dv[3], sv[3];
#pragma unroll
    for (int i = 0; i < 3; ++i) {
      const unsigned o = (i == 2 && lane >= 32) ? OOB : (unsigned)(t * EPB + (lane + 64 * i) * 4) * 4u;
      dv[i] = __builtin_amdgcn_raw_buffer_load_b128(dr, o, 0, 0);
      sv[i] = __builtin_amdgcn_raw_buffer_load_b128(sr, o, 0, 0);
    }
    const u64 ep0 = __hip_atomic_load((GAS u64*)(ws + WS_EPOCH), RLX_AGENT);
    SB();
    *(LAS v4u*)(pcnt + lane * 4) = v4u{0, 0, 0, 0};
    pcnt[256 + lane] = 0;
    const unsigned dummy = 256u + lane;
    unsigned tt[12], rk[12];
#pragma unroll
    for (int i = 0; i < 12; ++i) {
      const int e = t * EPB + (lane + 64 * (i >> 2)) * 4;
      const unsigned lo = (e < E && !(i >= 8 && lane >= 32)) ? 0u : dummy;
      tt[i] = max(min(dv[i >> 2][i & 3] / (unsigned)TM, dummy), lo);
      rk[i] = __hip_atomic_fetch_add(pcnt + tt[i], 1u, RLX_WG);
    }
#pragma unroll
    for (int i = 0; i < 12; ++i)
      pcell[tt[i] * 16 + (rk[i] & 15u)] = (dv[i >> 2][i & 3] - tt[i] * TM) | (min(sv[i >> 2][i & 3], 16383u) << 6);
    *(LAS u64*)(L + L_MISC) = ep0;
  } else {
   if (w == 3) {
    const v4u v1 = __builtin_amdgcn_raw_buffer_load_b128(rsrc(b1, H * 2), lane * 16, 0, 0);
    const v4u v2 = __builtin_amdgcn_raw_buffer_load_b128(rsrc(b2, D * 2), lane * 16, 0, 0);
    const unsigned ev = epsp[0];
    *(LAS unsigned*)(L + L_CNT + lane * 4) = 0;
#pragma unroll
    for (int i = 0; i < 8; ++i)
      *(LAS float*)(L + L_B1 + ((lane >> 3) * 64 + (((lane >> 2) & 1) + 2 * (i >> 2)) * 16 + 4 * (i & 3) + (lane & 3)) * 4) = (i & 1) ? bfhi(v1[i >> 1]) : bflo(v1[i >> 1]);
    *(LAS v4u*)(L + (lane < 32 ? L_B2 + lane * 16 : L_FLAT + lane * 16)) = v2;
    *(LAS v2u*)(L + L_MISC + 8) = v2u{0u, ev};
   } else if (w == 1 || w == 2) {
    unsigned tv[8];
    tile_w2_load(w2r, tf, lane, tv);
    tile_w2_store(wsr, tf, lane, tv);
   }
  }
  if (chain) { load_w1<0>(w1d, w1off); load_w1<1>(w1d, w1off); }
  PRIO(3);
  bar();
  STAMP(1);
  const u64 tag = *(LAS u64*)(L + L_MISC) + 1;
  const unsigned tagA = (unsigned)tag & 0xFFFFFFu, tagB = tag_b(tag);
  {
    v4u e[2]; unsigned c[2];
#pragma unroll
    for (int i = 0; i < 2; ++i) { e[i] = *(LAS v4u*)(pcell + (w * 2 + i) * 256 + lane * 4); c[i] = pcnt[(w * 2 + i) * 16 + (lane >> 2)]; }
#pragma unroll
    for (int i = 0; i < 2; ++i) {
      const unsigned tc = (w * 2 + i) * 16 + (lane >> 2), cc = min(c[i], 31u);
      const v4u g = {(e[i].x & 0xFFFFFu) | (e[i].y << 20), ((e[i].y >> 12) & 0xFFu) | (tagA << 8), (e[i].z & 0xFFFFFu) | (e[i].w << 20),
                     ((e[i].w >> 12) & 0xFFu) | (cc << 8) | (tagB << 13)};
      __builtin_amdgcn_raw_buffer_store_b128(g, wsr, tc < (unsigned)T ? WS_CELL + tc * 16384 + t * 64 + (lane & 3) * 16 : OOB, 0, SC1);
    }
  }
  SB();
  PRIO(1);
  if (chain) { load_w1<2>(w1d, w1off); load_w1<3>(w1d, w1off); } else __builtin_amdgcn_s_sleep(PSLEEP);
  STAMP(2);
  bool any2 = false;
  unsigned ent[16], rk[16], spins = 0;
  LAS unsigned* flat = (LAS unsigned*)(L + L_FLAT);
  if (chain) {
    PRIO(2);
    v4u g[4];
    const unsigned coff = WS_CELL + t * 16384 + w * 4096 + lane * 16;
#pragma unroll
    for (int i = 0; i < 4; ++i) g[i] = __builtin_amdgcn_raw_buffer_load_b128(wsr, coff + i * 1024, 0, SC1);
    bool tmo = false;
    const int pl = T - 64 * w - (lane >> 2);
#define GR_OK(i) (16 * (i) >= pl || ((g[i].y >> 8) == tagA && (g[i].w >> 13) == tagB))
    if (!__all(GR_OK(0) && GR_OK(1) && GR_OK(2) && GR_OK(3))) {
      for (;;) {
#pragma unroll
        for (int i = 0; i < 4; ++i) {
          const bool ok = GR_OK(i);
          const v4u nv = __builtin_amdgcn_raw_buffer_load_b128(wsr, ok ? OOB : coff + i * 1024, 0, SC1);
#pragma unroll
          for (int k = 0; k < 4; ++k) g[i][k] = ok ? g[i][k] : nv[k];
        }
        if (__all(GR_OK(0) && GR_OK(1) && GR_OK(2) && GR_OK(3))) break;
        if (++spins > SPIN_CAP) { tmo = true; break; }
      }
      ++spins;
    }
    STAMP(3);
    LAS unsigned char* stg = L + L_PSTG + w * 5120;
#pragma unroll
    for (int i = 0; i < 4; ++i) *(LAS v4u*)(stg + (16 * i + (lane >> 2)) * 80 + (lane & 3) * 16) = g[i];
#pragma unroll
    for (int i = 0; i < 4; ++i) g[i] = *(LAS v4u*)(stg + lane * 80 + i * 16);
    const int nval = (64 * w + lane < T && !tmo) ? (int)((g[0].w >> 8) & 31u) : 0;
    any2 = __any(nval > 8);
    bool bad = nval > 16;
    const unsigned dummy = 40u + (unsigned)(lane % 24);
    auto rank = [&](int lo) __attribute__((always_inline)) {
#pragma unroll
      for (int i = lo; i < lo + 8; ++i) {
        const unsigned a = g[i >> 2][i & 2], b = g[i >> 2][(i & 2) + 1];
        ent[i] = i < nval ? ((i & 1) ? __builtin_amdgcn_alignbit(b, a, 20) : a) & 0xFFFFFu : dummy;
        rk[i] = __hip_atomic_fetch_add((LAS unsigned*)(L + L_CNT + ((ent[i] & 63u) << 2)), 1u << (8 * w), RLX_WG);
      }
#pragma unroll
      for (int i = lo; i < lo + 8; ++i) { rk[i] = (rk[i] >> (8 * w)) & 255u; bad |= rk[i] == 255u; }
    };
    rank(0);
    if (__builtin_expect(any2, 0)) rank(8);
    if ((__any(bad) || tmo) && lane == 0) __hip_atomic_fetch_or((LAS unsigned*)(L + L_MISC + 8), tmo ? 3u : 1u, RLX_WG);
  } else {
    PRIO(0);
    load_w1<0>(w1d, w1off); load_w1<1>(w1d, w1off); load_w1<2>(w1d, w1off);
    wait_vm<16>(); xpose<0>(0x05040100u, 0x07060302u);
    wait_vm<8>(); xpose<1>(0x05040100u, 0x07060302u);
  }
  bar();
  STAMP(4);
  bool slow, timedout;
  int s0, nit;
  u64 pk = 0;
  const int n0 = bulk ? (10 - NCN) * w + (8 * NCN - 40) : NCN * w;
  const int nn = bulk ? 10 - NCN : NCN;
  {
    const unsigned cw = *(LAS unsigned*)(L + L_CNT + lane * 4);
    const unsigned flagv = __builtin_amdgcn_readfirstlane(*(LAS unsigned*)(L + L_MISC + 8));
    const unsigned deg = __builtin_amdgcn_sad_u8(cw, 0u, 0u), below = __builtin_amdgcn_sad_u8(cw & ((1u << (8 * (w & 3))) - 1u), 0u, 0u);
    const unsigned l4 = (deg + 4u) & ~3u;
    const bool lastn = lane < 4 * NCN ? (lane % NCN) == NCN - 1 : ((lane - 4 * NCN) % (10 - NCN)) == 9 - NCN;
    const unsigned plen = lane < TM ? l4 + (lastn ? 4u * (R - 1) : 0u) : 0u;
    const unsigned incl = wave_incl_scan(plen), start = incl - plen;
    timedout = (flagv & 2u) != 0u;
    slow = flagv != 0u || __builtin_amdgcn_readlane(incl, 63) > (unsigned)CAP;
    const unsigned tail = __builtin_amdgcn_readlane(incl, n0 + nn - 1) - 4 * (R - 1);
    s0 = __builtin_amdgcn_readlane(start, n0);
    nit = ((tail - s0) / 4 + R - 1) / R;
#pragma unroll
    for (int j = 0; j < 5; ++j) pk |= (u64)(((unsigned)__builtin_amdgcn_readlane(l4, n0 + j) >> 2) & 0xFFFu) << (12 * j);
    if (slow) s0 = 0;
    if (!slow) {
      const unsigned st2 = (unsigned)__builtin_amdgcn_ds_bpermute((n0 + (lane >> 3)) << 2, (int)(start | (deg << 16)));
      if (chain) {
        const unsigned B4 = L_FLAT + 4u * (lane < TM ? start + 1u + below : CAP + 64u);
        auto place = [&](int lo) __attribute__((always_inline)) {
          unsigned b[8];
#pragma unroll
          for (int i = 0; i < 8; ++i) b[i] = (unsigned)__builtin_amdgcn_ds_bpermute((int)((ent[lo + i] & 63u) << 2), (int)B4);
#pragma unroll
          for (int i = 0; i < 8; ++i) *(LAS unsigned*)(L + b[i] + 4u * rk[lo + i]) = ent[lo + i];
        };
        place(0);
        if (__builtin_expect(any2, 0)) place(8);
      }
      const unsigned dg = st2 >> 16, k = lane & 7, pp = k == 7 ? 0u : dg + 1u + k;
      const bool mine = (lane >> 3) < nn && pp < ((dg + 4u) & ~3u);
      const unsigned at = mine ? (st2 & 0xFFFFu) + pp : (lane >= 48 && lane - 48 < 4 * (R - 1) ? tail + lane - 48 : CAP + lane);
      flat[at] = (mine && k == 7) ? (unsigned)(node0 + n0 + (lane >> 3)) << 6 : EOOB;
    }
  }
  if (bulk) { load_w1<3>(w1d, w1off); wait_vm<8>(); xpose<2>(0x05040100u, 0x07060302u); }
  bar();
  STAMP(5);
  PRIO(2);
  if (w == 1 || w == 2) {
    asm volatile("s_waitcnt vmcnt(0)" ::: "memory");
    if (lane == 0 && tf < 256) __hip_atomic_store((GAS unsigned*)(ws + WS_WFLAG) + tf, (unsigned)tag, RLX_AGENT);
  }
  const unsigned l16 = m * 16;
  LAS unsigned* fp = flat + s0 + q;
  v4u bl[R], br[R];
  unsigned en[R];
#pragma unroll
  for (int i = 0; i < R; ++i) en[i] = fp[4 * i];
  const float epsf = bflo(*(LAS unsigned*)(L + L_MISC + 12));
#pragma unroll
  for (int i = 0; i < R; ++i) {
    const unsigned o = ((en[i] << 3) & ~511u) | l16;
    bl[i] = __builtin_amdgcn_raw_buffer_load_b128(xr, o, 0, 0);
    br[i] = __builtin_amdgcn_raw_buffer_load_b128(xr, o + 256, 0, 0);
    en[i] = fp[4 * (R + i)];
  }
  asm volatile("" ::: "memory");
  SB();
  if (chain) { PRIO(XPRIO); xpose<0>(0x05040100u, 0x07060302u); xpose<1>(0x05040100u, 0x07060302u); xpose<2>(0x05040100u, 0x07060302u); }
  wait_vm<2 * R>();
  xpose<3>(0x05040100u, 0x07060302u);
  if (chain) PRIO(0); else PRIO(BPRIO);
  STAMP(6);
  unsigned wu2 = 0;
  v4u fl0;
  if (__builtin_expect(!slow, 1)) {
    const v4u sel = {m == 0 ? 0x3F80u : (m == 1 ? 0x3F800000u : 0u), m == 2 ? 0x3F80u : (m == 3 ? 0x3F800000u : 0u),
                     m == 4 ? 0x3F80u : (m == 5 ? 0x3F800000u : 0u), m == 6 ? 0x3F80u : (m == 7 ? 0x3F800000u : 0u)};
    const v4u self = {sel.x | (lane == 8 ? 0x3F80u : (lane == 9 ? 0x3F800000u : 0u)), sel.y | (lane == 10 ? 0x3F80u : (lane == 11 ? 0x3F800000u : 0u)),
                      sel.z | (lane == 12 ? 0x3F80u : (lane == 13 ? 0x3F800000u : 0u)), sel.w | (lane == 14 ? 0x3F80u : (lane == 15 ? 0x3F800000u : 0u))};
    v4u cur = self;
    fp += 8 * R;
    f32x4 a0 = {0.f, 0.f, 0.f, 0.f}, a1 = {0.f, 0.f, 0.f, 0.f};
    unsigned haddr = L_H + (lane >> 5) * 256 + m * 16 + (q & 1) * 8 + n0 * HP;
    int rem = (int)(pk & 0xFFF);
#define SLOT(i, REFILL) { \
        a0 = mfma(cur, bl[i], a0); \
        a1 = mfma(cur, br[i], a1); \
        cur = sel; \
        if (REFILL) { \
          const unsigned o = ((en[i] << 3) & ~511u) | l16; \
          bl[i] = __builtin_amdgcn_raw_buffer_load_b128(xr, o, 0, 0); \
          br[i] = __builtin_amdgcn_raw_buffer_load_b128(xr, o + 256, 0, 0); \
          en[i] = fp[4 * i]; \
        } \
        if (__builtin_expect(--rem == 0, 0)) { \
          float f[4]; \
          _Pragma("unroll") for (int r = 0; r < 4; ++r) { \
            auto s2 = __builtin_amdgcn_permlane32_swap(asu(a0[r]), asu(a1[r]), false, false); \
            const unsigned u0 = s2[0], u1 = s2[1]; \
            f[r] = fmaf(epsf, asf(u1), asf(u0)); \
          } \
          *(LAS v2u*)(L + haddr) = v2u{pack_bf16(f[0], f[1]), pack_bf16(f[2], f[3])}; \
          haddr += HP; \
          a0 = f32x4{0.f, 0.f, 0.f, 0.f}; a1 = a0; \
          cur = self; \
          pk >>= 12; \
          rem = (int)(pk & 0xFFF); \
        } }
    wu2 = __builtin_amdgcn_raw_buffer_load_b32(wsr, lane < 8 ? WS_W2T + ((t >> 3) & 31) * 8192 + w * 1024 + lane * 128 : OOB, 0, SC1);
    for (; nit > 1; --nit) {
#pragma unroll
      for (int i = 0; i < R; ++i) SLOT(i, true)
      fp += 4 * R;
    }
    fl0 = __builtin_amdgcn_raw_buffer_load_b128(wsr, WS_WFLAG + lane * 16, 0, SC1);
#pragma unroll
    for (int i = 0; i < R; ++i) SLOT(i, false)
  } else {
#pragma unroll 1
    for (int j = 0; j < nn; ++j) {
      const int node = node0 + n0 + j;
      const v2u s2 = __builtin_amdgcn_raw_buffer_load_b64(xr, (unsigned)node * 512u + lane * 8, 0, 0);
      float a0 = bflo(s2.x), a1 = bfhi(s2.x), a2 = bflo(s2.y), a3 = bfhi(s2.y);
      if (node < N)
        for (int eb = 0; eb < E; eb += 64) {
          const int e = eb + lane;
          const int dv2 = (int)__builtin_amdgcn_raw_buffer_load_b32(rsrc(dst, E * 4), e * 4, 0, 0);
          const int sv2 = (int)__builtin_amdgcn_raw_buffer_load_b32(rsrc(src, E * 4), e * 4, 0, 0);
          u64 mkk = __ballot(dv2 == node && e < E);
          while (mkk) {
            const int b = __builtin_ctzll(mkk); mkk &= mkk - 1;
            const unsigned sidx = (unsigned)__builtin_amdgcn_readlane(sv2, b);
            const v2u rv = __builtin_amdgcn_raw_buffer_load_b64(xr, sidx < (unsigned)N ? sidx * 512u + lane * 8 : OOB, 0, 0);
            a0 += bflo(rv.x); a1 += bfhi(rv.x); a2 += bflo(rv.y); a3 += bfhi(rv.y);
          }
        }
      *(LAS v2u*)(L + L_H + (n0 + j) * HP + lane * 8) =
          v2u{pack_bf16(fmaf(epsf, bflo(s2.x), a0), fmaf(epsf, bfhi(s2.x), a1)), pack_bf16(fmaf(epsf, bflo(s2.y), a2), fmaf(epsf, bfhi(s2.y), a3))};
    }
    fl0 = __builtin_amdgcn_raw_buffer_load_b128(wsr, WS_WFLAG + lane * 16, 0, SC1);
  }
  STAMP(7);
  PRIO(0);
  {
    bool okw = false;
    const unsigned tl = (unsigned)tag;
    for (unsigned sp = 0; sp <= SPIN_CAP; ++sp) {
      if (__all(fl0.x == tl && fl0.y == tl && fl0.z == tl && fl0.w == tl)) { okw = true; break; }
      fl0 = __builtin_amdgcn_raw_buffer_load_b128(wsr, WS_WFLAG + lane * 16, 0, SC1);
    }
    if (__builtin_expect(!okw, 0)) {
#pragma unroll 1
      for (int i = 0; i < 32; ++i) { unsigned tv[8]; tile_w2_load(w2r, w * 32 + i, lane, tv); tile_w2_store(wsr, w * 32 + i, lane, tv); }
      asm volatile("s_waitcnt vmcnt(0)" ::: "memory");
    }
    __builtin_amdgcn_fence(__ATOMIC_ACQUIRE, "wavefront");
  }
  if (!timedout && tid == 192 && t == 0) __hip_atomic_store((GAS u64*)(ws + WS_EPOCH), tag, RLX_AGENT);
  asm volatile("" :: "v"(wu2), "v"(wu), "v"(wx), "v"(cpf));
  const unsigned w2off = WS_W2T + w * 32768 + lane * 16;
  static_for<0, W2PRE>([&](auto I) { load_w2<I>(wsd, w2off); });
  f32x4 bias1[4];
#pragma unroll
  for (int c = 0; c < 4; ++c) bias1[c] = *(LAS f32x4*)(L + L_B1 + (64 * w + 16 * q + 4 * c) * 4);
  SB();
  bar();
  STAMP(8);
  f32x4 bias2[2];
  {
    LAS unsigned char* hb = L + L_H + m * HP + q * 16;
    LAS unsigned char* h1w = L + L_H1 + m * H1P + (64 * w + 16 * q) * 2;
    constexpr int P1 = 5;
    v4u b[P1];
    f32x4 acc[3][4];
    v4u bb2;
    auto epi = [&](int mt, int half) __attribute__((always_inline)) {
      unsigned o[4];
#pragma unroll
      for (int k = 0; k < 4; ++k) o[k] = relu_pack(acc[mt][2 * half + (k >> 1)][2 * (k & 1)], acc[mt][2 * half + (k >> 1)][2 * (k & 1) + 1]);
      *(LAS v4u*)(h1w + mt * 16 * H1P + 16 * half) = v4u{o[0], o[1], o[2], o[3]};
    };
#define U1_KS(u) ((u) < 12 ? (u) / 3 : 4 + ((u) - 12) % 4)
#define U1_MT(u) ((u) < 12 ? (u) % 3 : ((u) - 12) / 4)
#pragma unroll
    for (int u = 0; u < P1; ++u) b[u] = *(LAS v4u*)(hb + U1_MT(u) * 16 * HP + U1_KS(u) * 64);
    static_for<0, 24>([&](auto U) {
      constexpr int u = U, ks = U1_KS(u), mt = U1_MT(u);
      if constexpr (u % 6 == 0) PRIO(3 - u / 6);
      static_for<0, 4>([&](auto C) {
        if constexpr (ks == 0) mfma_p0<PB + 4 * C>(acc[mt][C], b[u % P1], bias1[C]);
        else mfma_p<PB + 16 * ks + 4 * C>(acc[mt][C], b[u % P1]);
      });
      if constexpr (u + P1 < 24) b[u % P1] = *(LAS v4u*)(hb + U1_MT(u + P1) * 16 * HP + U1_KS(u + P1) * 64);
      if constexpr (u >= 2 && u < 18) load_w2<6 + u>(wsd, w2off);
      if constexpr (u == 20) bb2 = *(LAS v4u*)(L + L_B2 + (32 * w + 8 * q) * 2);
      if constexpr (u == 17 || u == 21) epi(mt - 1, 0);
      if constexpr (u == 18 || u == 22) epi(mt - 1, 1);
      SB();
    });
    asm volatile("s_nop 7" : "+v"(acc[2][0]), "+v"(acc[2][1]), "+v"(acc[2][2]), "+v"(acc[2][3]));
    epi(2, 0); epi(2, 1);
#pragma unroll
    for (int nt = 0; nt < 2; ++nt) {
      bias2[nt] = f32x4{bflo(bb2[2 * nt]), bfhi(bb2[2 * nt]), bflo(bb2[2 * nt + 1]), bfhi(bb2[2 * nt + 1])};
      asm volatile("" : "+v"(bias2[nt]));
    }
  }
  SB();
  STAMP(9);
  bar();
  v2u ep;
  {
    f32x4 acc[3][2];
    LAS unsigned char* hb = L + L_H1 + m * H1P + q * 16;
    const __amdgpu_buffer_rsrc_t outr = rsrc(out, N * 512);
    auto put = [&](int mt) __attribute__((always_inline)) {
      const int r = 16 * mt + m;
      v4u o;
      o.x = pack_bf16(acc[mt][0][0], acc[mt][0][1]); o.y = pack_bf16(acc[mt][0][2], acc[mt][0][3]);
      o.z = pack_bf16(acc[mt][1][0], acc[mt][1][1]); o.w = pack_bf16(acc[mt][1][2], acc[mt][1][3]);
      __builtin_amdgcn_raw_buffer_store_b128(o, outr, r < TM ? (unsigned)(node0 + r) * 512u + 64 * w + 16 * q : OOB, 0, OUTAUX);
    };
    constexpr int P2 = 8;
    v4u b[P2];
#define U2_KS(u) ((u) < 24 ? (u) / 3 : 8 + ((u) - 24) % 8)
#define U2_MT(u) ((u) < 24 ? (u) % 3 : ((u) - 24) / 8)
#pragma unroll
    for (int u = 0; u < P2; ++u) b[u] = *(LAS v4u*)(hb + U2_MT(u) * 16 * H1P + U2_KS(u) * 64);
    static_for<0, 48>([&](auto U) {
      constexpr int u = U, ks = U2_KS(u), mt = U2_MT(u);
      if constexpr (u % 12 == 0) PRIO(3 - u / 12);
      if constexpr (u < 16 && (u & 1) == 0) load_w2<24 + u / 2>(wsd, w2off);
      constexpr int issued = u < 16 ? 25 + u / 2 : 32;
      if constexpr (mt == 0) wait_vm<issued - (2 * ks + 2)>();
      if constexpr (ks == 0) { mfma_p0<w2reg(0)>(acc[mt][0], b[u % P2], bias2[0]); mfma_p0<w2reg(1)>(acc[mt][1], b[u % P2], bias2[1]); }
      else { mfma_p<w2reg(2 * ks)>(acc[mt][0], b[u % P2]); mfma_p<w2reg(2 * ks + 1)>(acc[mt][1], b[u % P2]); }
      if constexpr (u + P2 < 48) b[u % P2] = *(LAS v4u*)(hb + U2_MT(u + P2) * 16 * H1P + U2_KS(u + P2) * 64);
      if constexpr (u == 32) ep = __builtin_amdgcn_raw_buffer_load_b64(wsr, tid == 192 ? WS_EPOCH : OOB, 0, SC1);
      if constexpr (u == 36 || u == 44) put(mt - 1);
      SB();
    });
    asm volatile("s_nop 7" : "+v"(acc[2][0]), "+v"(acc[2][1]));
    put(2);
  }
  if (!timedout && tid == 192 && (ep.x != (unsigned)tag || ep.y != (unsigned)(tag >> 32))) __hip_atomic_store((GAS u64*)(ws + WS_EPOCH), tag, RLX_AGENT);
  STAMP(10);
  STAMP_OUT((unsigned long long*)(ws + (64u << 20)), blockIdx.x * 8 + w);
  asm volatile(".Lgin_end:");
}

extern "C" void kernel_launch(void* const* d_in, const int* in_sizes, int n_in, void* d_out, int out_size, void* d_ws, size_t ws_size,
                              hipStream_t stream) {
  const int N = in_sizes[0] / D, E = in_sizes[1];
  const int T = (N + TM - 1) / TM;
  (void)hipFuncSetAttribute((const void*)gin_one, hipFuncAttributeMaxDynamicSharedMemorySize, L_END);
  gin_one<<<T, 512, L_END, stream>>>((const unsigned short*)d_in[0], (const int*)d_in[1], (const int*)d_in[2], (const unsigned short*)d_in[3],
                                 (const unsigned short*)d_in[4], (const unsigned short*)d_in[5], (const unsigned short*)d_in[6],
                                 (const unsigned short*)d_in[7], (unsigned short*)d_out, (unsigned char*)d_ws, N, E, T);
}
```

```cpp
#define PH 1
#include <hip/hip_runtime.h>
#include <hip/hip_bf16.h>
#include <stdint.h>
#include <type_traits>

typedef unsigned v4u __attribute__((ext_vector_type(4)));
typedef unsigned v2u __attribute__((ext_vector_type(2)));
typedef float f32x4 __attribute__((ext_vector_type(4)));
typedef float f32x2 __attribute__((ext_vector_type(2)));
typedef short bf16x8 __attribute__((ext_vector_type(8)));
typedef __bf16 bf16x2_t __attribute__((ext_vector_type(2)));
typedef unsigned long long u64;
#define GAS __attribute__((address_space(1)))
#define LAS __attribute__((address_space(3)))
#define DI __device__ __forceinline__
#define RLX_AGENT __ATOMIC_RELAXED, __HIP_MEMORY_SCOPE_AGENT
#define RLX_WG __ATOMIC_RELAXED, __HIP_MEMORY_SCOPE_WORKGROUP
#define SB() __builtin_amdgcn_sched_barrier(0)
#define PRIO(p) __builtin_amdgcn_s_setprio(p)
#define STAMP(i) do { if ((i) == PH) t_ph = __builtin_amdgcn_s_memrealtime(); } while (0)
#define STAMP_DECL
#define STAMP_OUT(base, idx)

constexpr int D = 256, H = 512, TM = 40;
constexpr int EPB = 640;
constexpr int CAP = 1536;
constexpr int HP = 544, H1P = 1056;
constexpr unsigned OOB = 0x40000000u, EOOB = OOB >> 3;
#define RING 4
constexpr int R = RING;
constexpr int SC1 = 16;
constexpr unsigned SPIN_CAP = 4096;
#define FAIR 1
#define XWARM 0
#define BPRIO 0
#define BSCHED 0
#define XPRIO 0
#define ESLEEP 3
#define PSLEEP 24
#define NB1 8
#define NB2 16
#define NA 16
#define NCN 5
#define OUTAUX 16
#define W2PRE 8
constexpr unsigned WS_EPOCH = 0, WS_WFLAG = 4096, WS_W2T = 65536, WS_CELL = 1u << 20, WS_BYTES = WS_CELL + 256u * 16384u;
constexpr int L_H = 0, L_H1 = L_H + 48 * HP, L_FLAT = L_H1 + 48 * H1P, L_CNT = L_FLAT + (CAP + 128) * 4, L_MISC = L_CNT + 256,
              L_B1 = L_MISC + 32, L_B2 = L_B1 + 2048, L_END = L_B2 + 512;
constexpr int L_PSTG = L_H;
constexpr int L_PCELL = L_H1, L_PCNT = L_H1 + 320 * 64;

DI unsigned pack_bf16(float a, float b) { f32x2 v = {a, b}; bf16x2_t r = __builtin_convertvector(v, bf16x2_t); return __builtin_bit_cast(unsigned, r); }
DI float bflo(unsigned u) { return __builtin_bit_cast(float, u << 16); }
DI float bfhi(unsigned u) { return __builtin_bit_cast(float, u & 0xffff0000u); }
DI float asf(unsigned u) { return __builtin_bit_cast(float, u); }
DI unsigned asu(float u) { return __builtin_bit_cast(unsigned, u); }
DI f32x4 mfma(v4u a, v4u b, f32x4 c) {
  return __builtin_amdgcn_mfma_f32_16x16x32_bf16(__builtin_bit_cast(bf16x8, a), __builtin_bit_cast(bf16x8, b), c, 0, 0, 0);
}
typedef short s16x2 __attribute__((ext_vector_type(2)));
DI unsigned relu_pack(float a, float b) {
  const s16x2 v = __builtin_bit_cast(s16x2, pack_bf16(a, b));
  return __builtin_bit_cast(unsigned, __builtin_elementwise_max(v, s16x2{0, 0}));
}
DI void bar() { asm volatile("s_waitcnt lgkmcnt(0)" ::: "memory"); __builtin_amdgcn_s_barrier(); asm volatile("" ::: "memory"); }
template <int CTRL> DI unsigned dpp_add(unsigned x) { return x + (unsigned)__builtin_amdgcn_update_dpp(0, (int)x, CTRL, 0xF, 0xF, false); }
DI unsigned wave_incl_scan(unsigned x) {
  x = dpp_add<0x111>(x); x = dpp_add<0x112>(x); x = dpp_add<0x114>(x); x = dpp_add<0x118>(x);
  x = x + (unsigned)__builtin_amdgcn_update_dpp(0, (int)x, 0x142, 0xA, 0xF, false);
  x = x + (unsigned)__builtin_amdgcn_update_dpp(0, (int)x, 0x143, 0xC, 0xF, false);
  return x;
}
DI unsigned bsum(unsigned a, unsigned b) { return __builtin_amdgcn_sad_u8(a, 0u, __builtin_amdgcn_sad_u8(b, 0u, 0u)); }
DI __amdgpu_buffer_rsrc_t rsrc(const void* p, unsigned bytes) { return __builtin_amdgcn_make_buffer_rsrc((void*)p, 0, bytes, 0x00020000); }
DI unsigned tag_b(u64 tag) { return ((unsigned)tag + (unsigned)(tag >> 24) * 0x9E3779B1u) & 0x7FFFFu; }

DI void tile_w2_load(__amdgpu_buffer_rsrc_t w2r, int f, int lane, unsigned (&v)[8]) {
  const int ww = f >> 5, ks = (f >> 1) & 15, nt = f & 1, i = lane & 15, kg = lane >> 4;
  const int qo = (2 * ks + (kg >> 1)) & 3;
  const int row0 = 64 * (ks >> 1) + 32 * (qo & 1) + 4 * (qo >> 1) + 2 * (kg & 1);
  const unsigned o = f < 256 ? (row0 * D + 32 * ww + 8 * (i >> 2) + 4 * nt + (i & 3)) * 2 : OOB;
#pragma unroll
  for (int j = 0; j < 8; ++j) v[j] = __builtin_amdgcn_raw_buffer_load_b16(w2r, o + (j >> 2) * D * 2, (j & 3) * 8 * D * 2, 0);
}
DI void tile_w2_store(__amdgpu_buffer_rsrc_t wsr, int f, int lane, const unsigned (&v)[8]) {
  const v4u o = {v[0] | (v[1] << 16), v[2] | (v[3] << 16), v[4] | (v[5] << 16), v[6] | (v[7] << 16)};
  __builtin_amdgcn_raw_buffer_store_b128(o, wsr, f < 256 ? WS_W2T + f * 1024 + lane * 16 : OOB, 0, SC1);
}
constexpr int PB = 96;
template <int G> DI void load_w1(v4u srd, unsigned off) {
  unsigned so;
  constexpr int r = PB + 32 + 32 * G;
  asm volatile("s_mov_b32 %0, %3\n\t"
               "buffer_load_dwordx4 v[%c5:%c5+3], %1, %2, %0 offen\n\tbuffer_load_dwordx4 v[%c5+4:%c5+7], %1, %2, %0 offen offset:1024\n\t"
               "buffer_load_dwordx4 v[%c5+8:%c5+11], %1, %2, %0 offen offset:2048\n\tbuffer_load_dwordx4 v[%c5+12:%c5+15], %1, %2, %0 offen offset:3072\n\t"
               "s_mov_b32 %0, %4\n\t"
               "buffer_load_dwordx4 v[%c5+16:%c5+19], %1, %2, %0 offen\n\tbuffer_load_dwordx4 v[%c5+20:%c5+23], %1, %2, %0 offen offset:1024\n\t"
               "buffer_load_dwordx4 v[%c5+24:%c5+27], %1, %2, %0 offen offset:2048\n\tbuffer_load_dwordx4 v[%c5+28:%c5+31], %1, %2, %0 offen offset:3072"
               : "=&s"(so) : "v"(off), "s"(srd), "i"(G * 65536), "i"(G * 65536 + 4096), "i"(r) : "v255");
}
template <int G> DI void xpose(unsigned lo, unsigned hi) {
#pragma unroll
  for (int d = 0; d < 4; ++d)
#pragma unroll
    for (int p = 0; p < 2; ++p) {
      unsigned t0, t1;
      asm volatile("v_perm_b32 v[%c5], v[%c4+4], v[%c4], %2\n\tv_perm_b32 v[%c5+16], v[%c4+6], v[%c4+2], %2\n\t"
                   "v_perm_b32 v[%c5+4], v[%c4+4], v[%c4], %3\n\tv_perm_b32 v[%c5+20], v[%c4+6], v[%c4+2], %3\n\t"
                   "v_mov_b32 %0, v[%c5]\n\tv_mov_b32 %1, v[%c5+4]\n\t"
                   "v_mov_b32_dpp v[%c5], v[%c5+16] row_ror:8 row_mask:0xf bank_mask:0xc\n\t"
                   "v_mov_b32_dpp v[%c5+4], v[%c5+20] row_ror:8 row_mask:0xf bank_mask:0xc\n\t"
                   "v_mov_b32_dpp v[%c5+16], %0 row_ror:8 row_mask:0xf bank_mask:0x3\n\t"
                   "v_mov_b32_dpp v[%c5+20], %1 row_ror:8 row_mask:0xf bank_mask:0x3"
                   : "=&v"(t0), "=&v"(t1) : "s"(lo), "s"(hi), "i"(PB + 32 + 32 * G + 8 * d + p), "i"(PB + 32 * G + 8 * p + d));
    }
}
DI void xpose_all() { xpose<0>(0x05040100u, 0x07060302u); xpose<1>(0x05040100u, 0x07060302u); xpose<2>(0x05040100u, 0x07060302u); xpose<3>(0x05040100u, 0x07060302u); }
constexpr int w2reg(int i) { return i < 8 ? PB + 128 + 4 * i : PB + 4 * (i - 8); }
template <int I> DI void load_w2(v4u srd, unsigned off) {
  unsigned so;
  asm volatile("s_mov_b32 %0, %3\n\tbuffer_load_dwordx4 v[%c4:%c4+3], %1, %2, %0 offen offset:%c5 sc1"
               : "=&s"(so) : "v"(off), "s"(srd), "i"((I >> 2) * 4096), "i"(w2reg(I)), "i"((I & 3) * 1024));
}
template <int A> DI void mfma_p(f32x4& acc, v4u b) { asm volatile("v_mfma_f32_16x16x32_bf16 %0, v[%c2:%c2+3], %1, %0" : "+v"(acc) : "v"(b), "i"(A)); }
template <int A> DI void mfma_p0(f32x4& acc, v4u b, f32x4 c) {
  asm volatile("v_mfma_f32_16x16x32_bf16 %0, v[%c3:%c3+3], %1, %2" : "=&v"(acc) : "v"(b), "v"(c), "i"(A));
}
template <int N> DI void wait_vm() { asm volatile("s_waitcnt vmcnt(%c0)" :: "i"(N)); }
template <int I, int E, typename F> DI void static_for(F&& f) { if constexpr (I < E) { f(std::integral_constant<int, I>{}); static_for<I + 1, E>(f); } }

__global__ __launch_bounds__(512, 2) __attribute__((amdgpu_num_vgpr(PB / 2))) void gin_one(const unsigned short* __restrict__ x, const int* __restrict__ src, const int* __restrict__ dst,
                                                  const unsigned short* __restrict__ epsp, const unsigned short* __restrict__ w1,
                                                  const unsigned short* __restrict__ b1, const unsigned short* __restrict__ w2,
                                                  const unsigned short* __restrict__ b2, unsigned short* __restrict__ out, unsigned char* ws,
                                                  int N, int E, int T) {
  extern __shared__ __attribute__((aligned(16))) unsigned char Ls[];
  LAS unsigned char* L = (LAS unsigned char*)Ls;
  const int tid = threadIdx.x, lane = tid & 63, w = __builtin_amdgcn_readfirstlane(tid >> 6);
  const int t = blockIdx.x, node0 = t * TM;
  const u64 t_begin = __builtin_amdgcn_s_memrealtime();
  u64 t_ph = t_begin;
  const unsigned bulk = w >> 2, chain = bulk ^ 1;
  unsigned cpf;
  {
    u64 pc; unsigned len;
    asm volatile("s_getpc_b64 %0\n.Lgin_pc:\n\ts_mov_b32 %1, .Lgin_end-.Lgin_pc" : "=s"(pc), "=s"(len));
    cpf = __builtin_amdgcn_raw_buffer_load_b32(rsrc((const void*)pc, len), (((t >> 3) & 31) * 8 + w) * 128 + lane * 32768, 0, 0);
  }
  const __amdgpu_buffer_rsrc_t wsr = rsrc(ws, WS_BYTES), xr = rsrc(x, N * 512), w1r = rsrc(w1, D * H * 2), w2r = rsrc(w2, D * H * 2);

  const int q = lane >> 4, m = lane & 15;
  const v4u w1d = {(unsigned)(u64)w1, (unsigned)((u64)w1 >> 32) & 0xffffu, D * H * 2, 0x00020000u};
  const v4u wsd = {(unsigned)(u64)ws, (unsigned)((u64)ws >> 32) & 0xffffu, WS_BYTES, 0x00020000u};
  const unsigned w1off = (((lane >> 3) & 1) * 32 + q * 8) * 1024 + w * 128 + (lane & 7) * 16;
  LAS unsigned* pcnt = (LAS unsigned*)(L + L_PCNT);
  LAS unsigned* pcell = (LAS unsigned*)(L + L_PCELL);

  if (w != 4) __builtin_amdgcn_s_sleep(ESLEEP);
  const unsigned wu = __builtin_amdgcn_raw_buffer_load_b32(w1r, w == 0 ? ((t >> 3) & 31) * 8192 + lane * 128 : OOB, 0, 0);
  const unsigned wx = 0;
  const int tf = t + (w - 1) * T;
  if (w == 4) {
    PRIO(3);
    const __amdgpu_buffer_rsrc_t dr = rsrc(dst, E * 4), sr = rsrc(src, E * 4);
    v4u dv[3], sv[3];
#pragma unroll
    for (int i = 0; i < 3; ++i) {
      const unsigned o = (i == 2 && lane >= 32) ? OOB : (unsigned)(t * EPB + (lane + 64 * i) * 4) * 4u;
      dv[i] = __builtin_amdgcn_raw_buffer_load_b128(dr, o, 0, 0);
      sv[i] = __builtin_amdgcn_raw_buffer_load_b128(sr, o, 0, 0);
    }
    const u64 ep0 = __hip_atomic_load((GAS u64*)(ws + WS_EPOCH), RLX_AGENT);
    SB();
    *(LAS v4u*)(pcnt + lane * 4) = v4u{0, 0, 0, 0};
    pcnt[256 + lane] = 0;
    const unsigned dummy = 256u + lane;
    unsigned tt[12], rk[12];
#pragma unroll
    for (int i = 0; i < 12; ++i) {
      const int e = t * EPB + (lane + 64 * (i >> 2)) * 4;
      const unsigned lo = (e < E && !(i >= 8 && lane >= 32)) ? 0u : dummy;
      tt[i] = max(min(dv[i >> 2][i & 3] / (unsigned)TM, dummy), lo);
      rk[i] = __hip_atomic_fetch_add(pcnt + tt[i], 1u, RLX_WG);
    }
#pragma unroll
    for (int i = 0; i < 12; ++i)
      pcell[tt[i] * 16 + (rk[i] & 15u)] = (dv[i >> 2][i & 3] - tt[i] * TM) | (min(sv[i >> 2][i & 3], 16383u) << 6);
    *(LAS u64*)(L + L_MISC) = ep0;
  } else {
   if (w == 3) {
    const v4u v1 = __builtin_amdgcn_raw_buffer_load_b128(rsrc(b1, H * 2), lane * 16, 0, 0);
    const v4u v2 = __builtin_amdgcn_raw_buffer_load_b128(rsrc(b2, D * 2), lane * 16, 0, 0);
    const unsigned ev = epsp[0];
    *(LAS unsigned*)(L + L_CNT + lane * 4) = 0;
#pragma unroll
    for (int i = 0; i < 8; ++i)
      *(LAS float*)(L + L_B1 + ((lane >> 3) * 64 + (((lane >> 2) & 1) + 2 * (i >> 2)) * 16 + 4 * (i & 3) + (lane & 3)) * 4) = (i & 1) ? bfhi(v1[i >> 1]) : bflo(v1[i >> 1]);
    *(LAS v4u*)(L + (lane < 32 ? L_B2 + lane * 16 : L_FLAT + lane * 16)) = v2;
    *(LAS v2u*)(L + L_MISC + 8) = v2u{0u, ev};
   } else if (w == 1 || w == 2) {
    unsigned tv[8];
    tile_w2_load(w2r, tf, lane, tv);
    tile_w2_store(wsr, tf, lane, tv);
   }
  }
  if (chain) { load_w1<0>(w1d, w1off); load_w1<1>(w1d, w1off); }
  PRIO(3);
  bar();
  STAMP(1);
  const u64 tag = *(LAS u64*)(L + L_MISC) + 1;
  const unsigned tagA = (unsigned)tag & 0xFFFFFFu, tagB = tag_b(tag);
  {
    v4u e[2]; unsigned c[2];
#pragma unroll
    for (int i = 0; i < 2; ++i) { e[i] = *(LAS v4u*)(pcell + (w * 2 + i) * 256 + lane * 4); c[i] = pcnt[(w * 2 + i) * 16 + (lane >> 2)]; }
#pragma unroll
    for (int i = 0; i < 2; ++i) {
      const unsigned tc = (w * 2 + i) * 16 + (lane >> 2), cc = min(c[i], 31u);
      const v4u g = {(e[i].x & 0xFFFFFu) | (e[i].y << 20), ((e[i].y >> 12) & 0xFFu) | (tagA << 8), (e[i].z & 0xFFFFFu) | (e[i].w << 20),
                     ((e[i].w >> 12) & 0xFFu) | (cc << 8) | (tagB << 13)};
      __builtin_amdgcn_raw_buffer_store_b128(g, wsr, tc < (unsigned)T ? WS_CELL + tc * 16384 + t * 64 + (lane & 3) * 16 : OOB, 0, SC1);
    }
  }
  SB();
  PRIO(1);
  if (chain) { load_w1<2>(w1d, w1off); load_w1<3>(w1d, w1off); } else __builtin_amdgcn_s_sleep(PSLEEP);
  STAMP(2);
  bool any2 = false;
  unsigned ent[16], rk[16], spins = 0;
  LAS unsigned* flat = (LAS unsigned*)(L + L_FLAT);
  if (chain) {
    PRIO(2);
    v4u g[4];
    const unsigned coff = WS_CELL + t * 16384 + w * 4096 + lane * 16;
#pragma unroll
    for (int i = 0; i < 4; ++i) g[i] = __builtin_amdgcn_raw_buffer_load_b128(wsr, coff + i * 1024, 0, SC1);
    bool tmo = false;
    const int pl = T - 64 * w - (lane >> 2);
#define GR_OK(i) (16 * (i) >= pl || ((g[i].y >> 8) == tagA && (g[i].w >> 13) == tagB))
    if (!__all(GR_OK(0) && GR_OK(1) && GR_OK(2) && GR_OK(3))) {
      for (;;) {
#pragma unroll
        for (int i = 0; i < 4; ++i) {
          const bool ok = GR_OK(i);
          const v4u nv = __builtin_amdgcn_raw_buffer_load_b128(wsr, ok ? OOB : coff + i * 1024, 0, SC1);
#pragma unroll
          for (int k = 0; k < 4; ++k) g[i][k] = ok ? g[i][k] : nv[k];
        }
        if (__all(GR_OK(0) && GR_OK(1) && GR_OK(2) && GR_OK(3))) break;
        if (++spins > SPIN_CAP) { tmo = true; break; }
      }
      ++spins;
    }
    STAMP(3);
    LAS unsigned char* stg = L + L_PSTG + w * 5120;
#pragma unroll
    for (int i = 0; i < 4; ++i) *(LAS v4u*)(stg + (16 * i + (lane >> 2)) * 80 + (lane & 3) * 16) = g[i];
#pragma unroll
    for (int i = 0; i < 4; ++i) g[i] = *(LAS v4u*)(stg + lane * 80 + i * 16);
    const int nval = (64 * w + lane < T && !tmo) ? (int)((g[0].w >> 8) & 31u) : 0;
    any2 = __any(nval > 8);
    bool bad = nval > 16;
    const unsigned dummy = 40u + (unsigned)(lane % 24);
    auto rank = [&](int lo) __attribute__((always_inline)) {
#pragma unroll
      for (int i = lo; i < lo + 8; ++i) {
        const unsigned a = g[i >> 2][i & 2], b = g[i >> 2][(i & 2) + 1];
        ent[i] = i < nval ? ((i & 1) ? __builtin_amdgcn_alignbit(b, a, 20) : a) & 0xFFFFFu : dummy;
        rk[i] = __hip_atomic_fetch_add((LAS unsigned*)(L + L_CNT + ((ent[i] & 63u) << 2)), 1u << (8 * w), RLX_WG);
      }
#pragma unroll
      for (int i = lo; i < lo + 8; ++i) { rk[i] = (rk[i] >> (8 * w)) & 255u; bad |= rk[i] == 255u; }
    };
    rank(0);
    if (__builtin_expect(any2, 0)) rank(8);
    if ((__any(bad) || tmo) && lane == 0) __hip_atomic_fetch_or((LAS unsigned*)(L + L_MISC + 8), tmo ? 3u : 1u, RLX_WG);
  } else {
    PRIO(0);
    load_w1<0>(w1d, w1off); load_w1<1>(w1d, w1off); load_w1<2>(w1d, w1off);
    wait_vm<16>(); xpose<0>(0x05040100u, 0x07060302u);
    wait_vm<8>(); xpose<1>(0x05040100u, 0x07060302u);
  }
  bar();
  STAMP(4);
  bool slow, timedout;
  int s0, nit;
  u64 pk = 0;
  const int n0 = bulk ? (10 - NCN) * w + (8 * NCN - 40) : NCN * w;
  const int nn = bulk ? 10 - NCN : NCN;
  {
    const unsigned cw = *(LAS unsigned*)(L + L_CNT + lane * 4);
    const unsigned flagv = __builtin_amdgcn_readfirstlane(*(LAS unsigned*)(L + L_MISC + 8));
    const unsigned deg = __builtin_amdgcn_sad_u8(cw, 0u, 0u), below = __builtin_amdgcn_sad_u8(cw & ((1u << (8 * (w & 3))) - 1u), 0u, 0u);
    const unsigned l4 = (deg + 4u) & ~3u;
    const bool lastn = lane < 4 * NCN ? (lane % NCN) == NCN - 1 : ((lane - 4 * NCN) % (10 - NCN)) == 9 - NCN;
    const unsigned plen = lane < TM ? l4 + (lastn ? 4u * (R - 1) : 0u) : 0u;
    const unsigned incl = wave_incl_scan(plen), start = incl - plen;
    timedout = (flagv & 2u) != 0u;
    slow = flagv != 0u || __builtin_amdgcn_readlane(incl, 63) > (unsigned)CAP;
    const unsigned tail = __builtin_amdgcn_readlane(incl, n0 + nn - 1) - 4 * (R - 1);
    s0 = __builtin_amdgcn_readlane(start, n0);
    nit = ((tail - s0) / 4 + R - 1) / R;
#pragma unroll
    for (int j = 0; j < 5; ++j) pk |= (u64)(((unsigned)__builtin_amdgcn_readlane(l4, n0 + j) >> 2) & 0xFFFu) << (12 * j);
    if (slow) s0 = 0;
    if (!slow) {
      const unsigned st2 = (unsigned)__builtin_amdgcn_ds_bpermute((n0 + (lane >> 3)) << 2, (int)(start | (deg << 16)));
      if (chain) {
        const unsigned B4 = L_FLAT + 4u * (lane < TM ? start + 1u + below : CAP + 64u);
        auto place = [&](int lo) __attribute__((always_inline)) {
          unsigned b[8];
#pragma unroll
          for (int i = 0; i < 8; ++i) b[i] = (unsigned)__builtin_amdgcn_ds_bpermute((int)((ent[lo + i] & 63u) << 2), (int)B4);
#pragma unroll
          for (int i = 0; i < 8; ++i) *(LAS unsigned*)(L + b[i] + 4u * rk[lo + i]) = ent[lo + i];
        };
        place(0);
        if (__builtin_expect(any2, 0)) place(8);
      }
      const unsigned dg = st2 >> 16, k = lane & 7, pp = k == 7 ? 0u : dg + 1u + k;
      const bool mine = (lane >> 3) < nn && pp < ((dg + 4u) & ~3u);
      const unsigned at = mine ? (st2 & 0xFFFFu) + pp : (lane >= 48 && lane - 48 < 4 * (R - 1) ? tail + lane - 48 : CAP + lane);
      flat[at] = (mine && k == 7) ? (unsigned)(node0 + n0 + (lane >> 3)) << 6 : EOOB;
    }
  }
  if (bulk) { load_w1<3>(w1d, w1off); wait_vm<8>(); xpose<2>(0x05040100u, 0x07060302u); }
  bar();
  STAMP(5);
  PRIO(2);
  if (w == 1 || w == 2) {
    asm volatile("s_waitcnt vmcnt(0)" ::: "memory");
    if (lane == 0 && tf < 256) __hip_atomic_store((GAS unsigned*)(ws + WS_WFLAG) + tf, (unsigned)tag, RLX_AGENT);
  }
  const unsigned l16 = m * 16;
  LAS unsigned* fp = flat + s0 + q;
  v4u bl[R], br[R];
  unsigned en[R];
#pragma unroll
  for (int i = 0; i < R; ++i) en[i] = fp[4 * i];
  const float epsf = bflo(*(LAS unsigned*)(L + L_MISC + 12));
#pragma unroll
  for (int i = 0; i < R; ++i) {
    const unsigned o = ((en[i] << 3) & ~511u) | l16;
    bl[i] = __builtin_amdgcn_raw_buffer_load_b128(xr, o, 0, 0);
    br[i] = __builtin_amdgcn_raw_buffer_load_b128(xr, o + 256, 0, 0);
    en[i] = fp[4 * (R + i)];
  }
  asm volatile("" ::: "memory");
  SB();
  if (chain) { PRIO(XPRIO); xpose<0>(0x05040100u, 0x07060302u); xpose<1>(0x05040100u, 0x07060302u); xpose<2>(0x05040100u, 0x07060302u); }
  wait_vm<2 * R>();
  xpose<3>(0x05040100u, 0x07060302u);
  if (chain) PRIO(0); else PRIO(BPRIO);
  STAMP(6);
  unsigned wu2 = 0;
  v4u fl0;
  if (__builtin_expect(!slow, 1)) {
    const v4u sel = {m == 0 ? 0x3F80u : (m == 1 ? 0x3F800000u : 0u), m == 2 ? 0x3F80u : (m == 3 ? 0x3F800000u : 0u),
                     m == 4 ? 0x3F80u : (m == 5 ? 0x3F800000u : 0u), m == 6 ? 0x3F80u : (m == 7 ? 0x3F800000u : 0u)};
    const v4u self = {sel.x | (lane == 8 ? 0x3F80u : (lane == 9 ? 0x3F800000u : 0u)), sel.y | (lane == 10 ? 0x3F80u : (lane == 11 ? 0x3F800000u : 0u)),
                      sel.z | (lane == 12 ? 0x3F80u : (lane == 13 ? 0x3F800000u : 0u)), sel.w | (lane == 14 ? 0x3F80u : (lane == 15 ? 0x3F800000u : 0u))};
    v4u cur = self;
    fp += 8 * R;
    f32x4 a0 = {0.f, 0.f, 0.f, 0.f}, a1 = {0.f, 0.f, 0.f, 0.f};
    unsigned haddr = L_H + (lane >> 5) * 256 + m * 16 + (q & 1) * 8 + n0 * HP;
    int rem = (int)(pk & 0xFFF);
#define SLOT(i, REFILL) { \
        a0 = mfma(cur, bl[i], a0); \
        a1 = mfma(cur, br[i], a1); \
        cur = sel; \
        if (REFILL) { \
          const unsigned o = ((en[i] << 3) & ~511u) | l16; \
          bl[i] = __builtin_amdgcn_raw_buffer_load_b128(xr, o, 0, 0); \
          br[i] = __builtin_amdgcn_raw_buffer_load_b128(xr, o + 256, 0, 0); \
          en[i] = fp[4 * i]; \
        } \
        if (__builtin_expect(--rem == 0, 0)) { \
          float f[4]; \
          _Pragma("unroll") for (int r = 0; r < 4; ++r) { \
            auto s2 = __builtin_amdgcn_permlane32_swap(asu(a0[r]), asu(a1[r]), false, false); \
            const unsigned u0 = s2[0], u1 = s2[1]; \
            f[r] = fmaf(epsf, asf(u1), asf(u0)); \
          } \
          *(LAS v2u*)(L + haddr) = v2u{pack_bf16(f[0], f[1]), pack_bf16(f[2], f[3])}; \
          haddr += HP; \
          a0 = f32x4{0.f, 0.f, 0.f, 0.f}; a1 = a0; \
          cur = self; \
          pk >>= 12; \
          rem = (int)(pk & 0xFFF); \
        } }
    wu2 = __builtin_amdgcn_raw_buffer_load_b32(wsr, lane < 8 ? WS_W2T + ((t >> 3) & 31) * 8192 + w * 1024 + lane * 128 : OOB, 0, SC1);
    for (; nit > 1; --nit) {
#pragma unroll
      for (int i = 0; i < R; ++i) SLOT(i, true)
      fp += 4 * R;
    }
    fl0 = __builtin_amdgcn_raw_buffer_load_b128(wsr, WS_WFLAG + lane * 16, 0, SC1);
#pragma unroll
    for (int i = 0; i < R; ++i) SLOT(i, false)
  } else {
#pragma unroll 1
    for (int j = 0; j < nn; ++j) {
      const int node = node0 + n0 + j;
      const v2u s2 = __builtin_amdgcn_raw_buffer_load_b64(xr, (unsigned)node * 512u + lane * 8, 0, 0);
      float a0 = bflo(s2.x), a1 = bfhi(s2.x), a2 = bflo(s2.y), a3 = bfhi(s2.y);
      if (node < N)
        for (int eb = 0; eb < E; eb += 64) {
          const int e = eb + lane;
          const int dv2 = (int)__builtin_amdgcn_raw_buffer_load_b32(rsrc(dst, E * 4), e * 4, 0, 0);
          const int sv2 = (int)__builtin_amdgcn_raw_buffer_load_b32(rsrc(src, E * 4), e * 4, 0, 0);
          u64 mkk = __ballot(dv2 == node && e < E);
          while (mkk) {
            const int b = __builtin_ctzll(mkk); mkk &= mkk - 1;
            const unsigned sidx = (unsigned)__builtin_amdgcn_readlane(sv2, b);
            const v2u rv = __builtin_amdgcn_raw_buffer_load_b64(xr, sidx < (unsigned)N ? sidx * 512u + lane * 8 : OOB, 0, 0);
            a0 += bflo(rv.x); a1 += bfhi(rv.x); a2 += bflo(rv.y); a3 += bfhi(rv.y);
          }
        }
      *(LAS v2u*)(L + L_H + (n0 + j) * HP + lane * 8) =
          v2u{pack_bf16(fmaf(epsf, bflo(s2.x), a0), fmaf(epsf, bfhi(s2.x), a1)), pack_bf16(fmaf(epsf, bflo(s2.y), a2), fmaf(epsf, bfhi(s2.y), a3))};
    }
    fl0 = __builtin_amdgcn_raw_buffer_load_b128(wsr, WS_WFLAG + lane * 16, 0, SC1);
  }
  STAMP(7);
  PRIO(0);
  {
    bool okw = false;
    const unsigned tl = (unsigned)tag;
    for (unsigned sp = 0; sp <= SPIN_CAP; ++sp) {
      if (__all(fl0.x == tl && fl0.y == tl && fl0.z == tl && fl0.w == tl)) { okw = true; break; }
      fl0 = __builtin_amdgcn_raw_buffer_load_b128(wsr, WS_WFLAG + lane * 16, 0, SC1);
    }
    if (__builtin_expect(!okw, 0)) {
#pragma unroll 1
      for (int i = 0; i < 32; ++i) { unsigned tv[8]; tile_w2_load(w2r, w * 32 + i, lane, tv); tile_w2_store(wsr, w * 32 + i, lane, tv); }
      asm volatile("s_waitcnt vmcnt(0)" ::: "memory");
    }
    __builtin_amdgcn_fence(__ATOMIC_ACQUIRE, "wavefront");
  }
  if (!timedout && tid == 192 && t == 0) __hip_atomic_store((GAS u64*)(ws + WS_EPOCH), tag, RLX_AGENT);
  asm volatile("" :: "v"(wu2), "v"(wu), "v"(wx), "v"(cpf));
  const unsigned w2off = WS_W2T + w * 32768 + lane * 16;
  static_for<0, W2PRE>([&](auto I) { load_w2<I>(wsd, w2off); });
  f32x4 bias1[4];
#pragma unroll
  for (int c = 0; c < 4; ++c) bias1[c] = *(LAS f32x4*)(L + L_B1 + (64 * w + 16 * q + 4 * c) * 4);
  SB();
  bar();
  STAMP(8);
  f32x4 bias2[2];
  {
    LAS unsigned char* hb = L + L_H + m * HP + q * 16;
    LAS unsigned char* h1w = L + L_H1 + m * H1P + (64 * w + 16 * q) * 2;
    constexpr int P1 = 5;
    v4u b[P1];
    f32x4 acc[3][4];
    v4u bb2;
    auto epi = [&](int mt, int half) __attribute__((always_inline)) {
      unsigned o[4];
#pragma unroll
      for (int k = 0; k < 4; ++k) o[k] = relu_pack(acc[mt][2 * half + (k >> 1)][2 * (k & 1)], acc[mt][2 * half + (k >> 1)][2 * (k & 1) + 1]);
      *(LAS v4u*)(h1w + mt * 16 * H1P + 16 * half) = v4u{o[0], o[1], o[2], o[3]};
    };
#define U1_KS(u) ((u) < 12 ? (u) / 3 : 4 + ((u) - 12) % 4)
#define U1_MT(u) ((u) < 12 ? (u) % 3 : ((u) - 12) / 4)
#pragma unroll
    for (int u = 0; u < P1; ++u) b[u] = *(LAS v4u*)(hb + U1_MT(u) * 16 * HP + U1_KS(u) * 64);
    static_for<0, 24>([&](auto U) {
      constexpr int u = U, ks = U1_KS(u), mt = U1_MT(u);
      if constexpr (u % 6 == 0) PRIO(3 - u / 6);
      static_for<0, 4>([&](auto C) {
        if constexpr (ks == 0) mfma_p0<PB + 4 * C>(acc[mt][C], b[u % P1], bias1[C]);
        else mfma_p<PB + 16 * ks + 4 * C>(acc[mt][C], b[u % P1]);
      });
      if constexpr (u + P1 < 24) b[u % P1] = *(LAS v4u*)(hb + U1_MT(u + P1) * 16 * HP + U1_KS(u + P1) * 64);
      if constexpr (u >= 2 && u < 18) load_w2<6 + u>(wsd, w2off);
      if constexpr (u == 20) bb2 = *(LAS v4u*)(L + L_B2 + (32 * w + 8 * q) * 2);
      if constexpr (u == 17 || u == 21) epi(mt - 1, 0);
      if constexpr (u == 18 || u == 22) epi(mt - 1, 1);
      SB();
    });
    asm volatile("s_nop 7" : "+v"(acc[2][0]), "+v"(acc[2][1]), "+v"(acc[2][2]), "+v"(acc[2][3]));
    epi(2, 0); epi(2, 1);
#pragma unroll
    for (int nt = 0; nt < 2; ++nt) {
      bias2[nt] = f32x4{bflo(bb2[2 * nt]), bfhi(bb2[2 * nt]), bflo(bb2[2 * nt + 1]), bfhi(bb2[2 * nt + 1])};
      asm volatile("" : "+v"(bias2[nt]));
    }
  }
  SB();
  STAMP(9);
  bar();
  v2u ep;
  {
    f32x4 acc[3][2];
    LAS unsigned char* hb = L + L_H1 + m * H1P + q * 16;
    const __amdgpu_buffer_rsrc_t outr = rsrc(out, N * 512);
    auto put = [&](int mt) __attribute__((always_inline)) {
      const int r = 16 * mt + m;
      v4u o;
      o.x = pack_bf16(acc[mt][0][0], acc[mt][0][1]); o.y = pack_bf16(acc[mt][0][2], acc[mt][0][3]);
      o.z = pack_bf16(acc[mt][1][0], acc[mt][1][1]); o.w = pack_bf16(acc[mt][1][2], acc[mt][1][3]);
      __builtin_amdgcn_raw_buffer_store_b128(o, outr, r < TM ? (unsigned)(node0 + r) * 512u + 64 * w + 16 * q : OOB, 0, OUTAUX);
    };
    constexpr int P2 = 8;
    v4u b[P2];
#define U2_KS(u) ((u) < 24 ? (u) / 3 : 8 + ((u) - 24) % 8)
#define U2_MT(u) ((u) < 24 ? (u) % 3 : ((u) - 24) / 8)
#pragma unroll
    for (int u = 0; u < P2; ++u) b[u] = *(LAS v4u*)(hb + U2_MT(u) * 16 * H1P + U2_KS(u) * 64);
    static_for<0, 48>([&](auto U) {
      constexpr int u = U, ks = U2_KS(u), mt = U2_MT(u);
      if constexpr (u % 12 == 0) PRIO(3 - u / 12);
      if constexpr (u < 16 && (u & 1) == 0) load_w2<24 + u / 2>(wsd, w2off);
      constexpr int issued = u < 16 ? 25 + u / 2 : 32;
      if constexpr (mt == 0) wait_vm<issued - (2 * ks + 2)>();
      if constexpr (ks == 0) { mfma_p0<w2reg(0)>(acc[mt][0], b[u % P2], bias2[0]); mfma_p0<w2reg(1)>(acc[mt][1], b[u % P2], bias2[1]); }
      else { mfma_p<w2reg(2 * ks)>(acc[mt][0], b[u % P2]); mfma_p<w2reg(2 * ks + 1)>(acc[mt][1], b[u % P2]); }
      if constexpr (u + P2 < 48) b[u % P2] = *(LAS v4u*)(hb + U2_MT(u + P2) * 16 * H1P + U2_KS(u + P2) * 64);
      if constexpr (u == 32) ep = __builtin_amdgcn_raw_buffer_load_b64(wsr, tid == 192 ? WS_EPOCH : OOB, 0, SC1);
      if constexpr (u == 36 || u == 44) put(mt - 1);
      SB();
    });
    asm volatile("s_nop 7" : "+v"(acc[2][0]), "+v"(acc[2][1]));
    put(2);
  }
  if (!timedout && tid == 192 && (ep.x != (unsigned)tag || ep.y != (unsigned)(tag >> 32))) __hip_atomic_store((GAS u64*)(ws + WS_EPOCH), tag, RLX_AGENT);
  STAMP(10);
  STAMP_OUT((unsigned long long*)(ws + (64u << 20)), blockIdx.x * 8 + w);
  while (__builtin_amdgcn_s_memrealtime() - t_begin < 3000 + (t_ph - t_begin)) __builtin_amdgcn_s_sleep(1);
  asm volatile(".Lgin_end:");
}

extern "C" void kernel_launch(void* const* d_in, const int* in_sizes, int n_in, void* d_out, int out_size, void* d_ws, size_t ws_size,
                              hipStream_t stream) {
  const int N = in_sizes[0] / D, E = in_sizes[1];
  const int T = (N + TM - 1) / TM;
  (void)hipFuncSetAttribute((const void*)gin_one, hipFuncAttributeMaxDynamicSharedMemorySize, L_END);
  gin_one<<<T, 512, L_END, stream>>>((const unsigned short*)d_in[0], (const int*)d_in[1], (const int*)d_in[2], (const unsigned short*)d_in[3],
                                 (const unsigned short*)d_in[4], (const unsigned short*)d_in[5], (const unsigned short*)d_in[6],
                                 (const unsigned short*)d_in[7], (unsigned short*)d_out, (unsigned char*)d_ws, N, E, T);
}
```

```cpp
#include <hip/hip_runtime.h>
#include <hip/hip_bf16.h>
#include <stdint.h>
#include <type_traits>

typedef unsigned v4u __attribute__((ext_vector_type(4)));
typedef unsigned v2u __attribute__((ext_vector_type(2)));
typedef float f32x4 __attribute__((ext_vector_type(4)));
typedef float f32x2 __attribute__((ext_vector_type(2)));
typedef short bf16x8 __attribute__((ext_vector_type(8)));
typedef __bf16 bf16x2_t __attribute__((ext_vector_type(2)));
typedef unsigned long long u64;
#define GAS __attribute__((address_space(1)))
#define LAS __attribute__((address_space(3)))
#define DI __device__ __forceinline__
#define RLX_AGENT __ATOMIC_RELAXED, __HIP_MEMORY_SCOPE_AGENT
#define RLX_WG __ATOMIC_RELAXED, __HIP_MEMORY_SCOPE_WORKGROUP
#define SB() __builtin_amdgcn_sched_barrier(0)
#define PRIO(p) __builtin_amdgcn_s_setprio(p)
#define STAMP(i)
#define STAMP_DECL
#define STAMP_OUT(base, idx)

constexpr int D = 256, H = 512, TM = 40;
constexpr int EPB = 640;
constexpr int CAP = 1536;
constexpr int HP = 544, H1P = 1056;
constexpr unsigned OOB = 0x40000000u, EOOB = OOB >> 3;
#define RING 4
constexpr int R = RING;
constexpr int SC1 = 16;
constexpr unsigned SPIN_CAP = 4096;
#define FAIR 1
#define XWARM 1
#define XP_EARLY 2
#define GATE 1
#define SPLIT 0
#define CELL_MODE 1
#define PRETOUCH 0
#define BPRIO 0
#define BSCHED 0
#define XPRIO 0
#define ESLEEP 3
#define PSLEEP 24
#define NB1 8
#define NB2 16
#define NA 16
#define NCN 5
#define OUTAUX 16
#define W2PRE 8
constexpr unsigned WS_EPOCH = 0, WS_WFLAG = 4096, WS_W2T = 65536, WS_CELL = 1u << 20, WS_BYTES = WS_CELL + 256u * 16384u;
constexpr int L_H = 0, L_H1 = L_H + 48 * HP, L_FLAT = L_H1 + 48 * H1P, L_CNT = L_FLAT + (CAP + 128) * 4, L_MISC = L_CNT + 256,
              L_B1 = L_MISC + 32, L_B2 = L_B1 + 2048, L_END = L_B2 + 512;
constexpr int L_PSTG = L_H;
constexpr int L_PCELL = L_H1, L_PCNT = L_H1 + 320 * 64;

DI unsigned pack_bf16(float a, float b) { f32x2 v = {a, b}; bf16x2_t r = __builtin_convertvector(v, bf16x2_t); return __builtin_bit_cast(unsigned, r); }
DI float bflo(unsigned u) { return __builtin_bit_cast(float, u << 16); }
DI float bfhi(unsigned u) { return __builtin_bit_cast(float, u & 0xffff0000u); }
DI float asf(unsigned u) { return __builtin_bit_cast(float, u); }
DI unsigned asu(float u) { return __builtin_bit_cast(unsigned, u); }
DI f32x4 mfma(v4u a, v4u b, f32x4 c) {
  return __builtin_amdgcn_mfma_f32_16x16x32_bf16(__builtin_bit_cast(bf16x8, a), __builtin_bit_cast(bf16x8, b), c, 0, 0, 0);
}
typedef short s16x2 __attribute__((ext_vector_type(2)));
DI unsigned relu_pack(float a, float b) {
  const s16x2 v = __builtin_bit_cast(s16x2, pack_bf16(a, b));
  return __builtin_bit_cast(unsigned, __builtin_elementwise_max(v, s16x2{0, 0}));
}
DI void bar() { asm volatile("s_waitcnt lgkmcnt(0)" ::: "memory"); __builtin_amdgcn_s_barrier(); asm volatile("" ::: "memory"); }
template <int CTRL> DI unsigned dpp_add(unsigned x) { return x + (unsigned)__builtin_amdgcn_update_dpp(0, (int)x, CTRL, 0xF, 0xF, false); }
DI unsigned wave_incl_scan(unsigned x) {
  x = dpp_add<0x111>(x); x = dpp_add<0x112>(x); x = dpp_add<0x114>(x); x = dpp_add<0x118>(x);
  x = x + (unsigned)__builtin_amdgcn_update_dpp(0, (int)x, 0x142, 0xA, 0xF, false);
  x = x + (unsigned)__builtin_amdgcn_update_dpp(0, (int)x, 0x143, 0xC, 0xF, false);
  return x;
}
DI unsigned bsum(unsigned a, unsigned b) { return __builtin_amdgcn_sad_u8(a, 0u, __builtin_amdgcn_sad_u8(b, 0u, 0u)); }
DI __amdgpu_buffer_rsrc_t rsrc(const void* p, unsigned bytes) { return __builtin_amdgcn_make_buffer_rsrc((void*)p, 0, bytes, 0x00020000); }
DI unsigned tag_b(u64 tag) { return ((unsigned)tag + (unsigned)(tag >> 24) * 0x9E3779B1u) & 0x7FFFFu; }

DI void tile_w2_load(__amdgpu_buffer_rsrc_t w2r, int f, int lane, unsigned (&v)[8]) {
  const int ww = f >> 5, ks = (f >> 1) & 15, nt = f & 1, i = lane & 15, kg = lane >> 4;
  const int qo = (2 * ks + (kg >> 1)) & 3;
  const int row0 = 64 * (ks >> 1) + 32 * (qo & 1) + 4 * (qo >> 1) + 2 * (kg & 1);
  const unsigned o = f < 256 ? (row0 * D + 32 * ww + 8 * (i >> 2) + 4 * nt + (i & 3)) * 2 : OOB;
#pragma unroll
  for (int j = 0; j < 8; ++j) v[j] = __builtin_amdgcn_raw_buffer_load_b16(w2r, o + (j >> 2) * D * 2, (j & 3) * 8 * D * 2, 0);
}
DI void tile_w2_store(__amdgpu_buffer_rsrc_t wsr, int f, int lane, const unsigned (&v)[8]) {
  const v4u o = {v[0] | (v[1] << 16), v[2] | (v[3] << 16), v[4] | (v[5] << 16), v[6] | (v[7] << 16)};
  __builtin_amdgcn_raw_buffer_store_b128(o, wsr, f < 256 ? WS_W2T + f * 1024 + lane * 16 : OOB, 0, SC1);
}
constexpr int PB = 96;
template <int G> DI void load_w1(v4u srd, unsigned off) {
  unsigned so;
  constexpr int r = PB + 32 + 32 * G;
  asm volatile("s_mov_b32 %0, %3\n\t"
               "buffer_load_dwordx4 v[%c5:%c5+3], %1, %2, %0 offen\n\tbuffer_load_dwordx4 v[%c5+4:%c5+7], %1, %2, %0 offen offset:1024\n\t"
               "buffer_load_dwordx4 v[%c5+8:%c5+11], %1, %2, %0 offen offset:2048\n\tbuffer_load_dwordx4 v[%c5+12:%c5+15], %1, %2, %0 offen offset:3072\n\t"
               "s_mov_b32 %0, %4\n\t"
               "buffer_load_dwordx4 v[%c5+16:%c5+19], %1, %2, %0 offen\n\tbuffer_load_dwordx4 v[%c5+20:%c5+23], %1, %2, %0 offen offset:1024\n\t"
               "buffer_load_dwordx4 v[%c5+24:%c5+27], %1, %2, %0 offen offset:2048\n\tbuffer_load_dwordx4 v[%c5+28:%c5+31], %1, %2, %0 offen offset:3072"
               : "=&s"(so) : "v"(off), "s"(srd), "i"(G * 65536), "i"(G * 65536 + 4096), "i"(r) : "v255");
}
template <int G> DI void xpose(unsigned lo, unsigned hi) {
#pragma unroll
  for (int d = 0; d < 4; ++d)
#pragma unroll
    for (int p = 0; p < 2; ++p) {
      unsigned t0, t1;
      asm volatile("v_perm_b32 v[%c5], v[%c4+4], v[%c4], %2\n\tv_perm_b32 v[%c5+16], v[%c4+6], v[%c4+2], %2\n\t"
                   "v_perm_b32 v[%c5+4], v[%c4+4], v[%c4], %3\n\tv_perm_b32 v[%c5+20], v[%c4+6], v[%c4+2], %3\n\t"
                   "v_mov_b32 %0, v[%c5]\n\tv_mov_b32 %1, v[%c5+4]\n\t"
                   "v_mov_b32_dpp v[%c5], v[%c5+16] row_ror:8 row_mask:0xf bank_mask:0xc\n\t"
                   "v_mov_b32_dpp v[%c5+4], v[%c5+20] row_ror:8 row_mask:0xf bank_mask:0xc\n\t"
                   "v_mov_b32_dpp v[%c5+16], %0 row_ror:8 row_mask:0xf bank_mask:0x3\n\t"
                   "v_mov_b32_dpp v[%c5+20], %1 row_ror:8 row_mask:0xf bank_mask:0x3"
                   : "=&v"(t0), "=&v"(t1) : "s"(lo), "s"(hi), "i"(PB + 32 + 32 * G + 8 * d + p), "i"(PB + 32 * G + 8 * p + d));
    }
}
DI void xpose_all() { xpose<0>(0x05040100u, 0x07060302u); xpose<1>(0x05040100u, 0x07060302u); xpose<2>(0x05040100u, 0x07060302u); xpose<3>(0x05040100u, 0x07060302u); }
constexpr int w2reg(int i) { return i < 8 ? PB + 128 + 4 * i : PB + 4 * (i - 8); }
template <int I> DI void load_w2(v4u srd, unsigned off) {
  unsigned so;
  asm volatile("s_mov_b32 %0, %3\n\tbuffer_load_dwordx4 v[%c4:%c4+3], %1, %2, %0 offen offset:%c5 sc1"
               : "=&s"(so) : "v"(off), "s"(srd), "i"((I >> 2) * 4096), "i"(w2reg(I)), "i"((I & 3) * 1024));
}
template <int A> DI void mfma_p(f32x4& acc, v4u b) { asm volatile("v_mfma_f32_16x16x32_bf16 %0, v[%c2:%c2+3], %1, %0" : "+v"(acc) : "v"(b), "i"(A)); }
template <int A> DI void mfma_p0(f32x4& acc, v4u b, f32x4 c) {
  asm volatile("v_mfma_f32_16x16x32_bf16 %0, v[%c3:%c3+3], %1, %2" : "=&v"(acc) : "v"(b), "v"(c), "i"(A));
}
template <int N> DI void wait_vm() { asm volatile("s_waitcnt vmcnt(%c0)" :: "i"(N)); }
template <int I, int E, typename F> DI void static_for(F&& f) { if constexpr (I < E) { f(std::integral_constant<int, I>{}); static_for<I + 1, E>(f); } }

__global__ __launch_bounds__(512, 2) __attribute__((amdgpu_num_vgpr(PB / 2))) void gin_one(const unsigned short* __restrict__ x, const int* __restrict__ src, const int* __restrict__ dst,
                                                  const unsigned short* __restrict__ epsp, const unsigned short* __restrict__ w1,
                                                  const unsigned short* __restrict__ b1, const unsigned short* __restrict__ w2,
                                                  const unsigned short* __restrict__ b2, unsigned short* __restrict__ out, unsigned char* ws,
                                                  int N, int E, int T) {
  extern __shared__ __attribute__((aligned(16))) unsigned char Ls[];
  LAS unsigned char* L = (LAS unsigned char*)Ls;
  const int tid = threadIdx.x, lane = tid & 63, w = __builtin_amdgcn_readfirstlane(tid >> 6);
  const int t = blockIdx.x, node0 = t * TM;
  const unsigned bulk = w >> 2, chain = bulk ^ 1;
  unsigned cpf;
  {
    u64 pc; unsigned len;
    asm volatile("s_getpc_b64 %0\n.Lgin_pc:\n\ts_mov_b32 %1, .Lgin_end-.Lgin_pc" : "=s"(pc), "=s"(len));
    cpf = __builtin_amdgcn_raw_buffer_load_b32(rsrc((const void*)pc, len), (((t >> 3) & 31) * 8 + w) * 128 + lane * 32768, 0, 0);
  }
  const __amdgpu_buffer_rsrc_t wsr = rsrc(ws, WS_BYTES), xr = rsrc(x, N * 512), w1r = rsrc(w1, D * H * 2), w2r = rsrc(w2, D * H * 2);
  STAMP_DECL STAMP(0);
  const int q = lane >> 4, m = lane & 15;
  const v4u w1d = {(unsigned)(u64)w1, (unsigned)((u64)w1 >> 32) & 0xffffu, D * H * 2, 0x00020000u};
  const v4u wsd = {(unsigned)(u64)ws, (unsigned)((u64)ws >> 32) & 0xffffu, WS_BYTES, 0x00020000u};
  const unsigned w1off = (((lane >> 3) & 1) * 32 + q * 8) * 1024 + w * 128 + (lane & 7) * 16;
  LAS unsigned* pcnt = (LAS unsigned*)(L + L_PCNT);
  LAS unsigned* pcell = (LAS unsigned*)(L + L_PCELL);

  if (w != 4) __builtin_amdgcn_s_sleep(ESLEEP);
  const unsigned wu = __builtin_amdgcn_raw_buffer_load_b32(w1r, w == 0 ? ((t >> 3) & 31) * 8192 + lane * 128 : OOB, 0, 0);
  const unsigned pt = 0;
  const int tf = t + (w - 1) * T;
  if (w == 4) {
    PRIO(3);
    const __amdgpu_buffer_rsrc_t dr = rsrc(dst, E * 4), sr = rsrc(src, E * 4);
    v4u dv[3], sv[3];
#pragma unroll
    for (int i = 0; i < 3; ++i) {
      const unsigned o = (i == 2 && lane >= 32) ? OOB : (unsigned)(t * EPB + (lane + 64 * i) * 4) * 4u;
      dv[i] = __builtin_amdgcn_raw_buffer_load_b128(dr, o, 0, 0);
      sv[i] = __builtin_amdgcn_raw_buffer_load_b128(sr, o, 0, 0);
    }
    const u64 ep0 = __hip_atomic_load((GAS u64*)(ws + WS_EPOCH), RLX_AGENT);
    SB();
    *(LAS v4u*)(pcnt + lane * 4) = v4u{0, 0, 0, 0};
    pcnt[256 + lane] = 0;
    const unsigned dummy = 256u + lane;
    unsigned tt[12], rk[12];
#pragma unroll
    for (int i = 0; i < 12; ++i) {
      const int e = t * EPB + (lane + 64 * (i >> 2)) * 4;
      const unsigned lo = (e < E && !(i >= 8 && lane >= 32)) ? 0u : dummy;
      tt[i] = max(min(dv[i >> 2][i & 3] / (unsigned)TM, dummy), lo);
      rk[i] = __hip_atomic_fetch_add(pcnt + tt[i], 1u, RLX_WG);
    }
#pragma unroll
    for (int i = 0; i < 12; ++i)
      pcell[tt[i] * 16 + (rk[i] & 15u)] = (dv[i >> 2][i & 3] - tt[i] * TM) | (min(sv[i >> 2][i & 3], 16383u) << 6);
    *(LAS u64*)(L + L_MISC) = ep0;
  } else {
   if (w == 3) {
    const v4u v1 = __builtin_amdgcn_raw_buffer_load_b128(rsrc(b1, H * 2), lane * 16, 0, 0);
    const v4u v2 = __builtin_amdgcn_raw_buffer_load_b128(rsrc(b2, D * 2), lane * 16, 0, 0);
    const unsigned ev = epsp[0];
    *(LAS unsigned*)(L + L_CNT + lane * 4) = 0;
#pragma unroll
    for (int i = 0; i < 8; ++i)
      *(LAS float*)(L + L_B1 + ((lane >> 3) * 64 + (((lane >> 2) & 1) + 2 * (i >> 2)) * 16 + 4 * (i & 3) + (lane & 3)) * 4) = (i & 1) ? bfhi(v1[i >> 1]) : bflo(v1[i >> 1]);
    *(LAS v4u*)(L + (lane < 32 ? L_B2 + lane * 16 : L_FLAT + lane * 16)) = v2;
    *(LAS v2u*)(L + L_MISC + 8) = v2u{0u, ev};
    *(LAS unsigned*)(L + L_MISC + 16) = 0u;
   } else if (w == 1 || w == 2) {
    unsigned tv[8];
    tile_w2_load(w2r, tf, lane, tv);
    tile_w2_store(wsr, tf, lane, tv);
   }
  }
  if (chain) { load_w1<0>(w1d, w1off); load_w1<1>(w1d, w1off); }
  PRIO(3);
  bar();
  STAMP(1);
  const u64 tag = *(LAS u64*)(L + L_MISC) + 1;
  const unsigned tagA = (unsigned)tag & 0xFFFFFFu, tagB = tag_b(tag);
  {
    v4u e[2]; unsigned c[2];
#pragma unroll
    for (int i = 0; i < 2; ++i) { e[i] = *(LAS v4u*)(pcell + (w * 2 + i) * 256 + lane * 4); c[i] = pcnt[(w * 2 + i) * 16 + (lane >> 2)]; }
#pragma unroll
    for (int i = 0; i < 2; ++i) {
      const unsigned tc = (w * 2 + i) * 16 + (lane >> 2), cc = min(c[i], 31u);
      const v4u g = {(e[i].x & 0xFFFFFu) | (e[i].y << 20), ((e[i].y >> 12) & 0xFFu) | (tagA << 8), (e[i].z & 0xFFFFFu) | (e[i].w << 20),
                     ((e[i].w >> 12) & 0xFFu) | (cc << 8) | (tagB << 13)};
      __builtin_amdgcn_raw_buffer_store_b128(g, wsr, tc < (unsigned)T ? WS_CELL + tc * 16384 + t * 64 + (lane & 3) * 16 : OOB, 0, SC1);
    }
  }
  SB();
  PRIO(1);
  if (chain) { load_w1<2>(w1d, w1off); load_w1<3>(w1d, w1off); } else __builtin_amdgcn_s_sleep(PSLEEP);
  STAMP(2);
  bool any2 = false;
  unsigned wx;
  asm volatile("" : "=v"(wx));
  unsigned ent[16], rk[16], spins = 0;
  LAS unsigned* flat = (LAS unsigned*)(L + L_FLAT);
  if (chain) {
    PRIO(2);
    v4u g[4];
    const unsigned coff = WS_CELL + t * 16384 + w * 4096 + lane * 16;
    constexpr unsigned CSTEP = 1024;
#pragma unroll
    for (int i = 0; i < 4; ++i) g[i] = __builtin_amdgcn_raw_buffer_load_b128(wsr, coff + i * CSTEP, 0, SC1);
    bool tmo = false;
    const int pl = T - 64 * w - (lane >> 2);
#define GR_OK(i) (16 * (i) >= pl || ((g[i].y >> 8) == tagA && (g[i].w >> 13) == tagB))
    if (!__all(GR_OK(0) && GR_OK(1) && GR_OK(2) && GR_OK(3))) {
      for (;;) {
#pragma unroll
        for (int i = 0; i < 4; ++i) {
          const bool ok = GR_OK(i);
          const v4u nv = __builtin_amdgcn_raw_buffer_load_b128(wsr, ok ? OOB : coff + i * CSTEP, 0, SC1);
#pragma unroll
          for (int k = 0; k < 4; ++k) g[i][k] = ok ? g[i][k] : nv[k];
        }
        if (__all(GR_OK(0) && GR_OK(1) && GR_OK(2) && GR_OK(3))) break;
        if (++spins > SPIN_CAP) { tmo = true; break; }
      }
      ++spins;
    }
    STAMP(3);
    if (lane == 0) __hip_atomic_fetch_add((LAS unsigned*)(L + L_MISC + 16), 1u, RLX_WG);
    LAS unsigned char* stg = L + L_PSTG + w * 5120;
#pragma unroll
    for (int i = 0; i < 4; ++i) *(LAS v4u*)(stg + (16 * i + (lane >> 2)) * 80 + (lane & 3) * 16) = g[i];
#pragma unroll
    for (int i = 0; i < 4; ++i) g[i] = *(LAS v4u*)(stg + lane * 80 + i * 16);
    const int nval = (64 * w + lane < T && !tmo) ? (int)((g[0].w >> 8) & 31u) : 0;
    any2 = __any(nval > 8);
    bool bad = nval > 16;
    const unsigned dummy = 40u + (unsigned)(lane % 24);
    auto rank = [&](int lo) __attribute__((always_inline)) {
#pragma unroll
      for (int i = lo; i < lo + 8; ++i) {
        const unsigned a = g[i >> 2][i & 2], b = g[i >> 2][(i & 2) + 1];
        ent[i] = i < nval ? ((i & 1) ? __builtin_amdgcn_alignbit(b, a, 20) : a) & 0xFFFFFu : dummy;
        rk[i] = __hip_atomic_fetch_add((LAS unsigned*)(L + L_CNT + ((ent[i] & 63u) << 2)), 1u << (8 * w), RLX_WG);
      }
#pragma unroll
      for (int i = lo; i < lo + 8; ++i) { rk[i] = (rk[i] >> (8 * w)) & 255u; bad |= rk[i] == 255u; }
    };
    rank(0);
    if (__builtin_expect(any2, 0)) rank(8);
    if ((__any(bad) || tmo) && lane == 0) __hip_atomic_fetch_or((LAS unsigned*)(L + L_MISC + 8), tmo ? 3u : 1u, RLX_WG);
  } else {
    PRIO(0);
    load_w1<0>(w1d, w1off); load_w1<1>(w1d, w1off);
    wait_vm<8>(); xpose<0>(0x05040100u, 0x07060302u);
    wait_vm<0>(); xpose<1>(0x05040100u, 0x07060302u);
    while (__builtin_amdgcn_readfirstlane(__hip_atomic_load((LAS unsigned*)(L + L_MISC + 16), RLX_WG)) < 4u) __builtin_amdgcn_s_sleep(1);
    load_w1<2>(w1d, w1off);
    load_w1<3>(w1d, w1off);
    {
      const unsigned o = XWARM && (w - 4) * 64 + lane < 160 ? node0 * 512 + ((w - 4) * 64 + lane) * 128 : OOB;
      const v4u xd = {(unsigned)(u64)x, (unsigned)((u64)x >> 32) & 0xffffu, (unsigned)N * 512u, 0x00020000u};
      asm volatile("buffer_load_dword %0, %1, %2, 0 offen" : "+v"(wx) : "v"(o), "s"(xd));
    }
  }
  bar();
  STAMP(4);
  bool slow, timedout;
  int s0, nit;
  u64 pk = 0;
  const int n0 = bulk ? (10 - NCN) * w + (8 * NCN - 40) : NCN * w;
  const int nn = bulk ? 10 - NCN : NCN;
  {
    const unsigned cw = *(LAS unsigned*)(L + L_CNT + lane * 4);
    const unsigned flagv = __builtin_amdgcn_readfirstlane(*(LAS unsigned*)(L + L_MISC + 8));
    const unsigned deg = __builtin_amdgcn_sad_u8(cw, 0u, 0u), below = __builtin_amdgcn_sad_u8(cw & ((1u << (8 * (w & 3))) - 1u), 0u, 0u);
    const unsigned l4 = (deg + 4u) & ~3u;
    const bool lastn = lane < 4 * NCN ? (lane % NCN) == NCN - 1 : ((lane - 4 * NCN) % (10 - NCN)) == 9 - NCN;
    const unsigned plen = lane < TM ? l4 + (lastn ? 4u * (R - 1) : 0u) : 0u;
    const unsigned incl = wave_incl_scan(plen), start = incl - plen;
    timedout = (flagv & 2u) != 0u;
    slow = flagv != 0u || __builtin_amdgcn_readlane(incl, 63) > (unsigned)CAP;
    const unsigned tail = __builtin_amdgcn_readlane(incl, n0 + nn - 1) - 4 * (R - 1);
    s0 = __builtin_amdgcn_readlane(start, n0);
    nit = ((tail - s0) / 4 + R - 1) / R;
#pragma unroll
    for (int j = 0; j < 5; ++j) pk |= (u64)(((unsigned)__builtin_amdgcn_readlane(l4, n0 + j) >> 2) & 0xFFFu) << (12 * j);
    if (slow) s0 = 0;
    if (!slow) {
      const unsigned st2 = (unsigned)__builtin_amdgcn_ds_bpermute((n0 + (lane >> 3)) << 2, (int)(start | (deg << 16)));
      if (chain) {
        const unsigned B4 = L_FLAT + 4u * (lane < TM ? start + 1u + below : CAP + 64u);
        auto place = [&](int lo) __attribute__((always_inline)) {
          unsigned b[8];
#pragma unroll
          for (int i = 0; i < 8; ++i) b[i] = (unsigned)__builtin_amdgcn_ds_bpermute((int)((ent[lo + i] & 63u) << 2), (int)B4);
#pragma unroll
          for (int i = 0; i < 8; ++i) *(LAS unsigned*)(L + b[i] + 4u * rk[lo + i]) = ent[lo + i];
        };
        place(0);
        if (__builtin_expect(any2, 0)) place(8);
      }
      const unsigned dg = st2 >> 16, k = lane & 7, pp = k == 7 ? 0u : dg + 1u + k;
      const bool mine = (lane >> 3) < nn && pp < ((dg + 4u) & ~3u);
      const unsigned at = mine ? (st2 & 0xFFFFu) + pp : (lane >= 48 && lane - 48 < 4 * (R - 1) ? tail + lane - 48 : CAP + lane);
      flat[at] = (mine && k == 7) ? (unsigned)(node0 + n0 + (lane >> 3)) << 6 : EOOB;
    }
  }
  if (bulk) {
    wait_vm<9>(); xpose<2>(0x05040100u, 0x07060302u);
  }
  bar();
  STAMP(5);
  PRIO(2);
  if (w == 1 || w == 2) {
    asm volatile("s_waitcnt vmcnt(0)" ::: "memory");
    if (lane == 0 && tf < 256) __hip_atomic_store((GAS unsigned*)(ws + WS_WFLAG) + tf, (unsigned)tag, RLX_AGENT);
  }
  const unsigned l16 = m * 16;
  LAS unsigned* fp = flat + s0 + q;
  v4u bl[R], br[R];
  unsigned en[R];
#pragma unroll
  for (int i = 0; i < R; ++i) en[i] = fp[4 * i];
  const float epsf = bflo(*(LAS unsigned*)(L + L_MISC + 12));
#pragma unroll
  for (int i = 0; i < R; ++i) {
    const unsigned o = ((en[i] << 3) & ~511u) | l16;
    bl[i] = __builtin_amdgcn_raw_buffer_load_b128(xr, o, 0, 0);
    br[i] = __builtin_amdgcn_raw_buffer_load_b128(xr, o + 256, 0, 0);
    en[i] = fp[4 * (R + i)];
  }
  asm volatile("" ::: "memory");
  SB();
  if (chain) { PRIO(XPRIO); xpose<0>(0x05040100u, 0x07060302u); xpose<1>(0x05040100u, 0x07060302u); xpose<2>(0x05040100u, 0x07060302u); }
  wait_vm<2 * R + 1>();
  xpose<3>(0x05040100u, 0x07060302u);
  if (chain) PRIO(0); else PRIO(BPRIO);
  STAMP(6);
  unsigned wu2 = 0;
  v4u fl0;
  if (__builtin_expect(!slow, 1)) {
    const v4u sel = {m == 0 ? 0x3F80u : (m == 1 ? 0x3F800000u : 0u), m == 2 ? 0x3F80u : (m == 3 ? 0x3F800000u : 0u),
                     m == 4 ? 0x3F80u : (m == 5 ? 0x3F800000u : 0u), m == 6 ? 0x3F80u : (m == 7 ? 0x3F800000u : 0u)};
    const v4u self = {sel.x | (lane == 8 ? 0x3F80u : (lane == 9 ? 0x3F800000u : 0u)), sel.y | (lane == 10 ? 0x3F80u : (lane == 11 ? 0x3F800000u : 0u)),
                      sel.z | (lane == 12 ? 0x3F80u : (lane == 13 ? 0x3F800000u : 0u)), sel.w | (lane == 14 ? 0x3F80u : (lane == 15 ? 0x3F800000u : 0u))};
    v4u cur = self;
    fp += 8 * R;
    f32x4 a0 = {0.f, 0.f, 0.f, 0.f}, a1 = {0.f, 0.f, 0.f, 0.f};
    unsigned haddr = L_H + (lane >> 5) * 256 + m * 16 + (q & 1) * 8 + n0 * HP;
    int rem = (int)(pk & 0xFFF);
#define SLOT(i, REFILL) { \
        a0 = mfma(cur, bl[i], a0); \
        a1 = mfma(cur, br[i], a1); \
        cur = sel; \
        if (REFILL) { \
          const unsigned o = ((en[i] << 3) & ~511u) | l16; \
          bl[i] = __builtin_amdgcn_raw_buffer_load_b128(xr, o, 0, 0); \
          br[i] = __builtin_amdgcn_raw_buffer_load_b128(xr, o + 256, 0, 0); \
          en[i] = fp[4 * i]; \
        } \
        if (__builtin_expect(--rem == 0, 0)) { \
          float f[4]; \
          _Pragma("unroll") for (int r = 0; r < 4; ++r) { \
            auto s2 = __builtin_amdgcn_permlane32_swap(asu(a0[r]), asu(a1[r]), false, false); \
            const unsigned u0 = s2[0], u1 = s2[1]; \
            f[r] = fmaf(epsf, asf(u1), asf(u0)); \
          } \
          *(LAS v2u*)(L + haddr) = v2u{pack_bf16(f[0], f[1]), pack_bf16(f[2], f[3])}; \
          haddr += HP; \
          a0 = f32x4{0.f, 0.f, 0.f, 0.f}; a1 = a0; \
          cur = self; \
          pk >>= 12; \
          rem = (int)(pk & 0xFFF); \
        } }
    wu2 = __builtin_amdgcn_raw_buffer_load_b32(wsr, lane < 8 ? WS_W2T + ((t >> 3) & 31) * 8192 + w * 1024 + lane * 128 : OOB, 0, SC1);
    for (; nit > 1; --nit) {
#pragma unroll
      for (int i = 0; i < R; ++i) SLOT(i, true)
      fp += 4 * R;
    }
    fl0 = __builtin_amdgcn_raw_buffer_load_b128(wsr, WS_WFLAG + lane * 16, 0, SC1);
#pragma unroll
    for (int i = 0; i < R; ++i) SLOT(i, false)
  } else {
#pragma unroll 1
    for (int j = 0; j < nn; ++j) {
      const int node = node0 + n0 + j;
      const v2u s2 = __builtin_amdgcn_raw_buffer_load_b64(xr, (unsigned)node * 512u + lane * 8, 0, 0);
      float a0 = bflo(s2.x), a1 = bfhi(s2.x), a2 = bflo(s2.y), a3 = bfhi(s2.y);
      if (node < N)
        for (int eb = 0; eb < E; eb += 64) {
          const int e = eb + lane;
          const int dv2 = (int)__builtin_amdgcn_raw_buffer_load_b32(rsrc(dst, E * 4), e * 4, 0, 0);
          const int sv2 = (int)__builtin_amdgcn_raw_buffer_load_b32(rsrc(src, E * 4), e * 4, 0, 0);
          u64 mkk = __ballot(dv2 == node && e < E);
          while (mkk) {
            const int b = __builtin_ctzll(mkk); mkk &= mkk - 1;
            const unsigned sidx = (unsigned)__builtin_amdgcn_readlane(sv2, b);
            const v2u rv = __builtin_amdgcn_raw_buffer_load_b64(xr, sidx < (unsigned)N ? sidx * 512u + lane * 8 : OOB, 0, 0);
            a0 += bflo(rv.x); a1 += bfhi(rv.x); a2 += bflo(rv.y); a3 += bfhi(rv.y);
          }
        }
      *(LAS v2u*)(L + L_H + (n0 + j) * HP + lane * 8) =
          v2u{pack_bf16(fmaf(epsf, bflo(s2.x), a0), fmaf(epsf, bfhi(s2.x), a1)), pack_bf16(fmaf(epsf, bflo(s2.y), a2), fmaf(epsf, bfhi(s2.y), a3))};
    }
    fl0 = __builtin_amdgcn_raw_buffer_load_b128(wsr, WS_WFLAG + lane * 16, 0, SC1);
  }
  STAMP(7);
  PRIO(0);
  {
    bool okw = false;
    const unsigned tl = (unsigned)tag;
    for (unsigned sp = 0; sp <= SPIN_CAP; ++sp) {
      if (__all(fl0.x == tl && fl0.y == tl && fl0.z == tl && fl0.w == tl)) { okw = true; break; }
      fl0 = __builtin_amdgcn_raw_buffer_load_b128(wsr, WS_WFLAG + lane * 16, 0, SC1);
    }
    if (__builtin_expect(!okw, 0)) {
#pragma unroll 1
      for (int i = 0; i < 32; ++i) { unsigned tv[8]; tile_w2_load(w2r, w * 32 + i, lane, tv); tile_w2_store(wsr, w * 32 + i, lane, tv); }
      asm volatile("s_waitcnt vmcnt(0)" ::: "memory");
    }
    __builtin_amdgcn_fence(__ATOMIC_ACQUIRE, "wavefront");
  }
  if (!timedout && tid == 192 && t == 0) __hip_atomic_store((GAS u64*)(ws + WS_EPOCH), tag, RLX_AGENT);
  asm volatile("" :: "v"(wu2), "v"(wu), "v"(wx), "v"(cpf), "v"(pt));
  const unsigned w2off = WS_W2T + w * 32768 + lane * 16;
  static_for<0, W2PRE>([&](auto I) { load_w2<I>(wsd, w2off); });
  f32x4 bias1[4];
#pragma unroll
  for (int c = 0; c < 4; ++c) bias1[c] = *(LAS f32x4*)(L + L_B1 + (64 * w + 16 * q + 4 * c) * 4);
  SB();
  bar();
  STAMP(8);
  f32x4 bias2[2];
  {
    LAS unsigned char* hb = L + L_H + m * HP + q * 16;
    LAS unsigned char* h1w = L + L_H1 + m * H1P + (64 * w + 16 * q) * 2;
    constexpr int P1 = 5;
    v4u b[P1];
    f32x4 acc[3][4];
    v4u bb2;
    auto epi = [&](int mt, int half) __attribute__((always_inline)) {
      unsigned o[4];
#pragma unroll
      for (int k = 0; k < 4; ++k) o[k] = relu_pack(acc[mt][2 * half + (k >> 1)][2 * (k & 1)], acc[mt][2 * half + (k >> 1)][2 * (k & 1) + 1]);
      *(LAS v4u*)(h1w + mt * 16 * H1P + 16 * half) = v4u{o[0], o[1], o[2], o[3]};
    };
#define U1_KS(u) ((u) < 12 ? (u) / 3 : 4 + ((u) - 12) % 4)
#define U1_MT(u) ((u) < 12 ? (u) % 3 : ((u) - 12) / 4)
#pragma unroll
    for (int u = 0; u < P1; ++u) b[u] = *(LAS v4u*)(hb + U1_MT(u) * 16 * HP + U1_KS(u) * 64);
    static_for<0, 24>([&](auto U) {
      constexpr int u = U, ks = U1_KS(u), mt = U1_MT(u);
      if constexpr (u % 6 == 0) PRIO(3 - u / 6);
      static_for<0, 4>([&](auto C) {
        if constexpr (ks == 0) mfma_p0<PB + 4 * C>(acc[mt][C], b[u % P1], bias1[C]);
        else mfma_p<PB + 16 * ks + 4 * C>(acc[mt][C], b[u % P1]);
      });
      if constexpr (u + P1 < 24) b[u % P1] = *(LAS v4u*)(hb + U1_MT(u + P1) * 16 * HP + U1_KS(u + P1) * 64);
      if constexpr (u >= 2 && u < 18) load_w2<6 + u>(wsd, w2off);
      if constexpr (u == 20) bb2 = *(LAS v4u*)(L + L_B2 + (32 * w + 8 * q) * 2);
      if constexpr (u == 17 || u == 21) epi(mt - 1, 0);
      if constexpr (u == 18 || u == 22) epi(mt - 1, 1);
      SB();
    });
    asm volatile("s_nop 7" : "+v"(acc[2][0]), "+v"(acc[2][1]), "+v"(acc[2][2]), "+v"(acc[2][3]));
    epi(2, 0); epi(2, 1);
#pragma unroll
    for (int nt = 0; nt < 2; ++nt) {
      bias2[nt] = f32x4{bflo(bb2[2 * nt]), bfhi(bb2[2 * nt]), bflo(bb2[2 * nt + 1]), bfhi(bb2[2 * nt + 1])};
      asm volatile("" : "+v"(bias2[nt]));
    }
  }
  SB();
  STAMP(9);
  bar();
  v2u ep;
  {
    f32x4 acc[3][2];
    LAS unsigned char* hb = L + L_H1 + m * H1P + q * 16;
    const __amdgpu_buffer_rsrc_t outr = rsrc(out, N * 512);
    auto put = [&](int mt) __attribute__((always_inline)) {
      const int r = 16 * mt + m;
      v4u o;
      o.x = pack_bf16(acc[mt][0][0], acc[mt][0][1]); o.y = pack_bf16(acc[mt][0][2], acc[mt][0][3]);
      o.z = pack_bf16(acc[mt][1][0], acc[mt][1][1]); o.w = pack_bf16(acc[mt][1][2], acc[mt][1][3]);
      __builtin_amdgcn_raw_buffer_store_b128(o, outr, r < TM ? (unsigned)(node0 + r) * 512u + 64 * w + 16 * q : OOB, 0, OUTAUX);
    };
    constexpr int P2 = 8;
    v4u b[P2];
#define U2_KS(u) ((u) < 24 ? (u) / 3 : 8 + ((u) - 24) % 8)
#define U2_MT(u) ((u) < 24 ? (u) % 3 : ((u) - 24) / 8)
#pragma unroll
    for (int u = 0; u < P2; ++u) b[u] = *(LAS v4u*)(hb + U2_MT(u) * 16 * H1P + U2_KS(u) * 64);
    static_for<0, 48>([&](auto U) {
      constexpr int u = U, ks = U2_KS(u), mt = U2_MT(u);
      if constexpr (u % 12 == 0) PRIO(3 - u / 12);
      if constexpr (u < 16 && (u & 1) == 0) load_w2<24 + u / 2>(wsd, w2off);
      constexpr int issued = u < 16 ? 25 + u / 2 : 32;
      if constexpr (mt == 0) wait_vm<issued - (2 * ks + 2)>();
      if constexpr (ks == 0) { mfma_p0<w2reg(0)>(acc[mt][0], b[u % P2], bias2[0]); mfma_p0<w2reg(1)>(acc[mt][1], b[u % P2], bias2[1]); }
      else { mfma_p<w2reg(2 * ks)>(acc[mt][0], b[u % P2]); mfma_p<w2reg(2 * ks + 1)>(acc[mt][1], b[u % P2]); }
      if constexpr (u + P2 < 48) b[u % P2] = *(LAS v4u*)(hb + U2_MT(u + P2) * 16 * H1P + U2_KS(u + P2) * 64);
      if constexpr (u == 32) ep = __builtin_amdgcn_raw_buffer_load_b64(wsr, tid == 192 ? WS_EPOCH : OOB, 0, SC1);
      if constexpr (u == 36 || u == 44) put(mt - 1);
      SB();
    });
    asm volatile("s_nop 7" : "+v"(acc[2][0]), "+v"(acc[2][1]));
    put(2);
  }
  if (!timedout && tid == 192 && (ep.x != (unsigned)tag || ep.y != (unsigned)(tag >> 32))) __hip_atomic_store((GAS u64*)(ws + WS_EPOCH), tag, RLX_AGENT);
  STAMP(10);
  STAMP_OUT((unsigned long long*)(ws + (64u << 20)), blockIdx.x * 8 + w);
  asm volatile(".Lgin_end:");
}

extern "C" void kernel_launch(void* const* d_in, const int* in_sizes, int n_in, void* d_out, int out_size, void* d_ws, size_t ws_size,
                              hipStream_t stream) {
  const int N = in_sizes[0] / D, E = in_sizes[1];
  const int T = (N + TM - 1) / TM;
  (void)hipFuncSetAttribute((const void*)gin_one, hipFuncAttributeMaxDynamicSharedMemorySize, L_END);
  gin_one<<<T, 512, L_END, stream>>>((const unsigned short*)d_in[0], (const int*)d_in[1], (const int*)d_in[2], (const unsigned short*)d_in[3],
                                 (const unsigned short*)d_in[4], (const unsigned short*)d_in[5], (const unsigned short*)d_in[6],
                                 (const unsigned short*)d_in[7], (unsigned short*)d_out, (unsigned char*)d_ws, N, E, T);
}
```

```cpp
#define PH 0

#include <hip/hip_runtime.h>
#include <hip/hip_bf16.h>
#include <stdint.h>
#include <type_traits>

typedef unsigned v4u __attribute__((ext_vector_type(4)));
typedef unsigned v2u __attribute__((ext_vector_type(2)));
typedef float f32x4 __attribute__((ext_vector_type(4)));
typedef float f32x2 __attribute__((ext_vector_type(2)));
typedef short bf16x8 __attribute__((ext_vector_type(8)));
typedef __bf16 bf16x2_t __attribute__((ext_vector_type(2)));
typedef unsigned long long u64;
#define GAS __attribute__((address_space(1)))
#define LAS __attribute__((address_space(3)))
#define DI __device__ __forceinline__
#define RLX_AGENT __ATOMIC_RELAXED, __HIP_MEMORY_SCOPE_AGENT
#define RLX_WG __ATOMIC_RELAXED, __HIP_MEMORY_SCOPE_WORKGROUP
#define SB() __builtin_amdgcn_sched_barrier(0)
#define PRIO(p) __builtin_amdgcn_s_setprio(p)
#define STAMP(i) do { if ((i) == PH) t_ph = __builtin_amdgcn_s_memrealtime(); } while (0)
#define STAMP_DECL
#define STAMP_OUT(base, idx)

constexpr int D = 256, H = 512, TM = 40;
constexpr int EPB = 640;
constexpr int CAP = 1536;
constexpr int HP = 544, H1P = 1056;
constexpr unsigned OOB = 0x40000000u, EOOB = OOB >> 3;
#define RING 4
constexpr int R = RING;
constexpr int SC1 = 16;
constexpr unsigned SPIN_CAP = 4096;
#define FAIR 1
#define XWARM 1
#define XP_EARLY 2
#define GATE 1
#define SPLIT 0
#define CELL_MODE 1
#define PRETOUCH 0
#define ZPAD 0
#define BPRIO 0
#define BSCHED 0
#define XPRIO 0
#define ESLEEP 3
#define PSLEEP 24
#define NB1 8
#define NB2 16
#define NA 16
#define NCN 5
#define OUTAUX 16
#define W2PRE 8
constexpr unsigned WS_EPOCH = 0, WS_WFLAG = 4096, WS_W2T = 65536, WS_CELL = 1u << 20, WS_BYTES = WS_CELL + 256u * 16384u;
constexpr int L_H = 0, L_H1 = L_H + 48 * HP, L_FLAT = L_H1 + 48 * H1P, L_CNT = L_FLAT + (CAP + 128) * 4, L_MISC = L_CNT + 256,
              L_B1 = L_MISC + 32, L_B2 = L_B1 + 2048, L_END = L_B2 + 512;
constexpr int L_PSTG = L_H;
constexpr int L_PCELL = L_H1, L_PCNT = L_H1 + 320 * 64;

DI unsigned pack_bf16(float a, float b) { f32x2 v = {a, b}; bf16x2_t r = __builtin_convertvector(v, bf16x2_t); return __builtin_bit_cast(unsigned, r); }
DI float bflo(unsigned u) { return __builtin_bit_cast(float, u << 16); }
DI float bfhi(unsigned u) { return __builtin_bit_cast(float, u & 0xffff0000u); }
DI float asf(unsigned u) { return __builtin_bit_cast(float, u); }
DI unsigned asu(float u) { return __builtin_bit_cast(unsigned, u); }
DI f32x4 mfma(v4u a, v4u b, f32x4 c) {
  return __builtin_amdgcn_mfma_f32_16x16x32_bf16(__builtin_bit_cast(bf16x8, a), __builtin_bit_cast(bf16x8, b), c, 0, 0, 0);
}
typedef short s16x2 __attribute__((ext_vector_type(2)));
DI unsigned relu_pack(float a, float b) {
  const s16x2 v = __builtin_bit_cast(s16x2, pack_bf16(a, b));
  return __builtin_bit_cast(unsigned, __builtin_elementwise_max(v, s16x2{0, 0}));
}
DI void bar() { asm volatile("s_waitcnt lgkmcnt(0)" ::: "memory"); __builtin_amdgcn_s_barrier(); asm volatile("" ::: "memory"); }
template <int CTRL> DI unsigned dpp_add(unsigned x) { return x + (unsigned)__builtin_amdgcn_update_dpp(0, (int)x, CTRL, 0xF, 0xF, false); }
DI unsigned wave_incl_scan(unsigned x) {
  x = dpp_add<0x111>(x); x = dpp_add<0x112>(x); x = dpp_add<0x114>(x); x = dpp_add<0x118>(x);
  x = x + (unsigned)__builtin_amdgcn_update_dpp(0, (int)x, 0x142, 0xA, 0xF, false);
  x = x + (unsigned)__builtin_amdgcn_update_dpp(0, (int)x, 0x143, 0xC, 0xF, false);
  return x;
}
DI unsigned bsum(unsigned a, unsigned b) { return __builtin_amdgcn_sad_u8(a, 0u, __builtin_amdgcn_sad_u8(b, 0u, 0u)); }
DI __amdgpu_buffer_rsrc_t rsrc(const void* p, unsigned bytes) { return __builtin_amdgcn_make_buffer_rsrc((void*)p, 0, bytes, 0x00020000); }
DI unsigned tag_b(u64 tag) { return ((unsigned)tag + (unsigned)(tag >> 24) * 0x9E3779B1u) & 0x7FFFFu; }

DI void tile_w2_load(__amdgpu_buffer_rsrc_t w2r, int f, int lane, unsigned (&v)[8]) {
  const int ww = f >> 5, ks = (f >> 1) & 15, nt = f & 1, i = lane & 15, kg = lane >> 4;
  const int qo = (2 * ks + (kg >> 1)) & 3;
  const int row0 = 64 * (ks >> 1) + 32 * (qo & 1) + 4 * (qo >> 1) + 2 * (kg & 1);
  const unsigned o = f < 256 ? (row0 * D + 32 * ww + 8 * (i >> 2) + 4 * nt + (i & 3)) * 2 : OOB;
#pragma unroll
  for (int j = 0; j < 8; ++j) v[j] = __builtin_amdgcn_raw_buffer_load_b16(w2r, o + (j >> 2) * D * 2, (j & 3) * 8 * D * 2, 0);
}
DI void tile_w2_store(__amdgpu_buffer_rsrc_t wsr, int f, int lane, const unsigned (&v)[8]) {
  const v4u o = {v[0] | (v[1] << 16), v[2] | (v[3] << 16), v[4] | (v[5] << 16), v[6] | (v[7] << 16)};
  __builtin_amdgcn_raw_buffer_store_b128(o, wsr, f < 256 ? WS_W2T + f * 1024 + lane * 16 : OOB, 0, SC1);
}
constexpr int PB = 96;
template <int G> DI void load_w1(v4u srd, unsigned off) {
  unsigned so;
  constexpr int r = PB + 32 + 32 * G;
  asm volatile("s_mov_b32 %0, %3\n\t"
               "buffer_load_dwordx4 v[%c5:%c5+3], %1, %2, %0 offen\n\tbuffer_load_dwordx4 v[%c5+4:%c5+7], %1, %2, %0 offen offset:1024\n\t"
               "buffer_load_dwordx4 v[%c5+8:%c5+11], %1, %2, %0 offen offset:2048\n\tbuffer_load_dwordx4 v[%c5+12:%c5+15], %1, %2, %0 offen offset:3072\n\t"
               "s_mov_b32 %0, %4\n\t"
               "buffer_load_dwordx4 v[%c5+16:%c5+19], %1, %2, %0 offen\n\tbuffer_load_dwordx4 v[%c5+20:%c5+23], %1, %2, %0 offen offset:1024\n\t"
               "buffer_load_dwordx4 v[%c5+24:%c5+27], %1, %2, %0 offen offset:2048\n\tbuffer_load_dwordx4 v[%c5+28:%c5+31], %1, %2, %0 offen offset:3072"
               : "=&s"(so) : "v"(off), "s"(srd), "i"(G * 65536), "i"(G * 65536 + 4096), "i"(r) : "v255");
}
template <int G> DI void xpose(unsigned lo, unsigned hi) {
#pragma unroll
  for (int d = 0; d < 4; ++d)
#pragma unroll
    for (int p = 0; p < 2; ++p) {
      unsigned t0, t1;
      asm volatile("v_perm_b32 v[%c5], v[%c4+4], v[%c4], %2\n\tv_perm_b32 v[%c5+16], v[%c4+6], v[%c4+2], %2\n\t"
                   "v_perm_b32 v[%c5+4], v[%c4+4], v[%c4], %3\n\tv_perm_b32 v[%c5+20], v[%c4+6], v[%c4+2], %3\n\t"
                   "v_mov_b32 %0, v[%c5]\n\tv_mov_b32 %1, v[%c5+4]\n\t"
                   "v_mov_b32_dpp v[%c5], v[%c5+16] row_ror:8 row_mask:0xf bank_mask:0xc\n\t"
                   "v_mov_b32_dpp v[%c5+4], v[%c5+20] row_ror:8 row_mask:0xf bank_mask:0xc\n\t"
                   "v_mov_b32_dpp v[%c5+16], %0 row_ror:8 row_mask:0xf bank_mask:0x3\n\t"
                   "v_mov_b32_dpp v[%c5+20], %1 row_ror:8 row_mask:0xf bank_mask:0x3"
                   : "=&v"(t0), "=&v"(t1) : "s"(lo), "s"(hi), "i"(PB + 32 + 32 * G + 8 * d + p), "i"(PB + 32 * G + 8 * p + d));
    }
}
DI void xpose_all() { xpose<0>(0x05040100u, 0x07060302u); xpose<1>(0x05040100u, 0x07060302u); xpose<2>(0x05040100u, 0x07060302u); xpose<3>(0x05040100u, 0x07060302u); }
constexpr int w2reg(int i) { return i < 8 ? PB + 128 + 4 * i : PB + 4 * (i - 8); }
template <int I> DI void load_w2(v4u srd, unsigned off) {
  unsigned so;
  asm volatile("s_mov_b32 %0, %3\n\tbuffer_load_dwordx4 v[%c4:%c4+3], %1, %2, %0 offen offset:%c5 sc1"
               : "=&s"(so) : "v"(off), "s"(srd), "i"((I >> 2) * 4096), "i"(w2reg(I)), "i"((I & 3) * 1024));
}
template <int A> DI void mfma_p(f32x4& acc, v4u b) { asm volatile("v_mfma_f32_16x16x32_bf16 %0, v[%c2:%c2+3], %1, %0" : "+v"(acc) : "v"(b), "i"(A)); }
template <int A> DI void mfma_p0(f32x4& acc, v4u b, f32x4 c) {
  asm volatile("v_mfma_f32_16x16x32_bf16 %0, v[%c3:%c3+3], %1, %2" : "=&v"(acc) : "v"(b), "v"(c), "i"(A));
}
template <int N> DI void wait_vm() { asm volatile("s_waitcnt vmcnt(%c0)" :: "i"(N)); }
template <int I, int E, typename F> DI void static_for(F&& f) { if constexpr (I < E) { f(std::integral_constant<int, I>{}); static_for<I + 1, E>(f); } }

__global__ __launch_bounds__(512, 2) __attribute__((amdgpu_num_vgpr(PB / 2))) void gin_one(const unsigned short* __restrict__ x, const int* __restrict__ src, const int* __restrict__ dst,
                                                  const unsigned short* __restrict__ epsp, const unsigned short* __restrict__ w1,
                                                  const unsigned short* __restrict__ b1, const unsigned short* __restrict__ w2,
                                                  const unsigned short* __restrict__ b2, unsigned short* __restrict__ out, unsigned char* ws,
                                                  int N, int E, int T) {
  extern __shared__ __attribute__((aligned(16))) unsigned char Ls[];
  LAS unsigned char* L = (LAS unsigned char*)Ls;
  const int tid = threadIdx.x, lane = tid & 63, w = __builtin_amdgcn_readfirstlane(tid >> 6);
  const int t = blockIdx.x, node0 = t * TM;
  const u64 t_begin = __builtin_amdgcn_s_memrealtime();
  u64 t_ph = t_begin;
  const unsigned bulk = w >> 2, chain = bulk ^ 1;
  unsigned cpf;
  {
    u64 pc; unsigned len;
    asm volatile("s_getpc_b64 %0\n.Lgin_pc:\n\ts_mov_b32 %1, .Lgin_end-.Lgin_pc" : "=s"(pc), "=s"(len));
    cpf = __builtin_amdgcn_raw_buffer_load_b32(rsrc((const void*)pc, len), (((t >> 3) & 31) * 8 + w) * 128 + lane * 32768, 0, 0);
  }
  const __amdgpu_buffer_rsrc_t wsr = rsrc(ws, WS_BYTES), xr = rsrc(x, N * 512), w1r = rsrc(w1, D * H * 2), w2r = rsrc(w2, D * H * 2);

  const int q = lane >> 4, m = lane & 15;
  const v4u w1d = {(unsigned)(u64)w1, (unsigned)((u64)w1 >> 32) & 0xffffu, D * H * 2, 0x00020000u};
  const v4u wsd = {(unsigned)(u64)ws, (unsigned)((u64)ws >> 32) & 0xffffu, WS_BYTES, 0x00020000u};
  const unsigned w1off = (((lane >> 3) & 1) * 32 + q * 8) * 1024 + w * 128 + (lane & 7) * 16;
  LAS unsigned* pcnt = (LAS unsigned*)(L + L_PCNT);
  LAS unsigned* pcell = (LAS unsigned*)(L + L_PCELL);

  if (w != 4) __builtin_amdgcn_s_sleep(ESLEEP);
  const unsigned wu = __builtin_amdgcn_raw_buffer_load_b32(w1r, w == 0 ? ((t >> 3) & 31) * 8192 + lane * 128 : OOB, 0, 0);
  const unsigned pt = 0;
  const int tf = t + (w - 1) * T;
  if (w == 4) {
    PRIO(3);
    const __amdgpu_buffer_rsrc_t dr = rsrc(dst, E * 4), sr = rsrc(src, E * 4);
    v4u dv[3], sv[3];
#pragma unroll
    for (int i = 0; i < 3; ++i) {
      const unsigned o = (i == 2 && lane >= 32) ? OOB : (unsigned)(t * EPB + (lane + 64 * i) * 4) * 4u;
      dv[i] = __builtin_amdgcn_raw_buffer_load_b128(dr, o, 0, 0);
      sv[i] = __builtin_amdgcn_raw_buffer_load_b128(sr, o, 0, 0);
    }
    const u64 ep0 = __hip_atomic_load((GAS u64*)(ws + WS_EPOCH), RLX_AGENT);
    SB();
    *(LAS v4u*)(pcnt + lane * 4) = v4u{0, 0, 0, 0};
    pcnt[256 + lane] = 0;
    const unsigned dummy = 256u + lane;
    unsigned tt[12], rk[12];
#pragma unroll
    for (int i = 0; i < 12; ++i) {
      const int e = t * EPB + (lane + 64 * (i >> 2)) * 4;
      const unsigned lo = (e < E && !(i >= 8 && lane >= 32)) ? 0u : dummy;
      tt[i] = max(min(dv[i >> 2][i & 3] / (unsigned)TM, dummy), lo);
      rk[i] = __hip_atomic_fetch_add(pcnt + tt[i], 1u, RLX_WG);
    }
#pragma unroll
    for (int i = 0; i < 12; ++i)
      pcell[tt[i] * 16 + (rk[i] & 15u)] = (dv[i >> 2][i & 3] - tt[i] * TM) | (min(sv[i >> 2][i & 3], 16383u) << 6);
    *(LAS u64*)(L + L_MISC) = ep0;
  } else {
   if (w == 3) {
    const v4u v1 = __builtin_amdgcn_raw_buffer_load_b128(rsrc(b1, H * 2), lane * 16, 0, 0);
    const v4u v2 = __builtin_amdgcn_raw_buffer_load_b128(rsrc(b2, D * 2), lane * 16, 0, 0);
    const unsigned ev = epsp[0];
    *(LAS unsigned*)(L + L_CNT + lane * 4) = 0;
#pragma unroll
    for (int i = 0; i < 8; ++i)
      *(LAS float*)(L + L_B1 + ((lane >> 3) * 64 + (((lane >> 2) & 1) + 2 * (i >> 2)) * 16 + 4 * (i & 3) + (lane & 3)) * 4) = (i & 1) ? bfhi(v1[i >> 1]) : bflo(v1[i >> 1]);
    *(LAS v4u*)(L + (lane < 32 ? L_B2 + lane * 16 : L_FLAT + lane * 16)) = v2;
    *(LAS v2u*)(L + L_MISC + 8) = v2u{0u, ev};
    *(LAS unsigned*)(L + L_MISC + 16) = 0u;
   } else if (w == 1 || w == 2) {
    unsigned tv[8];
    tile_w2_load(w2r, tf, lane, tv);
    tile_w2_store(wsr, tf, lane, tv);
   }
  }
  if (chain) { load_w1<0>(w1d, w1off); load_w1<1>(w1d, w1off); }
  PRIO(3);
  bar();
  STAMP(1);
  const u64 tag = *(LAS u64*)(L + L_MISC) + 1;
  const unsigned tagA = (unsigned)tag & 0xFFFFFFu, tagB = tag_b(tag);
  {
    v4u e[2]; unsigned c[2];
#pragma unroll
    for (int i = 0; i < 2; ++i) { e[i] = *(LAS v4u*)(pcell + (w * 2 + i) * 256 + lane * 4); c[i] = pcnt[(w * 2 + i) * 16 + (lane >> 2)]; }
#pragma unroll
    for (int i = 0; i < 2; ++i) {
      const unsigned tc = (w * 2 + i) * 16 + (lane >> 2), cc = min(c[i], 31u);
      const v4u g = {(e[i].x & 0xFFFFFu) | (e[i].y << 20), ((e[i].y >> 12) & 0xFFu) | (tagA << 8), (e[i].z & 0xFFFFFu) | (e[i].w << 20),
                     ((e[i].w >> 12) & 0xFFu) | (cc << 8) | (tagB << 13)};
      __builtin_amdgcn_raw_buffer_store_b128(g, wsr, tc < (unsigned)T ? WS_CELL + tc * 16384 + t * 64 + (lane & 3) * 16 : OOB, 0, SC1);
    }
  }
  SB();
  PRIO(1);
  if (chain) { load_w1<2>(w1d, w1off); load_w1<3>(w1d, w1off); } else __builtin_amdgcn_s_sleep(PSLEEP);
  STAMP(2);
  bool any2 = false;
  unsigned wx;
  asm volatile("" : "=v"(wx));
  unsigned ent[16], rk[16], spins = 0;
  LAS unsigned* flat = (LAS unsigned*)(L + L_FLAT);
  if (chain) {
    PRIO(2);
    v4u g[4];
    const unsigned coff = WS_CELL + t * 16384 + w * 4096 + lane * 16;
    constexpr unsigned CSTEP = 1024;
#pragma unroll
    for (int i = 0; i < 4; ++i) g[i] = __builtin_amdgcn_raw_buffer_load_b128(wsr, coff + i * CSTEP, 0, SC1);
    bool tmo = false;
    const int pl = T - 64 * w - (lane >> 2);
#define GR_OK(i) (16 * (i) >= pl || ((g[i].y >> 8) == tagA && (g[i].w >> 13) == tagB))
    if (!__all(GR_OK(0) && GR_OK(1) && GR_OK(2) && GR_OK(3))) {
      for (;;) {
#pragma unroll
        for (int i = 0; i < 4; ++i) {
          const bool ok = GR_OK(i);
          const v4u nv = __builtin_amdgcn_raw_buffer_load_b128(wsr, ok ? OOB : coff + i * CSTEP, 0, SC1);
#pragma unroll
          for (int k = 0; k < 4; ++k) g[i][k] = ok ? g[i][k] : nv[k];
        }
        if (__all(GR_OK(0) && GR_OK(1) && GR_OK(2) && GR_OK(3))) break;
        if (++spins > SPIN_CAP) { tmo = true; break; }
      }
      ++spins;
    }
    STAMP(3);
    if (lane == 0) __hip_atomic_fetch_add((LAS unsigned*)(L + L_MISC + 16), 1u, RLX_WG);
    LAS unsigned char* stg = L + L_PSTG + w * 5120;
#pragma unroll
    for (int i = 0; i < 4; ++i) *(LAS v4u*)(stg + (16 * i + (lane >> 2)) * 80 + (lane & 3) * 16) = g[i];
#pragma unroll
    for (int i = 0; i < 4; ++i) g[i] = *(LAS v4u*)(stg + lane * 80 + i * 16);
    const int nval = (64 * w + lane < T && !tmo) ? (int)((g[0].w >> 8) & 31u) : 0;
    any2 = __any(nval > 8);
    bool bad = nval > 16;
    const unsigned dummy = 40u + (unsigned)(lane % 24);
    auto rank = [&](int lo) __attribute__((always_inline)) {
#pragma unroll
      for (int i = lo; i < lo + 8; ++i) {
        const unsigned a = g[i >> 2][i & 2], b = g[i >> 2][(i & 2) + 1];
        ent[i] = i < nval ? ((i & 1) ? __builtin_amdgcn_alignbit(b, a, 20) : a) & 0xFFFFFu : dummy;
        rk[i] = __hip_atomic_fetch_add((LAS unsigned*)(L + L_CNT + ((ent[i] & 63u) << 2)), 1u << (8 * w), RLX_WG);
      }
#pragma unroll
      for (int i = lo; i < lo + 8; ++i) { rk[i] = (rk[i] >> (8 * w)) & 255u; bad |= rk[i] == 255u; }
    };
    rank(0);
    if (__builtin_expect(any2, 0)) rank(8);
    if ((__any(bad) || tmo) && lane == 0) __hip_atomic_fetch_or((LAS unsigned*)(L + L_MISC + 8), tmo ? 3u : 1u, RLX_WG);
  } else {
    PRIO(0);
    load_w1<0>(w1d, w1off); load_w1<1>(w1d, w1off);
    wait_vm<8>(); xpose<0>(0x05040100u, 0x07060302u);
    wait_vm<0>(); xpose<1>(0x05040100u, 0x07060302u);
    while (__builtin_amdgcn_readfirstlane(__hip_atomic_load((LAS unsigned*)(L + L_MISC + 16), RLX_WG)) < 4u) __builtin_amdgcn_s_sleep(1);
    load_w1<2>(w1d, w1off);
    load_w1<3>(w1d, w1off);
    {
      const unsigned o = XWARM && (w - 4) * 64 + lane < 160 ? node0 * 512 + ((w - 4) * 64 + lane) * 128 : OOB;
      const v4u xd = {(unsigned)(u64)x, (unsigned)((u64)x >> 32) & 0xffffu, (unsigned)N * 512u, 0x00020000u};
      asm volatile("buffer_load_dword %0, %1, %2, 0 offen" : "+v"(wx) : "v"(o), "s"(xd));
    }
  }
  bar();
  STAMP(4);
  bool slow, timedout;
  int s0, nit;
  u64 pk = 0;
  const int n0 = bulk ? (10 - NCN) * w + (8 * NCN - 40) : NCN * w;
  const int nn = bulk ? 10 - NCN : NCN;
  {
    const unsigned cw = *(LAS unsigned*)(L + L_CNT + lane * 4);
    const unsigned flagv = __builtin_amdgcn_readfirstlane(*(LAS unsigned*)(L + L_MISC + 8));
    const unsigned deg = __builtin_amdgcn_sad_u8(cw, 0u, 0u), below = __builtin_amdgcn_sad_u8(cw & ((1u << (8 * (w & 3))) - 1u), 0u, 0u);
    const unsigned l4 = (deg + 4u) & ~3u;
    const bool lastn = lane < 4 * NCN ? (lane % NCN) == NCN - 1 : ((lane - 4 * NCN) % (10 - NCN)) == 9 - NCN;
    const unsigned plen = lane < TM ? l4 + (lastn ? 4u * (R - 1) : 0u) : 0u;
    const unsigned incl = wave_incl_scan(plen), start = incl - plen;
    timedout = (flagv & 2u) != 0u;
    slow = flagv != 0u || __builtin_amdgcn_readlane(incl, 63) > (unsigned)CAP;
    const unsigned tail = __builtin_amdgcn_readlane(incl, n0 + nn - 1) - 4 * (R - 1);
    s0 = __builtin_amdgcn_readlane(start, n0);
    nit = ((tail - s0) / 4 + R - 1) / R;
#pragma unroll
    for (int j = 0; j < 5; ++j) pk |= (u64)(((unsigned)__builtin_amdgcn_readlane(l4, n0 + j) >> 2) & 0xFFFu) << (12 * j);
    if (slow) s0 = 0;
    if (!slow) {
      const unsigned st2 = (unsigned)__builtin_amdgcn_ds_bpermute((n0 + (lane >> 3)) << 2, (int)(start | (deg << 16)));
      if (chain) {
        const unsigned B4 = L_FLAT + 4u * (lane < TM ? start + 1u + below : CAP + 64u);
        auto place = [&](int lo) __attribute__((always_inline)) {
          unsigned b[8];
#pragma unroll
          for (int i = 0; i < 8; ++i) b[i] = (unsigned)__builtin_amdgcn_ds_bpermute((int)((ent[lo + i] & 63u) << 2), (int)B4);
#pragma unroll
          for (int i = 0; i < 8; ++i) *(LAS unsigned*)(L + b[i] + 4u * rk[lo + i]) = ent[lo + i];
        };
        place(0);
        if (__builtin_expect(any2, 0)) place(8);
      }
      const unsigned dg = st2 >> 16, k = lane & 7, pp = k == 7 ? 0u : dg + 1u + k;
      const bool mine = (lane >> 3) < nn && pp < ((dg + 4u) & ~3u);
      const unsigned at = mine ? (st2 & 0xFFFFu) + pp : (lane >= 48 && lane - 48 < 4 * (R - 1) ? tail + lane - 48 : CAP + lane);
      flat[at] = (mine && k == 7) ? (unsigned)(node0 + n0 + (lane >> 3)) << 6 : EOOB;
    }
  }
  if (bulk) {
    wait_vm<9>(); xpose<2>(0x05040100u, 0x07060302u);
  }
  bar();
  STAMP(5);
  PRIO(2);
  if (w == 1 || w == 2) {
    asm volatile("s_waitcnt vmcnt(0)" ::: "memory");
    if (lane == 0 && tf < 256) __hip_atomic_store((GAS unsigned*)(ws + WS_WFLAG) + tf, (unsigned)tag, RLX_AGENT);
  }
  const unsigned l16 = m * 16;
  LAS unsigned* fp = flat + s0 + q;
  v4u bl[R], br[R];
  unsigned en[R];
#pragma unroll
  for (int i = 0; i < R; ++i) en[i] = fp[4 * i];
  const float epsf = bflo(*(LAS unsigned*)(L + L_MISC + 12));
#pragma unroll
  for (int i = 0; i < R; ++i) {
    const unsigned o = ((en[i] << 3) & ~511u) | l16;
    bl[i] = __builtin_amdgcn_raw_buffer_load_b128(xr, o, 0, 0);
    br[i] = __builtin_amdgcn_raw_buffer_load_b128(xr, o + 256, 0, 0);
    en[i] = fp[4 * (R + i)];
  }
  asm volatile("" ::: "memory");
  SB();
  if (chain) { PRIO(XPRIO); xpose<0>(0x05040100u, 0x07060302u); xpose<1>(0x05040100u, 0x07060302u); xpose<2>(0x05040100u, 0x07060302u); }
  wait_vm<2 * R + 1>();
  xpose<3>(0x05040100u, 0x07060302u);
  if (chain) PRIO(0); else PRIO(BPRIO);
  STAMP(6);
  unsigned wu2 = 0;
  v4u fl0;
  if (__builtin_expect(!slow, 1)) {
    const v4u sel = {m == 0 ? 0x3F80u : (m == 1 ? 0x3F800000u : 0u), m == 2 ? 0x3F80u : (m == 3 ? 0x3F800000u : 0u),
                     m == 4 ? 0x3F80u : (m == 5 ? 0x3F800000u : 0u), m == 6 ? 0x3F80u : (m == 7 ? 0x3F800000u : 0u)};
    const v4u self = {sel.x | (lane == 8 ? 0x3F80u : (lane == 9 ? 0x3F800000u : 0u)), sel.y | (lane == 10 ? 0x3F80u : (lane == 11 ? 0x3F800000u : 0u)),
                      sel.z | (lane == 12 ? 0x3F80u : (lane == 13 ? 0x3F800000u : 0u)), sel.w | (lane == 14 ? 0x3F80u : (lane == 15 ? 0x3F800000u : 0u))};
    v4u cur = self;
    fp += 8 * R;
    f32x4 a0 = {0.f, 0.f, 0.f, 0.f}, a1 = {0.f, 0.f, 0.f, 0.f};
    unsigned haddr = L_H + (lane >> 5) * 256 + m * 16 + (q & 1) * 8 + n0 * HP;
    int rem = (int)(pk & 0xFFF);
#define SLOT(i, REFILL) { \
        a0 = mfma(cur, bl[i], a0); \
        a1 = mfma(cur, br[i], a1); \
        cur = sel; \
        if (REFILL) { \
          const unsigned o = ((en[i] << 3) & ~511u) | l16; \
          bl[i] = __builtin_amdgcn_raw_buffer_load_b128(xr, o, 0, 0); \
          br[i] = __builtin_amdgcn_raw_buffer_load_b128(xr, o + 256, 0, 0); \
          en[i] = fp[4 * i]; \
        } \
        if (__builtin_expect(--rem == 0, 0)) { \
          float f[4]; \
          _Pragma("unroll") for (int r = 0; r < 4; ++r) { \
            auto s2 = __builtin_amdgcn_permlane32_swap(asu(a0[r]), asu(a1[r]), false, false); \
            const unsigned u0 = s2[0], u1 = s2[1]; \
            f[r] = fmaf(epsf, asf(u1), asf(u0)); \
          } \
          *(LAS v2u*)(L + haddr) = v2u{pack_bf16(f[0], f[1]), pack_bf16(f[2], f[3])}; \
          haddr += HP; \
          a0 = f32x4{0.f, 0.f, 0.f, 0.f}; a1 = a0; \
          cur = self; \
          pk >>= 12; \
          rem = (int)(pk & 0xFFF); \
        } }
    wu2 = __builtin_amdgcn_raw_buffer_load_b32(wsr, lane < 8 ? WS_W2T + ((t >> 3) & 31) * 8192 + w * 1024 + lane * 128 : OOB, 0, SC1);
    for (; nit > 1; --nit) {
#pragma unroll
      for (int i = 0; i < R; ++i) SLOT(i, true)
      fp += 4 * R;
    }
    fl0 = __builtin_amdgcn_raw_buffer_load_b128(wsr, WS_WFLAG + lane * 16, 0, SC1);
#pragma unroll
    for (int i = 0; i < R; ++i) SLOT(i, false)
  } else {
#pragma unroll 1
    for (int j = 0; j < nn; ++j) {
      const int node = node0 + n0 + j;
      const v2u s2 = __builtin_amdgcn_raw_buffer_load_b64(xr, (unsigned)node * 512u + lane * 8, 0, 0);
      float a0 = bflo(s2.x), a1 = bfhi(s2.x), a2 = bflo(s2.y), a3 = bfhi(s2.y);
      if (node < N)
        for (int eb = 0; eb < E; eb += 64) {
          const int e = eb + lane;
          const int dv2 = (int)__builtin_amdgcn_raw_buffer_load_b32(rsrc(dst, E * 4), e * 4, 0, 0);
          const int sv2 = (int)__builtin_amdgcn_raw_buffer_load_b32(rsrc(src, E * 4), e * 4, 0, 0);
          u64 mkk = __ballot(dv2 == node && e < E);
          while (mkk) {
            const int b = __builtin_ctzll(mkk); mkk &= mkk - 1;
            const unsigned sidx = (unsigned)__builtin_amdgcn_readlane(sv2, b);
            const v2u rv = __builtin_amdgcn_raw_buffer_load_b64(xr, sidx < (unsigned)N ? sidx * 512u + lane * 8 : OOB, 0, 0);
            a0 += bflo(rv.x); a1 += bfhi(rv.x); a2 += bflo(rv.y); a3 += bfhi(rv.y);
          }
        }
      *(LAS v2u*)(L + L_H + (n0 + j) * HP + lane * 8) =
          v2u{pack_bf16(fmaf(epsf, bflo(s2.x), a0), fmaf(epsf, bfhi(s2.x), a1)), pack_bf16(fmaf(epsf, bflo(s2.y), a2), fmaf(epsf, bfhi(s2.y), a3))};
    }
    fl0 = __builtin_amdgcn_raw_buffer_load_b128(wsr, WS_WFLAG + lane * 16, 0, SC1);
  }
  STAMP(7);
  PRIO(0);
  {
    bool okw = false;
    const unsigned tl = (unsigned)tag;
    for (unsigned sp = 0; sp <= SPIN_CAP; ++sp) {
      if (__all(fl0.x == tl && fl0.y == tl && fl0.z == tl && fl0.w == tl)) { okw = true; break; }
      fl0 = __builtin_amdgcn_raw_buffer_load_b128(wsr, WS_WFLAG + lane * 16, 0, SC1);
    }
    if (__builtin_expect(!okw, 0)) {
#pragma unroll 1
      for (int i = 0; i < 32; ++i) { unsigned tv[8]; tile_w2_load(w2r, w * 32 + i, lane, tv); tile_w2_store(wsr, w * 32 + i, lane, tv); }
      asm volatile("s_waitcnt vmcnt(0)" ::: "memory");
    }
    __builtin_amdgcn_fence(__ATOMIC_ACQUIRE, "wavefront");
  }
  if (!timedout && tid == 192 && t == 0) __hip_atomic_store((GAS u64*)(ws + WS_EPOCH), tag, RLX_AGENT);
  asm volatile("" :: "v"(wu2), "v"(wu), "v"(wx), "v"(cpf), "v"(pt));
  const unsigned w2off = WS_W2T + w * 32768 + lane * 16;
  static_for<0, W2PRE>([&](auto I) { load_w2<I>(wsd, w2off); });
  f32x4 bias1[4];
#pragma unroll
  for (int c = 0; c < 4; ++c) bias1[c] = *(LAS f32x4*)(L + L_B1 + (64 * w + 16 * q + 4 * c) * 4);
  SB();
  bar();
  STAMP(8);
  f32x4 bias2[2];
  {
    LAS unsigned char* hb = L + L_H + m * HP + q * 16;
    LAS unsigned char* h1w = L + L_H1 + m * H1P + (64 * w + 16 * q) * 2;
    constexpr int P1 = 5;
    v4u b[P1];
    f32x4 acc[3][4];
    v4u bb2;
    auto epi = [&](int mt, int half) __attribute__((always_inline)) {
      unsigned o[4];
#pragma unroll
      for (int k = 0; k < 4; ++k) o[k] = relu_pack(acc[mt][2 * half + (k >> 1)][2 * (k & 1)], acc[mt][2 * half + (k >> 1)][2 * (k & 1) + 1]);
      *(LAS v4u*)(h1w + mt * 16 * H1P + 16 * half) = v4u{o[0], o[1], o[2], o[3]};
    };
#define U1_KS(u) ((u) < 12 ? (u) / 3 : 4 + ((u) - 12) % 4)
#define U1_MT(u) ((u) < 12 ? (u) % 3 : ((u) - 12) / 4)
#pragma unroll
    for (int u = 0; u < P1; ++u) b[u] = *(LAS v4u*)(hb + U1_MT(u) * 16 * HP + U1_KS(u) * 64);
    static_for<0, 24>([&](auto U) {
      constexpr int u = U, ks = U1_KS(u), mt = U1_MT(u);
      if constexpr (u % 6 == 0) PRIO(3 - u / 6);
      static_for<0, 4>([&](auto C) {
        if constexpr (ks == 0) mfma_p0<PB + 4 * C>(acc[mt][C], b[u % P1], bias1[C]);
        else mfma_p<PB + 16 * ks + 4 * C>(acc[mt][C], b[u % P1]);
      });
      if constexpr (u + P1 < 24) b[u % P1] = *(LAS v4u*)(hb + U1_MT(u + P1) * 16 * HP + U1_KS(u + P1) * 64);
      if constexpr (u >= 2 && u < 18) load_w2<6 + u>(wsd, w2off);
      if constexpr (u == 20) bb2 = *(LAS v4u*)(L + L_B2 + (32 * w + 8 * q) * 2);
      if constexpr (u == 17 || u == 21) epi(mt - 1, 0);
      if constexpr (u == 18 || u == 22) epi(mt - 1, 1);
      SB();
    });
    asm volatile("s_nop 7" : "+v"(acc[2][0]), "+v"(acc[2][1]), "+v"(acc[2][2]), "+v"(acc[2][3]));
    epi(2, 0); epi(2, 1);
#pragma unroll
    for (int nt = 0; nt < 2; ++nt) {
      bias2[nt] = f32x4{bflo(bb2[2 * nt]), bfhi(bb2[2 * nt]), bflo(bb2[2 * nt + 1]), bfhi(bb2[2 * nt + 1])};
      asm volatile("" : "+v"(bias2[nt]));
    }
  }
  SB();
  STAMP(9);
  bar();
  v2u ep;
  {
    f32x4 acc[3][2];
    LAS unsigned char* hb = L + L_H1 + m * H1P + q * 16;
    const __amdgpu_buffer_rsrc_t outr = rsrc(out, N * 512);
    auto put = [&](int mt) __attribute__((always_inline)) {
      const int r = 16 * mt + m;
      v4u o;
      o.x = pack_bf16(acc[mt][0][0], acc[mt][0][1]); o.y = pack_bf16(acc[mt][0][2], acc[mt][0][3]);
      o.z = pack_bf16(acc[mt][1][0], acc[mt][1][1]); o.w = pack_bf16(acc[mt][1][2], acc[mt][1][3]);
      __builtin_amdgcn_raw_buffer_store_b128(o, outr, r < TM ? (unsigned)(node0 + r) * 512u + 64 * w + 16 * q : OOB, 0, OUTAUX);
    };
    constexpr int P2 = 8;
    v4u b[P2];
#define U2_KS(u) ((u) < 24 ? (u) / 3 : 8 + ((u) - 24) % 8)
#define U2_MT(u) ((u) < 24 ? (u) % 3 : ((u) - 24) / 8)
#pragma unroll
    for (int u = 0; u < P2; ++u) b[u] = *(LAS v4u*)(hb + U2_MT(u) * 16 * H1P + U2_KS(u) * 64);
    static_for<0, 48>([&](auto U) {
      constexpr int u = U, ks = U2_KS(u), mt = U2_MT(u);
      if constexpr (u % 12 == 0) PRIO(3 - u / 12);
      if constexpr (u < 16 && (u & 1) == 0) load_w2<24 + u / 2>(wsd, w2off);
      constexpr int issued = u < 16 ? 25 + u / 2 : 32;
      if constexpr (mt == 0) wait_vm<issued - (2 * ks + 2)>();
      if constexpr (ks == 0) { mfma_p0<w2reg(0)>(acc[mt][0], b[u % P2], bias2[0]); mfma_p0<w2reg(1)>(acc[mt][1], b[u % P2], bias2[1]); }
      else { mfma_p<w2reg(2 * ks)>(acc[mt][0], b[u % P2]); mfma_p<w2reg(2 * ks + 1)>(acc[mt][1], b[u % P2]); }
      if constexpr (u + P2 < 48) b[u % P2] = *(LAS v4u*)(hb + U2_MT(u + P2) * 16 * H1P + U2_KS(u + P2) * 64);
      if constexpr (u == 32) ep = __builtin_amdgcn_raw_buffer_load_b64(wsr, tid == 192 ? WS_EPOCH : OOB, 0, SC1);
      if constexpr (u == 36 || u == 44) put(mt - 1);
      SB();
    });
    asm volatile("s_nop 7" : "+v"(acc[2][0]), "+v"(acc[2][1]));
    put(2);
  }
  if (!timedout && tid == 192 && (ep.x != (unsigned)tag || ep.y != (unsigned)(tag >> 32))) __hip_atomic_store((GAS u64*)(ws + WS_EPOCH), tag, RLX_AGENT);
  STAMP(10);
  STAMP_OUT((unsigned long long*)(ws + (64u << 20)), blockIdx.x * 8 + w);
  if (t == 0 && w == 0) while (__builtin_amdgcn_s_memrealtime() - t_begin < 3000 + (t_ph - t_begin)) __builtin_amdgcn_s_sleep(1);
  asm volatile(".Lgin_end:");
}

extern "C" void kernel_launch(void* const* d_in, const int* in_sizes, int n_in, void* d_out, int out_size, void* d_ws, size_t ws_size,
                              hipStream_t stream) {
  const int N = in_sizes[0] / D, E = in_sizes[1];
  const int T = (N + TM - 1) / TM;
  (void)hipFuncSetAttribute((const void*)gin_one, hipFuncAttributeMaxDynamicSharedMemorySize, L_END);
  gin_one<<<T, 512, L_END, stream>>>((const unsigned short*)d_in[0], (const int*)d_in[1], (const int*)d_in[2], (const unsigned short*)d_in[3],
                                 (const unsigned short*)d_in[4], (const unsigned short*)d_in[5], (const unsigned short*)d_in[6],
                                 (const unsigned short*)d_in[7], (unsigned short*)d_out, (unsigned char*)d_ws, N, E, T);
}
```

```cpp
#include <hip/hip_runtime.h>
#include <hip/hip_bf16.h>
#include <stdint.h>
#include <type_traits>

typedef unsigned v4u __attribute__((ext_vector_type(4)));
typedef unsigned v2u __attribute__((ext_vector_type(2)));
typedef float f32x4 __attribute__((ext_vector_type(4)));
typedef float f32x2 __attribute__((ext_vector_type(2)));
typedef short bf16x8 __attribute__((ext_vector_type(8)));
typedef __bf16 bf16x2_t __attribute__((ext_vector_type(2)));
typedef unsigned long long u64;
#define GAS __attribute__((address_space(1)))
#define LAS __attribute__((address_space(3)))
#define DI __device__ __forceinline__
#define RLX_AGENT __ATOMIC_RELAXED, __HIP_MEMORY_SCOPE_AGENT
#define RLX_WG __ATOMIC_RELAXED, __HIP_MEMORY_SCOPE_WORKGROUP
#define SB() __builtin_amdgcn_sched_barrier(0)
#define PRIO(p) __builtin_amdgcn_s_setprio(p)
#define STAMP(i)
#define STAMP_DECL
#define STAMP_OUT(base, idx)

constexpr int D = 256, H = 512, TM = 40;
constexpr int EPB = 640;
constexpr int CAP = 1536;
constexpr int HP = 544, H1P = 1056;
constexpr unsigned OOB = 0x40000000u, EOOB = OOB >> 3;
#define RING 4
constexpr int R = RING;
constexpr int SC1 = 16;
constexpr unsigned SPIN_CAP = 4096;
#define FAIR 1
#define XWARM 1
#define XP_EARLY 2
#define GATE 0
#define SPLIT 0
#define CELL_MODE 1
#define PRETOUCH 0
#define ZPAD 0
#define BPRIO 0
#define BSCHED 0
#define XPRIO 0
#define ESLEEP 3
#define PSLEEP 24
#define NB1 8
#define NB2 16
#define NA 16
#define NCN 5
#define OUTAUX 16
#define W2PRE 8
constexpr unsigned WS_EPOCH = 0, WS_WFLAG = 4096, WS_W2T = 65536, WS_CELL = 1u << 20, WS_BYTES = WS_CELL + 256u * 16384u;
constexpr int L_H = 0, L_H1 = L_H + 48 * HP, L_FLAT = L_H1 + 48 * H1P, L_CNT = L_FLAT + (CAP + 128) * 4, L_MISC = L_CNT + 256,
              L_B1 = L_MISC + 32, L_B2 = L_B1 + 2048, L_END = L_B2 + 512;
constexpr int L_PSTG = L_H;
constexpr int L_PCELL = L_H1, L_PCNT = L_H1 + 320 * 64;

DI unsigned pack_bf16(float a, float b) { f32x2 v = {a, b}; bf16x2_t r = __builtin_convertvector(v, bf16x2_t); return __builtin_bit_cast(unsigned, r); }
DI float bflo(unsigned u) { return __builtin_bit_cast(float, u << 16); }
DI float bfhi(unsigned u) { return __builtin_bit_cast(float, u & 0xffff0000u); }
DI float asf(unsigned u) { return __builtin_bit_cast(float, u); }
DI unsigned asu(float u) { return __builtin_bit_cast(unsigned, u); }
DI f32x4 mfma(v4u a, v4u b, f32x4 c) {
  return __builtin_amdgcn_mfma_f32_16x16x32_bf16(__builtin_bit_cast(bf16x8, a), __builtin_bit_cast(bf16x8, b), c, 0, 0, 0);
}
typedef short s16x2 __attribute__((ext_vector_type(2)));
DI unsigned relu_pack(float a, float b) {
  const s16x2 v = __builtin_bit_cast(s16x2, pack_bf16(a, b));
  return __builtin_bit_cast(unsigned, __builtin_elementwise_max(v, s16x2{0, 0}));
}
DI void bar() { asm volatile("s_waitcnt lgkmcnt(0)" ::: "memory"); __builtin_amdgcn_s_barrier(); asm volatile("" ::: "memory"); }
template <int CTRL> DI unsigned dpp_add(unsigned x) { return x + (unsigned)__builtin_amdgcn_update_dpp(0, (int)x, CTRL, 0xF, 0xF, false); }
DI unsigned wave_incl_scan(unsigned x) {
  x = dpp_add<0x111>(x); x = dpp_add<0x112>(x); x = dpp_add<0x114>(x); x = dpp_add<0x118>(x);
  x = x + (unsigned)__builtin_amdgcn_update_dpp(0, (int)x, 0x142, 0xA, 0xF, false);
  x = x + (unsigned)__builtin_amdgcn_update_dpp(0, (int)x, 0x143, 0xC, 0xF, false);
  return x;
}
DI unsigned bsum(unsigned a, unsigned b) { return __builtin_amdgcn_sad_u8(a, 0u, __builtin_amdgcn_sad_u8(b, 0u, 0u)); }
DI __amdgpu_buffer_rsrc_t rsrc(const void* p, unsigned bytes) { return __builtin_amdgcn_make_buffer_rsrc((void*)p, 0, bytes, 0x00020000); }
DI unsigned tag_b(u64 tag) { return ((unsigned)tag + (unsigned)(tag >> 24) * 0x9E3779B1u) & 0x7FFFFu; }

DI void tile_w2_load(__amdgpu_buffer_rsrc_t w2r, int f, int lane, unsigned (&v)[8]) {
  const int ww = f >> 5, ks = (f >> 1) & 15, nt = f & 1, i = lane & 15, kg = lane >> 4;
  const int qo = (2 * ks + (kg >> 1)) & 3;
  const int row0 = 64 * (ks >> 1) + 32 * (qo & 1) + 4 * (qo >> 1) + 2 * (kg & 1);
  const unsigned o = f < 256 ? (row0 * D + 32 * ww + 8 * (i >> 2) + 4 * nt + (i & 3)) * 2 : OOB;
#pragma unroll
  for (int j = 0; j < 8; ++j) v[j] = __builtin_amdgcn_raw_buffer_load_b16(w2r, o + (j >> 2) * D * 2, (j & 3) * 8 * D * 2, 0);
}
DI void tile_w2_store(__amdgpu_buffer_rsrc_t wsr, int f, int lane, const unsigned (&v)[8]) {
  const v4u o = {v[0] | (v[1] << 16), v[2] | (v[3] << 16), v[4] | (v[5] << 16), v[6] | (v[7] << 16)};
  __builtin_amdgcn_raw_buffer_store_b128(o, wsr, f < 256 ? WS_W2T + f * 1024 + lane * 16 : OOB, 0, SC1);
}
constexpr int PB = 96;
template <int G> DI void load_w1(v4u srd, unsigned off) {
  unsigned so;
  constexpr int r = PB + 32 + 32 * G;
  asm volatile("s_mov_b32 %0, %3\n\t"
               "buffer_load_dwordx4 v[%c5:%c5+3], %1, %2, %0 offen\n\tbuffer_load_dwordx4 v[%c5+4:%c5+7], %1, %2, %0 offen offset:1024\n\t"
               "buffer_load_dwordx4 v[%c5+8:%c5+11], %1, %2, %0 offen offset:2048\n\tbuffer_load_dwordx4 v[%c5+12:%c5+15], %1, %2, %0 offen offset:3072\n\t"
               "s_mov_b32 %0, %4\n\t"
               "buffer_load_dwordx4 v[%c5+16:%c5+19], %1, %2, %0 offen\n\tbuffer_load_dwordx4 v[%c5+20:%c5+23], %1, %2, %0 offen offset:1024\n\t"
               "buffer_load_dwordx4 v[%c5+24:%c5+27], %1, %2, %0 offen offset:2048\n\tbuffer_load_dwordx4 v[%c5+28:%c5+31], %1, %2, %0 offen offset:3072"
               : "=&s"(so) : "v"(off), "s"(srd), "i"(G * 65536), "i"(G * 65536 + 4096), "i"(r) : "v255");
}
template <int G> DI void xpose(unsigned lo, unsigned hi) {
#pragma unroll
  for (int d = 0; d < 4; ++d)
#pragma unroll
    for (int p = 0; p < 2; ++p) {
      unsigned t0, t1;
      asm volatile("v_perm_b32 v[%c5], v[%c4+4], v[%c4], %2\n\tv_perm_b32 v[%c5+16], v[%c4+6], v[%c4+2], %2\n\t"
                   "v_perm_b32 v[%c5+4], v[%c4+4], v[%c4], %3\n\tv_perm_b32 v[%c5+20], v[%c4+6], v[%c4+2], %3\n\t"
                   "v_mov_b32 %0, v[%c5]\n\tv_mov_b32 %1, v[%c5+4]\n\t"
                   "v_mov_b32_dpp v[%c5], v[%c5+16] row_ror:8 row_mask:0xf bank_mask:0xc\n\t"
                   "v_mov_b32_dpp v[%c5+4], v[%c5+20] row_ror:8 row_mask:0xf bank_mask:0xc\n\t"
                   "v_mov_b32_dpp v[%c5+16], %0 row_ror:8 row_mask:0xf bank_mask:0x3\n\t"
                   "v_mov_b32_dpp v[%c5+20], %1 row_ror:8 row_mask:0xf bank_mask:0x3"
                   : "=&v"(t0), "=&v"(t1) : "s"(lo), "s"(hi), "i"(PB + 32 + 32 * G + 8 * d + p), "i"(PB + 32 * G + 8 * p + d));
    }
}
DI void xpose_all() { xpose<0>(0x05040100u, 0x07060302u); xpose<1>(0x05040100u, 0x07060302u); xpose<2>(0x05040100u, 0x07060302u); xpose<3>(0x05040100u, 0x07060302u); }
constexpr int w2reg(int i) { return i < 8 ? PB + 128 + 4 * i : PB + 4 * (i - 8); }
template <int I> DI void load_w2(v4u srd, unsigned off) {
  unsigned so;
  asm volatile("s_mov_b32 %0, %3\n\tbuffer_load_dwordx4 v[%c4:%c4+3], %1, %2, %0 offen offset:%c5 sc1"
               : "=&s"(so) : "v"(off), "s"(srd), "i"((I >> 2) * 4096), "i"(w2reg(I)), "i"((I & 3) * 1024));
}
template <int A> DI void mfma_p(f32x4& acc, v4u b) { asm volatile("v_mfma_f32_16x16x32_bf16 %0, v[%c2:%c2+3], %1, %0" : "+v"(acc) : "v"(b), "i"(A)); }
template <int A> DI void mfma_p0(f32x4& acc, v4u b, f32x4 c) {
  asm volatile("v_mfma_f32_16x16x32_bf16 %0, v[%c3:%c3+3], %1, %2" : "=&v"(acc) : "v"(b), "v"(c), "i"(A));
}
template <int AUX> DI void touch(v4u srd, unsigned off) {
  if constexpr (AUX) asm volatile("buffer_load_dword v[%c2], %0, %1, 0 offen sc1" :: "v"(off), "s"(srd), "i"(PB - 1));
  else asm volatile("buffer_load_dword v[%c2], %0, %1, 0 offen" :: "v"(off), "s"(srd), "i"(PB - 1));
}
template <int N> DI void wait_vm() { asm volatile("s_waitcnt vmcnt(%c0)" :: "i"(N)); }
template <int I, int E, typename F> DI void static_for(F&& f) { if constexpr (I < E) { f(std::integral_constant<int, I>{}); static_for<I + 1, E>(f); } }

__global__ __launch_bounds__(512, 2) __attribute__((amdgpu_num_vgpr(PB / 2 - 1))) void gin_one(const unsigned short* __restrict__ x, const int* __restrict__ src, const int* __restrict__ dst,
                                                  const unsigned short* __restrict__ epsp, const unsigned short* __restrict__ w1,
                                                  const unsigned short* __restrict__ b1, const unsigned short* __restrict__ w2,
                                                  const unsigned short* __restrict__ b2, unsigned short* __restrict__ out, unsigned char* ws,
                                                  int N, int E, int T) {
  extern __shared__ __attribute__((aligned(16))) unsigned char Ls[];
  LAS unsigned char* L = (LAS unsigned char*)Ls;
  const int tid = threadIdx.x, lane = tid & 63, w = __builtin_amdgcn_readfirstlane(tid >> 6);
  const int t = blockIdx.x, node0 = t * TM;
  const unsigned bulk = w >> 2, chain = bulk ^ 1;
  const int rank = (t >> 3) & 31, share2 = (T >> 3) + rank;
  if (w == 7) {
    u64 pc; unsigned len;
    asm volatile("s_getpc_b64 %0\n.Lgin_pc:\n\ts_mov_b32 %1, .Lgin_end-.Lgin_pc" : "=s"(pc), "=s"(len));
    touch<0>(v4u{(unsigned)pc, (unsigned)(pc >> 32) & 0xffffu, len, 0x00020000u}, lane < 6 ? (rank * 6 + lane) * 128 : OOB);
  }
  const __amdgpu_buffer_rsrc_t wsr = rsrc(ws, WS_BYTES), xr = rsrc(x, N * 512), w1r = rsrc(w1, D * H * 2), w2r = rsrc(w2, D * H * 2);
  STAMP_DECL STAMP(0);
  const int q = lane >> 4, m = lane & 15;
  const v4u w1d = {(unsigned)(u64)w1, (unsigned)((u64)w1 >> 32) & 0xffffu, D * H * 2, 0x00020000u};
  const v4u wsd = {(unsigned)(u64)ws, (unsigned)((u64)ws >> 32) & 0xffffu, WS_BYTES, 0x00020000u};
  const unsigned w1off = (((lane >> 3) & 1) * 32 + q * 8) * 1024 + w * 128 + (lane & 7) * 16;
  LAS unsigned* pcnt = (LAS unsigned*)(L + L_PCNT);
  LAS unsigned* pcell = (LAS unsigned*)(L + L_PCELL);

  if (w != 4) __builtin_amdgcn_s_sleep(ESLEEP);
  if (w == 0 || (w == 5 && share2 < 32)) touch<0>(w1d, (w ? share2 : rank) * 8192 + lane * 128);
  const int tf = t + (w - 1) * T;
  if (w == 4) {
    PRIO(3);
    const __amdgpu_buffer_rsrc_t dr = rsrc(dst, E * 4), sr = rsrc(src, E * 4);
    v4u dv[3], sv[3];
#pragma unroll
    for (int i = 0; i < 3; ++i) {
      const unsigned o = (i == 2 && lane >= 32) ? OOB : (unsigned)(t * EPB + (lane + 64 * i) * 4) * 4u;
      dv[i] = __builtin_amdgcn_raw_buffer_load_b128(dr, o, 0, 0);
      sv[i] = __builtin_amdgcn_raw_buffer_load_b128(sr, o, 0, 0);
    }
    const u64 ep0 = __hip_atomic_load((GAS u64*)(ws + WS_EPOCH), RLX_AGENT);
    SB();
    *(LAS v4u*)(pcnt + lane * 4) = v4u{0, 0, 0, 0};
    pcnt[256 + lane] = 0;
    const unsigned dummy = 256u + lane;
    unsigned tt[12], rk[12];
#pragma unroll
    for (int i = 0; i < 12; ++i) {
      const int e = t * EPB + (lane + 64 * (i >> 2)) * 4;
      const unsigned lo = (e < E && !(i >= 8 && lane >= 32)) ? 0u : dummy;
      tt[i] = max(min(dv[i >> 2][i & 3] / (unsigned)TM, dummy), lo);
      rk[i] = __hip_atomic_fetch_add(pcnt + tt[i], 1u, RLX_WG);
    }
#pragma unroll
    for (int i = 0; i < 12; ++i)
      pcell[tt[i] * 16 + (rk[i] & 15u)] = (dv[i >> 2][i & 3] - tt[i] * TM) | (min(sv[i >> 2][i & 3], 16383u) << 6);
    *(LAS u64*)(L + L_MISC) = ep0;
  } else {
   if (w == 3) {
    const v4u v1 = __builtin_amdgcn_raw_buffer_load_b128(rsrc(b1, H * 2), lane * 16, 0, 0);
    const v4u v2 = __builtin_amdgcn_raw_buffer_load_b128(rsrc(b2, D * 2), lane * 16, 0, 0);
    const unsigned ev = epsp[0];
    *(LAS unsigned*)(L + L_CNT + lane * 4) = 0;
#pragma unroll
    for (int i = 0; i < 8; ++i)
      *(LAS float*)(L + L_B1 + ((lane >> 3) * 64 + (((lane >> 2) & 1) + 2 * (i >> 2)) * 16 + 4 * (i & 3) + (lane & 3)) * 4) = (i & 1) ? bfhi(v1[i >> 1]) : bflo(v1[i >> 1]);
    *(LAS v4u*)(L + (lane < 32 ? L_B2 + lane * 16 : L_FLAT + lane * 16)) = v2;
    *(LAS v2u*)(L + L_MISC + 8) = v2u{0u, ev};
    *(LAS unsigned*)(L + L_MISC + 16) = 0u;
   } else if (w == 1 || w == 2) {
    unsigned tv[8];
    tile_w2_load(w2r, tf, lane, tv);
    tile_w2_store(wsr, tf, lane, tv);
   }
  }
  if (chain) { load_w1<0>(w1d, w1off); load_w1<1>(w1d, w1off); }
  PRIO(3);
  bar();
  STAMP(1);
  const u64 tag = *(LAS u64*)(L + L_MISC) + 1;
  const unsigned tagA = (unsigned)tag & 0xFFFFFFu, tagB = tag_b(tag);
  {
    v4u e[2]; unsigned c[2];
#pragma unroll
    for (int i = 0; i < 2; ++i) { e[i] = *(LAS v4u*)(pcell + (w * 2 + i) * 256 + lane * 4); c[i] = pcnt[(w * 2 + i) * 16 + (lane >> 2)]; }
#pragma unroll
    for (int i = 0; i < 2; ++i) {
      const unsigned tc = (w * 2 + i) * 16 + (lane >> 2), cc = min(c[i], 31u);
      const v4u g = {(e[i].x & 0xFFFFFu) | (e[i].y << 20), ((e[i].y >> 12) & 0xFFu) | (tagA << 8), (e[i].z & 0xFFFFFu) | (e[i].w << 20),
                     ((e[i].w >> 12) & 0xFFu) | (cc << 8) | (tagB << 13)};
      __builtin_amdgcn_raw_buffer_store_b128(g, wsr, tc < (unsigned)T ? WS_CELL + tc * 16384 + t * 64 + (lane & 3) * 16 : OOB, 0, SC1);
    }
  }
  SB();
  PRIO(1);
  if (chain) { load_w1<2>(w1d, w1off); load_w1<3>(w1d, w1off); } else __builtin_amdgcn_s_sleep(PSLEEP);
  STAMP(2);
  bool any2 = false;
  unsigned ent[16], rk[16], spins = 0;
  LAS unsigned* flat = (LAS unsigned*)(L + L_FLAT);
  if (chain) {
    PRIO(2);
    v4u g[4];
    const unsigned coff = WS_CELL + t * 16384 + w * 4096 + lane * 16;
    constexpr unsigned CSTEP = 1024;
#pragma unroll
    for (int i = 0; i < 4; ++i) g[i] = __builtin_amdgcn_raw_buffer_load_b128(wsr, coff + i * CSTEP, 0, SC1);
    bool tmo = false;
    const int pl = T - 64 * w - (lane >> 2);
#define GR_OK(i) (16 * (i) >= pl || ((g[i].y >> 8) == tagA && (g[i].w >> 13) == tagB))
    if (!__all(GR_OK(0) && GR_OK(1) && GR_OK(2) && GR_OK(3))) {
      for (;;) {
#pragma unroll
        for (int i = 0; i < 4; ++i) {
          const bool ok = GR_OK(i);
          const v4u nv = __builtin_amdgcn_raw_buffer_load_b128(wsr, ok ? OOB : coff + i * CSTEP, 0, SC1);
#pragma unroll
          for (int k = 0; k < 4; ++k) g[i][k] = ok ? g[i][k] : nv[k];
        }
        if (__all(GR_OK(0) && GR_OK(1) && GR_OK(2) && GR_OK(3))) break;
        if (++spins > SPIN_CAP) { tmo = true; break; }
      }
      ++spins;
    }
    STAMP(3);
    if (lane == 0) __hip_atomic_fetch_add((LAS unsigned*)(L + L_MISC + 16), 1u, RLX_WG);
    LAS unsigned char* stg = L + L_PSTG + w * 5120;
#pragma unroll
    for (int i = 0; i < 4; ++i) *(LAS v4u*)(stg + (16 * i + (lane >> 2)) * 80 + (lane & 3) * 16) = g[i];
#pragma unroll
    for (int i = 0; i < 4; ++i) g[i] = *(LAS v4u*)(stg + lane * 80 + i * 16);
    const int nval = (64 * w + lane < T && !tmo) ? (int)((g[0].w >> 8) & 31u) : 0;
    any2 = __any(nval > 8);
    bool bad = nval > 16;
    const unsigned dummy = 40u + (unsigned)(lane % 24);
    auto rank = [&](int lo) __attribute__((always_inline)) {
#pragma unroll
      for (int i = lo; i < lo + 8; ++i) {
        const unsigned a = g[i >> 2][i & 2], b = g[i >> 2][(i & 2) + 1];
        ent[i] = i < nval ? ((i & 1) ? __builtin_amdgcn_alignbit(b, a, 20) : a) & 0xFFFFFu : dummy;
        rk[i] = __hip_atomic_fetch_add((LAS unsigned*)(L + L_CNT + ((ent[i] & 63u) << 2)), 1u << (8 * w), RLX_WG);
      }
#pragma unroll
      for (int i = lo; i < lo + 8; ++i) { rk[i] = (rk[i] >> (8 * w)) & 255u; bad |= rk[i] == 255u; }
    };
    rank(0);
    if (__builtin_expect(any2, 0)) rank(8);
    if ((__any(bad) || tmo) && lane == 0) __hip_atomic_fetch_or((LAS unsigned*)(L + L_MISC + 8), tmo ? 3u : 1u, RLX_WG);
  } else {
    PRIO(0);
    load_w1<0>(w1d, w1off); load_w1<1>(w1d, w1off);
    wait_vm<8>(); xpose<0>(0x05040100u, 0x07060302u);
    wait_vm<0>(); xpose<1>(0x05040100u, 0x07060302u);
    load_w1<2>(w1d, w1off);
    load_w1<3>(w1d, w1off);
    if (w < 7) touch<0>(v4u{(unsigned)(u64)x, (unsigned)((u64)x >> 32) & 0xffffu, (unsigned)N * 512u, 0x00020000u},
                        (w - 4) * 64 + lane < 160 ? node0 * 512 + ((w - 4) * 64 + lane) * 128 : OOB);
    else touch<0>(w1d, OOB);
  }
  bar();
  STAMP(4);
  bool slow, timedout;
  int s0, nit;
  u64 pk = 0;
  const int n0 = bulk ? (10 - NCN) * w + (8 * NCN - 40) : NCN * w;
  const int nn = bulk ? 10 - NCN : NCN;
  {
    const unsigned cw = *(LAS unsigned*)(L + L_CNT + lane * 4);
    const unsigned flagv = __builtin_amdgcn_readfirstlane(*(LAS unsigned*)(L + L_MISC + 8));
    const unsigned deg = __builtin_amdgcn_sad_u8(cw, 0u, 0u), below = __builtin_amdgcn_sad_u8(cw & ((1u << (8 * (w & 3))) - 1u), 0u, 0u);
    const unsigned l4 = (deg + 4u) & ~3u;
    const bool lastn = lane < 4 * NCN ? (lane % NCN) == NCN - 1 : ((lane - 4 * NCN) % (10 - NCN)) == 9 - NCN;
    const unsigned plen = lane < TM ? l4 + (lastn ? 4u * (R - 1) : 0u) : 0u;
    const unsigned incl = wave_incl_scan(plen), start = incl - plen;
    timedout = (flagv & 2u) != 0u;
    slow = flagv != 0u || __builtin_amdgcn_readlane(incl, 63) > (unsigned)CAP;
    const unsigned tail = __builtin_amdgcn_readlane(incl, n0 + nn - 1) - 4 * (R - 1);
    s0 = __builtin_amdgcn_readlane(start, n0);
    nit = ((tail - s0) / 4 + R - 1) / R;
#pragma unroll
    for (int j = 0; j < 5; ++j) pk |= (u64)(((unsigned)__builtin_amdgcn_readlane(l4, n0 + j) >> 2) & 0xFFFu) << (12 * j);
    if (slow) s0 = 0;
    if (!slow) {
      const unsigned st2 = (unsigned)__builtin_amdgcn_ds_bpermute((n0 + (lane >> 3)) << 2, (int)(start | (deg << 16)));
      if (chain) {
        const unsigned B4 = L_FLAT + 4u * (lane < TM ? start + 1u + below : CAP + 64u);
        auto place = [&](int lo) __attribute__((always_inline)) {
          unsigned b[8];
#pragma unroll
          for (int i = 0; i < 8; ++i) b[i] = (unsigned)__builtin_amdgcn_ds_bpermute((int)((ent[lo + i] & 63u) << 2), (int)B4);
#pragma unroll
          for (int i = 0; i < 8; ++i) *(LAS unsigned*)(L + b[i] + 4u * rk[lo + i]) = ent[lo + i];
        };
        place(0);
        if (__builtin_expect(any2, 0)) place(8);
      }
      const unsigned dg = st2 >> 16, k = lane & 7, pp = k == 7 ? 0u : dg + 1u + k;
      const bool mine = (lane >> 3) < nn && pp < ((dg + 4u) & ~3u);
      const unsigned at = mine ? (st2 & 0xFFFFu) + pp : (lane >= 48 && lane - 48 < 4 * (R - 1) ? tail + lane - 48 : CAP + lane);
      flat[at] = (mine && k == 7) ? (unsigned)(node0 + n0 + (lane >> 3)) << 6 : EOOB;
    }
  }
  if (bulk) {
    wait_vm<9>(); xpose<2>(0x05040100u, 0x07060302u);
  }
  bar();
  STAMP(5);
  PRIO(2);
  if (w == 1 || w == 2) {
    asm volatile("s_waitcnt vmcnt(0)" ::: "memory");
    if (lane == 0 && tf < 256) __hip_atomic_store((GAS unsigned*)(ws + WS_WFLAG) + tf, (unsigned)tag, RLX_AGENT);
  }
  const unsigned l16 = m * 16;
  LAS unsigned* fp = flat + s0 + q;
  v4u bl[R], br[R];
  unsigned en[R];
#pragma unroll
  for (int i = 0; i < R; ++i) en[i] = fp[4 * i];
  const float epsf = bflo(*(LAS unsigned*)(L + L_MISC + 12));
#pragma unroll
  for (int i = 0; i < R; ++i) {
    const unsigned o = ((en[i] << 3) & ~511u) | l16;
    bl[i] = __builtin_amdgcn_raw_buffer_load_b128(xr, o, 0, 0);
    br[i] = __builtin_amdgcn_raw_buffer_load_b128(xr, o + 256, 0, 0);
    en[i] = fp[4 * (R + i)];
  }
  asm volatile("" ::: "memory");
  SB();
  if (chain) { PRIO(XPRIO); xpose<0>(0x05040100u, 0x07060302u); xpose<1>(0x05040100u, 0x07060302u); xpose<2>(0x05040100u, 0x07060302u); }
  wait_vm<2 * R + 1>();
  xpose<3>(0x05040100u, 0x07060302u);
  if (chain) PRIO(0); else PRIO(BPRIO);
  STAMP(6);
  v4u fl0;
  if (__builtin_expect(!slow, 1)) {
    const v4u sel = {m == 0 ? 0x3F80u : (m == 1 ? 0x3F800000u : 0u), m == 2 ? 0x3F80u : (m == 3 ? 0x3F800000u : 0u),
                     m == 4 ? 0x3F80u : (m == 5 ? 0x3F800000u : 0u), m == 6 ? 0x3F80u : (m == 7 ? 0x3F800000u : 0u)};
    const v4u self = {sel.x | (lane == 8 ? 0x3F80u : (lane == 9 ? 0x3F800000u : 0u)), sel.y | (lane == 10 ? 0x3F80u : (lane == 11 ? 0x3F800000u : 0u)),
                      sel.z | (lane == 12 ? 0x3F80u : (lane == 13 ? 0x3F800000u : 0u)), sel.w | (lane == 14 ? 0x3F80u : (lane == 15 ? 0x3F800000u : 0u))};
    v4u cur = self;
    fp += 8 * R;
    f32x4 a0 = {0.f, 0.f, 0.f, 0.f}, a1 = {0.f, 0.f, 0.f, 0.f};
    unsigned haddr = L_H + (lane >> 5) * 256 + m * 16 + (q & 1) * 8 + n0 * HP;
    int rem = (int)(pk & 0xFFF);
#define SLOT(i, REFILL) { \
        a0 = mfma(cur, bl[i], a0); \
        a1 = mfma(cur, br[i], a1); \
        cur = sel; \
        if (REFILL) { \
          const unsigned o = ((en[i] << 3) & ~511u) | l16; \
          bl[i] = __builtin_amdgcn_raw_buffer_load_b128(xr, o, 0, 0); \
          br[i] = __builtin_amdgcn_raw_buffer_load_b128(xr, o + 256, 0, 0); \
          en[i] = fp[4 * i]; \
        } \
        if (__builtin_expect(--rem == 0, 0)) { \
          float f[4]; \
          _Pragma("unroll") for (int r = 0; r < 4; ++r) { \
            auto s2 = __builtin_amdgcn_permlane32_swap(asu(a0[r]), asu(a1[r]), false, false); \
            const unsigned u0 = s2[0], u1 = s2[1]; \
            f[r] = fmaf(epsf, asf(u1), asf(u0)); \
          } \
          *(LAS v2u*)(L + haddr) = v2u{pack_bf16(f[0], f[1]), pack_bf16(f[2], f[3])}; \
          haddr += HP; \
          a0 = f32x4{0.f, 0.f, 0.f, 0.f}; a1 = a0; \
          cur = self; \
          pk >>= 12; \
          rem = (int)(pk & 0xFFF); \
        } }
    if (w == 6 || (w == 7 && share2 < 32)) touch<1>(wsd, WS_W2T + (w == 7 ? share2 : rank) * 8192 + lane * 128);
    for (; nit > 1; --nit) {
#pragma unroll
      for (int i = 0; i < R; ++i) SLOT(i, true)
      fp += 4 * R;
    }
    fl0 = __builtin_amdgcn_raw_buffer_load_b128(wsr, WS_WFLAG + lane * 16, 0, SC1);
#pragma unroll
    for (int i = 0; i < R; ++i) SLOT(i, false)
  } else {
#pragma unroll 1
    for (int j = 0; j < nn; ++j) {
      const int node = node0 + n0 + j;
      const v2u s2 = __builtin_amdgcn_raw_buffer_load_b64(xr, (unsigned)node * 512u + lane * 8, 0, 0);
      float a0 = bflo(s2.x), a1 = bfhi(s2.x), a2 = bflo(s2.y), a3 = bfhi(s2.y);
      if (node < N)
        for (int eb = 0; eb < E; eb += 64) {
          const int e = eb + lane;
          const int dv2 = (int)__builtin_amdgcn_raw_buffer_load_b32(rsrc(dst, E * 4), e * 4, 0, 0);
          const int sv2 = (int)__builtin_amdgcn_raw_buffer_load_b32(rsrc(src, E * 4), e * 4, 0, 0);
          u64 mkk = __ballot(dv2 == node && e < E);
          while (mkk) {
            const int b = __builtin_ctzll(mkk); mkk &= mkk - 1;
            const unsigned sidx = (unsigned)__builtin_amdgcn_readlane(sv2, b);
            const v2u rv = __builtin_amdgcn_raw_buffer_load_b64(xr, sidx < (unsigned)N ? sidx * 512u + lane * 8 : OOB, 0, 0);
            a0 += bflo(rv.x); a1 += bfhi(rv.x); a2 += bflo(rv.y); a3 += bfhi(rv.y);
          }
        }
      *(LAS v2u*)(L + L_H + (n0 + j) * HP + lane * 8) =
          v2u{pack_bf16(fmaf(epsf, bflo(s2.x), a0), fmaf(epsf, bfhi(s2.x), a1)), pack_bf16(fmaf(epsf, bflo(s2.y), a2), fmaf(epsf, bfhi(s2.y), a3))};
    }
    fl0 = __builtin_amdgcn_raw_buffer_load_b128(wsr, WS_WFLAG + lane * 16, 0, SC1);
  }
  STAMP(7);
  PRIO(0);
  {
    bool okw = false;
    const unsigned tl = (unsigned)tag;
    for (unsigned sp = 0; sp <= SPIN_CAP; ++sp) {
      if (__all(fl0.x == tl && fl0.y == tl && fl0.z == tl && fl0.w == tl)) { okw = true; break; }
      fl0 = __builtin_amdgcn_raw_buffer_load_b128(wsr, WS_WFLAG + lane * 16, 0, SC1);
    }
    if (__builtin_expect(!okw, 0)) {
#pragma unroll 1
      for (int i = 0; i < 32; ++i) { unsigned tv[8]; tile_w2_load(w2r, w * 32 + i, lane, tv); tile_w2_store(wsr, w * 32 + i, lane, tv); }
      asm volatile("s_waitcnt vmcnt(0)" ::: "memory");
    }
    __builtin_amdgcn_fence(__ATOMIC_ACQUIRE, "wavefront");
  }
  if (!timedout && tid == 192 && t == 0) __hip_atomic_store((GAS u64*)(ws + WS_EPOCH), tag, RLX_AGENT);
  const unsigned w2off = WS_W2T + w * 32768 + lane * 16;
  static_for<0, W2PRE>([&](auto I) { load_w2<I>(wsd, w2off); });
  f32x4 bias1[4];
#pragma unroll
  for (int c = 0; c < 4; ++c) bias1[c] = *(LAS f32x4*)(L + L_B1 + (64 * w + 16 * q + 4 * c) * 4);
  SB();
  bar();
  STAMP(8);
  f32x4 bias2[2];
  {
    LAS unsigned char* hb = L + L_H + m * HP + q * 16;
    LAS unsigned char* h1w = L + L_H1 + m * H1P + (64 * w + 16 * q) * 2;
    constexpr int P1 = 5;
    v4u b[P1];
    f32x4 acc[3][4];
    v4u bb2;
    auto epi = [&](int mt, int half) __attribute__((always_inline)) {
      unsigned o[4];
#pragma unroll
      for (int k = 0; k < 4; ++k) o[k] = relu_pack(acc[mt][2 * half + (k >> 1)][2 * (k & 1)], acc[mt][2 * half + (k >> 1)][2 * (k & 1) + 1]);
      *(LAS v4u*)(h1w + mt * 16 * H1P + 16 * half) = v4u{o[0], o[1], o[2], o[3]};
    };
#define U1_KS(u) ((u) < 12 ? (u) / 3 : 4 + ((u) - 12) % 4)
#define U1_MT(u) ((u) < 12 ? (u) % 3 : ((u) - 12) / 4)
#pragma unroll
    for (int u = 0; u < P1; ++u) b[u] = *(LAS v4u*)(hb + U1_MT(u) * 16 * HP + U1_KS(u) * 64);
    static_for<0, 24>([&](auto U) {
      constexpr int u = U, ks = U1_KS(u), mt = U1_MT(u);
      if constexpr (u % 6 == 0) PRIO(3 - u / 6);
      static_for<0, 4>([&](auto C) {
        if constexpr (ks == 0) mfma_p0<PB + 4 * C>(acc[mt][C], b[u % P1], bias1[C]);
        else mfma_p<PB + 16 * ks + 4 * C>(acc[mt][C], b[u % P1]);
      });
      if constexpr (u + P1 < 24) b[u % P1] = *(LAS v4u*)(hb + U1_MT(u + P1) * 16 * HP + U1_KS(u + P1) * 64);
      if constexpr (u >= 2 && u < 18) load_w2<6 + u>(wsd, w2off);
      if constexpr (u == 20) bb2 = *(LAS v4u*)(L + L_B2 + (32 * w + 8 * q) * 2);
      if constexpr (u == 17 || u == 21) epi(mt - 1, 0);
      if constexpr (u == 18 || u == 22) epi(mt - 1, 1);
      SB();
    });
    asm volatile("s_nop 7" : "+v"(acc[2][0]), "+v"(acc[2][1]), "+v"(acc[2][2]), "+v"(acc[2][3]));
    epi(2, 0); epi(2, 1);
#pragma unroll
    for (int nt = 0; nt < 2; ++nt) {
      bias2[nt] = f32x4{bflo(bb2[2 * nt]), bfhi(bb2[2 * nt]), bflo(bb2[2 * nt + 1]), bfhi(bb2[2 * nt + 1])};
      asm volatile("" : "+v"(bias2[nt]));
    }
  }
  SB();
  STAMP(9);
  bar();
  {
    f32x4 acc[3][2];
    LAS unsigned char* hb = L + L_H1 + m * H1P + q * 16;
    const __amdgpu_buffer_rsrc_t outr = rsrc(out, N * 512);
    auto put = [&](int mt) __attribute__((always_inline)) {
      const int r = 16 * mt + m;
      v4u o;
      o.x = pack_bf16(acc[mt][0][0], acc[mt][0][1]); o.y = pack_bf16(acc[mt][0][2], acc[mt][0][3]);
      o.z = pack_bf16(acc[mt][1][0], acc[mt][1][1]); o.w = pack_bf16(acc[mt][1][2], acc[mt][1][3]);
      __builtin_amdgcn_raw_buffer_store_b128(o, outr, r < TM ? (unsigned)(node0 + r) * 512u + 64 * w + 16 * q : OOB, 0, OUTAUX);
    };
    constexpr int P2 = 8;
    v4u b[P2];
#define U2_KS(u) ((u) < 24 ? (u) / 3 : 8 + ((u) - 24) % 8)
#define U2_MT(u) ((u) < 24 ? (u) % 3 : ((u) - 24) / 8)
#pragma unroll
    for (int u = 0; u < P2; ++u) b[u] = *(LAS v4u*)(hb + U2_MT(u) * 16 * H1P + U2_KS(u) * 64);
    static_for<0, 48>([&](auto U) {
      constexpr int u = U, ks = U2_KS(u), mt = U2_MT(u);
      if constexpr (u % 12 == 0) PRIO(3 - u / 12);
      if constexpr (u < 16 && (u & 1) == 0) load_w2<24 + u / 2>(wsd, w2off);
      constexpr int issued = u < 16 ? 25 + u / 2 : 32;
      if constexpr (mt == 0) wait_vm<issued - (2 * ks + 2)>();
      if constexpr (ks == 0) { mfma_p0<w2reg(0)>(acc[mt][0], b[u % P2], bias2[0]); mfma_p0<w2reg(1)>(acc[mt][1], b[u % P2], bias2[1]); }
      else { mfma_p<w2reg(2 * ks)>(acc[mt][0], b[u % P2]); mfma_p<w2reg(2 * ks + 1)>(acc[mt][1], b[u % P2]); }
      if constexpr (u + P2 < 48) b[u % P2] = *(LAS v4u*)(hb + U2_MT(u + P2) * 16 * H1P + U2_KS(u + P2) * 64);
      if constexpr (u == 36 || u == 44) put(mt - 1);
      SB();
    });
    asm volatile("s_nop 7" : "+v"(acc[2][0]), "+v"(acc[2][1]));
    put(2);
  }
  if (!timedout && w == 3 && __hip_atomic_load((GAS u64*)(ws + WS_EPOCH), RLX_AGENT) != tag && lane == 0) __hip_atomic_store((GAS u64*)(ws + WS_EPOCH), tag, RLX_AGENT);
  STAMP(10);
  STAMP_OUT((unsigned long long*)(ws + (64u << 20)), blockIdx.x * 8 + w);
  asm volatile(".Lgin_end:");
}

extern "C" void kernel_launch(void* const* d_in, const int* in_sizes, int n_in, void* d_out, int out_size, void* d_ws, size_t ws_size,
                              hipStream_t stream) {
  const int N = in_sizes[0] / D, E = in_sizes[1];
  const int T = (N + TM - 1) / TM;
  (void)hipFuncSetAttribute((const void*)gin_one, hipFuncAttributeMaxDynamicSharedMemorySize, L_END);
  gin_one<<<T, 512, L_END, stream>>>((const unsigned short*)d_in[0], (const int*)d_in[1], (const int*)d_in[2], (const unsigned short*)d_in[3],
                                 (const unsigned short*)d_in[4], (const unsigned short*)d_in[5], (const unsigned short*)d_in[6],
                                 (const unsigned short*)d_in[7], (unsigned short*)d_out, (unsigned char*)d_ws, N, E, T);
}
```

```cpp
#include <hip/hip_runtime.h>
#include <hip/hip_bf16.h>
#include <stdint.h>
#include <type_traits>

typedef unsigned v4u __attribute__((ext_vector_type(4)));
typedef unsigned v2u __attribute__((ext_vector_type(2)));
typedef float f32x4 __attribute__((ext_vector_type(4)));
typedef float f32x2 __attribute__((ext_vector_type(2)));
typedef short bf16x8 __attribute__((ext_vector_type(8)));
typedef __bf16 bf16x2_t __attribute__((ext_vector_type(2)));
typedef unsigned long long u64;
#define GAS __attribute__((address_space(1)))
#define LAS __attribute__((address_space(3)))
#define DI __device__ __forceinline__
#define RLX_AGENT __ATOMIC_RELAXED, __HIP_MEMORY_SCOPE_AGENT
#define RLX_WG __ATOMIC_RELAXED, __HIP_MEMORY_SCOPE_WORKGROUP
#define SB() __builtin_amdgcn_sched_barrier(0)
#define PRIO(p) __builtin_amdgcn_s_setprio(p)
#define STAMP(i)
#define STAMP_DECL
#define STAMP_OUT(base, idx)

constexpr int D = 256, H = 512, TM = 40;
constexpr int EPB = 640;
constexpr int CAP = 1536;
constexpr int HP = 544, H1P = 1056;
constexpr unsigned OOB = 0x40000000u, EOOB = OOB >> 3;
#define RING 3
constexpr int R = RING;
constexpr int SC1 = 16;
constexpr unsigned SPIN_CAP = 4096;
#define FAIR 1
#define XWARM 1
#define XP_EARLY 2
#define GATE 0
#define SPLIT 0
#define CELL_MODE 1
#define PRETOUCH 0
#define ZPAD 0
#define CPFS 128
#define BPRIO 0
#define BSCHED 0
#define XPRIO 0
#define ESLEEP 3
#define PSLEEP 24
#define NB1 8
#define NB2 16
#define NA 16
#define NCN 5
#define OUTAUX 16
#define W2PRE 8
constexpr unsigned WS_EPOCH = 0, WS_WFLAG = 4096, WS_W2T = 65536, WS_CELL = 1u << 20, WS_BYTES = WS_CELL + 256u * 16384u;
constexpr int L_H = 0, L_H1 = L_H + 48 * HP, L_FLAT = L_H1 + 48 * H1P, L_CNT = L_FLAT + (CAP + 128) * 4, L_MISC = L_CNT + 256,
              L_B1 = L_MISC + 32, L_B2 = L_B1 + 2048, L_END = L_B2 + 512;
constexpr int L_PSTG = L_H;
constexpr int L_PCELL = L_H1, L_PCNT = L_H1 + 320 * 64;

DI unsigned pack_bf16(float a, float b) { f32x2 v = {a, b}; bf16x2_t r = __builtin_convertvector(v, bf16x2_t); return __builtin_bit_cast(unsigned, r); }
DI float bflo(unsigned u) { return __builtin_bit_cast(float, u << 16); }
DI float bfhi(unsigned u) { return __builtin_bit_cast(float, u & 0xffff0000u); }
DI float asf(unsigned u) { return __builtin_bit_cast(float, u); }
DI unsigned asu(float u) { return __builtin_bit_cast(unsigned, u); }
DI f32x4 mfma(v4u a, v4u b, f32x4 c) {
  return __builtin_amdgcn_mfma_f32_16x16x32_bf16(__builtin_bit_cast(bf16x8, a), __builtin_bit_cast(bf16x8, b), c, 0, 0, 0);
}
typedef short s16x2 __attribute__((ext_vector_type(2)));
DI unsigned relu_pack(float a, float b) {
  const s16x2 v = __builtin_bit_cast(s16x2, pack_bf16(a, b));
  return __builtin_bit_cast(unsigned, __builtin_elementwise_max(v, s16x2{0, 0}));
}
DI void bar() { asm volatile("s_waitcnt lgkmcnt(0)" ::: "memory"); __builtin_amdgcn_s_barrier(); asm volatile("" ::: "memory"); }
template <int CTRL> DI unsigned dpp_add(unsigned x) { return x + (unsigned)__builtin_amdgcn_update_dpp(0, (int)x, CTRL, 0xF, 0xF, false); }
DI unsigned wave_incl_scan(unsigned x) {
  x = dpp_add<0x111>(x); x = dpp_add<0x112>(x); x = dpp_add<0x114>(x); x = dpp_add<0x118>(x);
  x = x + (unsigned)__builtin_amdgcn_update_dpp(0, (int)x, 0x142, 0xA, 0xF, false);
  x = x + (unsigned)__builtin_amdgcn_update_dpp(0, (int)x, 0x143, 0xC, 0xF, false);
  return x;
}
DI unsigned bsum(unsigned a, unsigned b) { return __builtin_amdgcn_sad_u8(a, 0u, __builtin_amdgcn_sad_u8(b, 0u, 0u)); }
DI __amdgpu_buffer_rsrc_t rsrc(const void* p, unsigned bytes) { return __builtin_amdgcn_make_buffer_rsrc((void*)p, 0, bytes, 0x00020000); }
DI unsigned tag_b(u64 tag) { return ((unsigned)tag + (unsigned)(tag >> 24) * 0x9E3779B1u) & 0x7FFFFu; }

DI void tile_w2_load(__amdgpu_buffer_rsrc_t w2r, int f, int lane, unsigned (&v)[8]) {
  const int ww = f >> 5, ks = (f >> 1) & 15, nt = f & 1, i = lane & 15, kg = lane >> 4;
  const int qo = (2 * ks + (kg >> 1)) & 3;
  const int row0 = 64 * (ks >> 1) + 32 * (qo & 1) + 4 * (qo >> 1) + 2 * (kg & 1);
  const unsigned o = f < 256 ? (row0 * D + 32 * ww + 8 * (i >> 2) + 4 * nt + (i & 3)) * 2 : OOB;
#pragma unroll
  for (int j = 0; j < 8; ++j) v[j] = __builtin_amdgcn_raw_buffer_load_b16(w2r, o + (j >> 2) * D * 2, (j & 3) * 8 * D * 2, 0);
}
DI void tile_w2_store(__amdgpu_buffer_rsrc_t wsr, int f, int lane, const unsigned (&v)[8]) {
  const v4u o = {v[0] | (v[1] << 16), v[2] | (v[3] << 16), v[4] | (v[5] << 16), v[6] | (v[7] << 16)};
  __builtin_amdgcn_raw_buffer_store_b128(o, wsr, f < 256 ? WS_W2T + f * 1024 + lane * 16 : OOB, 0, SC1);
}
constexpr int PB = 96;
template <int G> DI void load_w1(v4u srd, unsigned off) {
  unsigned so;
  constexpr int r = PB + 32 + 32 * G;
  asm volatile("s_mov_b32 %0, %3\n\t"
               "buffer_load_dwordx4 v[%c5:%c5+3], %1, %2, %0 offen\n\tbuffer_load_dwordx4 v[%c5+4:%c5+7], %1, %2, %0 offen offset:1024\n\t"
               "buffer_load_dwordx4 v[%c5+8:%c5+11], %1, %2, %0 offen offset:2048\n\tbuffer_load_dwordx4 v[%c5+12:%c5+15], %1, %2, %0 offen offset:3072\n\t"
               "s_mov_b32 %0, %4\n\t"
               "buffer_load_dwordx4 v[%c5+16:%c5+19], %1, %2, %0 offen\n\tbuffer_load_dwordx4 v[%c5+20:%c5+23], %1, %2, %0 offen offset:1024\n\t"
               "buffer_load_dwordx4 v[%c5+24:%c5+27], %1, %2, %0 offen offset:2048\n\tbuffer_load_dwordx4 v[%c5+28:%c5+31], %1, %2, %0 offen offset:3072"
               : "=&s"(so) : "v"(off), "s"(srd), "i"(G * 65536), "i"(G * 65536 + 4096), "i"(r) : "v255");
}
template <int G> DI void xpose(unsigned lo, unsigned hi) {
#pragma unroll
  for (int d = 0; d < 4; ++d)
#pragma unroll
    for (int p = 0; p < 2; ++p) {
      unsigned t0, t1;
      asm volatile("v_perm_b32 v[%c5], v[%c4+4], v[%c4], %2\n\tv_perm_b32 v[%c5+16], v[%c4+6], v[%c4+2], %2\n\t"
                   "v_perm_b32 v[%c5+4], v[%c4+4], v[%c4], %3\n\tv_perm_b32 v[%c5+20], v[%c4+6], v[%c4+2], %3\n\t"
                   "v_mov_b32 %0, v[%c5]\n\tv_mov_b32 %1, v[%c5+4]\n\t"
                   "v_mov_b32_dpp v[%c5], v[%c5+16] row_ror:8 row_mask:0xf bank_mask:0xc\n\t"
                   "v_mov_b32_dpp v[%c5+4], v[%c5+20] row_ror:8 row_mask:0xf bank_mask:0xc\n\t"
                   "v_mov_b32_dpp v[%c5+16], %0 row_ror:8 row_mask:0xf bank_mask:0x3\n\t"
                   "v_mov_b32_dpp v[%c5+20], %1 row_ror:8 row_mask:0xf bank_mask:0x3"
                   : "=&v"(t0), "=&v"(t1) : "s"(lo), "s"(hi), "i"(PB + 32 + 32 * G + 8 * d + p), "i"(PB + 32 * G + 8 * p + d));
    }
}
DI void xpose_all() { xpose<0>(0x05040100u, 0x07060302u); xpose<1>(0x05040100u, 0x07060302u); xpose<2>(0x05040100u, 0x07060302u); xpose<3>(0x05040100u, 0x07060302u); }
constexpr int w2reg(int i) { return i < 8 ? PB + 128 + 4 * i : PB + 4 * (i - 8); }
template <int I> DI void load_w2(v4u srd, unsigned off) {
  unsigned so;
  asm volatile("s_mov_b32 %0, %3\n\tbuffer_load_dwordx4 v[%c4:%c4+3], %1, %2, %0 offen offset:%c5 sc1"
               : "=&s"(so) : "v"(off), "s"(srd), "i"((I >> 2) * 4096), "i"(w2reg(I)), "i"((I & 3) * 1024));
}
template <int A> DI void mfma_p(f32x4& acc, v4u b) { asm volatile("v_mfma_f32_16x16x32_bf16 %0, v[%c2:%c2+3], %1, %0" : "+v"(acc) : "v"(b), "i"(A)); }
template <int A> DI void mfma_p0(f32x4& acc, v4u b, f32x4 c) {
  asm volatile("v_mfma_f32_16x16x32_bf16 %0, v[%c3:%c3+3], %1, %2" : "=&v"(acc) : "v"(b), "v"(c), "i"(A));
}
template <int AUX> DI void touch(v4u srd, unsigned off) {
  if constexpr (AUX) asm volatile("buffer_load_dword v[%c2], %0, %1, 0 offen sc1" :: "v"(off), "s"(srd), "i"(PB - 1));
  else asm volatile("buffer_load_dword v[%c2], %0, %1, 0 offen" :: "v"(off), "s"(srd), "i"(PB - 1));
}
template <int N> DI void wait_vm() { asm volatile("s_waitcnt vmcnt(%c0)" :: "i"(N)); }
template <int I, int E, typename F> DI void static_for(F&& f) { if constexpr (I < E) { f(std::integral_constant<int, I>{}); static_for<I + 1, E>(f); } }

__global__ __launch_bounds__(512, 2) __attribute__((amdgpu_num_vgpr(PB / 2 - 1))) void gin_one(const unsigned short* __restrict__ x, const int* __restrict__ src, const int* __restrict__ dst,
                                                  const unsigned short* __restrict__ epsp, const unsigned short* __restrict__ w1,
                                                  const unsigned short* __restrict__ b1, const unsigned short* __restrict__ w2,
                                                  const unsigned short* __restrict__ b2, unsigned short* __restrict__ out, unsigned char* ws,
                                                  int N, int E, int T) {
  extern __shared__ __attribute__((aligned(16))) unsigned char Ls[];
  LAS unsigned char* L = (LAS unsigned char*)Ls;
  const int tid = threadIdx.x, lane = tid & 63, w = __builtin_amdgcn_readfirstlane(tid >> 6);
  const int t = blockIdx.x, node0 = t * TM;
  const unsigned bulk = w >> 2, chain = bulk ^ 1;
  const int rank = (t >> 3) & 31;
  {
    u64 pc; unsigned len;
    asm volatile("s_getpc_b64 %0\n.Lgin_pc:\n\ts_mov_b32 %1, .Lgin_end-.Lgin_pc" : "=s"(pc), "=s"(len));
    touch<0>(v4u{(unsigned)pc, (unsigned)(pc >> 32) & 0xffffu, len, 0x00020000u}, (rank * 8 + (tid >> 6)) * CPFS + lane * 32768);
  }
  SB();
  asm volatile("" :: "s"(x), "s"(src), "s"(dst), "s"(epsp), "s"(w1), "s"(b1), "s"(w2), "s"(b2), "s"(out), "s"(ws), "s"(N), "s"(E), "s"(T));
  const int share2 = (T >> 3) + rank;
  const __amdgpu_buffer_rsrc_t wsr = rsrc(ws, WS_BYTES), xr = rsrc(x, N * 512), w1r = rsrc(w1, D * H * 2), w2r = rsrc(w2, D * H * 2);
  STAMP_DECL STAMP(0);
  const int q = lane >> 4, m = lane & 15;
  const v4u w1d = {(unsigned)(u64)w1, (unsigned)((u64)w1 >> 32) & 0xffffu, D * H * 2, 0x00020000u};
  const v4u wsd = {(unsigned)(u64)ws, (unsigned)((u64)ws >> 32) & 0xffffu, WS_BYTES, 0x00020000u};
  const unsigned w1off = (((lane >> 3) & 1) * 32 + q * 8) * 1024 + w * 128 + (lane & 7) * 16;
  LAS unsigned* pcnt = (LAS unsigned*)(L + L_PCNT);
  LAS unsigned* pcell = (LAS unsigned*)(L + L_PCELL);

  const int tf = t + (w - 1) * T;
  if (w == 4) {
    PRIO(3);
    const __amdgpu_buffer_rsrc_t dr = rsrc(dst, E * 4), sr = rsrc(src, E * 4);
    v4u dv[3], sv[3];
#pragma unroll
    for (int i = 0; i < 3; ++i) {
      const unsigned o = (i == 2 && lane >= 32) ? OOB : (unsigned)(t * EPB + (lane + 64 * i) * 4) * 4u;
      dv[i] = __builtin_amdgcn_raw_buffer_load_b128(dr, o, 0, 0);
      sv[i] = __builtin_amdgcn_raw_buffer_load_b128(sr, o, 0, 0);
    }
    const u64 ep0 = __hip_atomic_load((GAS u64*)(ws + WS_EPOCH), RLX_AGENT);
    SB();
    *(LAS v4u*)(pcnt + lane * 4) = v4u{0, 0, 0, 0};
    pcnt[256 + lane] = 0;
    const unsigned dummy = 256u + lane;
    unsigned tt[12], rk[12];
#pragma unroll
    for (int i = 0; i < 12; ++i) {
      const int e = t * EPB + (lane + 64 * (i >> 2)) * 4;
      const unsigned lo = (e < E && !(i >= 8 && lane >= 32)) ? 0u : dummy;
      tt[i] = max(min(__umul24(min(dv[i >> 2][i & 3], 16383u), 52429u) >> 21, dummy), lo);
      rk[i] = __hip_atomic_fetch_add(pcnt + tt[i], 1u, RLX_WG);
    }
#pragma unroll
    for (int i = 0; i < 12; ++i)
      pcell[tt[i] * 16 + (rk[i] & 15u)] = (dv[i >> 2][i & 3] - tt[i] * TM) | (min(sv[i >> 2][i & 3], 16383u) << 6);
    *(LAS u64*)(L + L_MISC) = ep0;
  } else {
  __builtin_amdgcn_s_sleep(ESLEEP);
  if (w == 0 || (w == 5 && share2 < 32)) touch<0>(w1d, (w ? share2 : rank) * 8192 + lane * 128);
   if (w == 3) {
    const v4u v1 = __builtin_amdgcn_raw_buffer_load_b128(rsrc(b1, H * 2), lane * 16, 0, 0);
    const v4u v2 = __builtin_amdgcn_raw_buffer_load_b128(rsrc(b2, D * 2), lane * 16, 0, 0);
    const unsigned ev = epsp[0];
    *(LAS unsigned*)(L + L_CNT + lane * 4) = 0;
#pragma unroll
    for (int i = 0; i < 8; ++i)
      *(LAS float*)(L + L_B1 + ((lane >> 3) * 64 + (((lane >> 2) & 1) + 2 * (i >> 2)) * 16 + 4 * (i & 3) + (lane & 3)) * 4) = (i & 1) ? bfhi(v1[i >> 1]) : bflo(v1[i >> 1]);
    *(LAS v4u*)(L + (lane < 32 ? L_B2 + lane * 16 : L_FLAT + lane * 16)) = v2;
    *(LAS v2u*)(L + L_MISC + 8) = v2u{0u, ev};
    *(LAS unsigned*)(L + L_MISC + 16) = 0u;
   } else if (w == 1 || w == 2) {
    unsigned tv[8];
    tile_w2_load(w2r, tf, lane, tv);
    tile_w2_store(wsr, tf, lane, tv);
   }
  }
  if (chain) { load_w1<0>(w1d, w1off); load_w1<1>(w1d, w1off); }
  PRIO(3);
  bar();
  STAMP(1);
  const u64 tag = *(LAS u64*)(L + L_MISC) + 1;
  const unsigned tagA = (unsigned)tag & 0xFFFFFFu, tagB = tag_b(tag);
  {
    v4u e[2]; unsigned c[2];
#pragma unroll
    for (int i = 0; i < 2; ++i) { e[i] = *(LAS v4u*)(pcell + (w * 2 + i) * 256 + lane * 4); c[i] = pcnt[(w * 2 + i) * 16 + (lane >> 2)]; }
#pragma unroll
    for (int i = 0; i < 2; ++i) {
      const unsigned tc = (w * 2 + i) * 16 + (lane >> 2), cc = min(c[i], 31u);
      const v4u g = {(e[i].x & 0xFFFFFu) | (e[i].y << 20), ((e[i].y >> 12) & 0xFFu) | (tagA << 8), (e[i].z & 0xFFFFFu) | (e[i].w << 20),
                     ((e[i].w >> 12) & 0xFFu) | (cc << 8) | (tagB << 13)};
      __builtin_amdgcn_raw_buffer_store_b128(g, wsr, tc < (unsigned)T ? WS_CELL + tc * 16384 + t * 64 + (lane & 3) * 16 : OOB, 0, SC1);
    }
  }
  SB();
  PRIO(1);
  if (chain) { load_w1<2>(w1d, w1off); load_w1<3>(w1d, w1off); } else __builtin_amdgcn_s_sleep(PSLEEP);
  STAMP(2);
  bool any2 = false;
  unsigned ent[16], rk[16], spins = 0;
  LAS unsigned* flat = (LAS unsigned*)(L + L_FLAT);
  if (chain) {
    PRIO(2);
    v4u g[4];
    const unsigned coff = WS_CELL + t * 16384 + w * 4096 + lane * 16;
    constexpr unsigned CSTEP = 1024;
#pragma unroll
    for (int i = 0; i < 4; ++i) g[i] = __builtin_amdgcn_raw_buffer_load_b128(wsr, coff + i * CSTEP, 0, SC1);
    bool tmo = false;
    const int pl = T - 64 * w - (lane >> 2);
#define GR_OK(i) (16 * (i) >= pl || ((g[i].y >> 8) == tagA && (g[i].w >> 13) == tagB))
    if (!__all(GR_OK(0) && GR_OK(1) && GR_OK(2) && GR_OK(3))) {
      for (;;) {
#pragma unroll
        for (int i = 0; i < 4; ++i) {
          const bool ok = GR_OK(i);
          const v4u nv = __builtin_amdgcn_raw_buffer_load_b128(wsr, ok ? OOB : coff + i * CSTEP, 0, SC1);
#pragma unroll
          for (int k = 0; k < 4; ++k) g[i][k] = ok ? g[i][k] : nv[k];
        }
        if (__all(GR_OK(0) && GR_OK(1) && GR_OK(2) && GR_OK(3))) break;
        if (++spins > SPIN_CAP) { tmo = true; break; }
      }
      ++spins;
    }
    STAMP(3);
    if (lane == 0) __hip_atomic_fetch_add((LAS unsigned*)(L + L_MISC + 16), 1u, RLX_WG);
    LAS unsigned char* stg = L + L_PSTG + w * 5120;
#pragma unroll
    for (int i = 0; i < 4; ++i) *(LAS v4u*)(stg + (16 * i + (lane >> 2)) * 80 + (lane & 3) * 16) = g[i];
#pragma unroll
    for (int i = 0; i < 4; ++i) g[i] = *(LAS v4u*)(stg + lane * 80 + i * 16);
    const int nval = (64 * w + lane < T && !tmo) ? (int)((g[0].w >> 8) & 31u) : 0;
    any2 = __any(nval > 8);
    bool bad = nval > 16;
    const unsigned dummy = 40u + (unsigned)(lane % 24);
    auto rank = [&](int lo) __attribute__((always_inline)) {
#pragma unroll
      for (int i = lo; i < lo + 8; ++i) {
        const unsigned a = g[i >> 2][i & 2], b = g[i >> 2][(i & 2) + 1];
        ent[i] = i < nval ? ((i & 1) ? __builtin_amdgcn_alignbit(b, a, 20) : a) & 0xFFFFFu : dummy;
        rk[i] = __hip_atomic_fetch_add((LAS unsigned*)(L + L_CNT + ((ent[i] & 63u) << 2)), 1u << (8 * w), RLX_WG);
      }
#pragma unroll
      for (int i = lo; i < lo + 8; ++i) { rk[i] = (rk[i] >> (8 * w)) & 255u; bad |= rk[i] == 255u; }
    };
    rank(0);
    if (__builtin_expect(any2, 0)) rank(8);
    if ((__any(bad) || tmo) && lane == 0) __hip_atomic_fetch_or((LAS unsigned*)(L + L_MISC + 8), tmo ? 3u : 1u, RLX_WG);
  } else {
    PRIO(0);
    load_w1<0>(w1d, w1off); load_w1<1>(w1d, w1off);
    wait_vm<8>(); xpose<0>(0x05040100u, 0x07060302u);
    wait_vm<0>(); xpose<1>(0x05040100u, 0x07060302u);
    load_w1<2>(w1d, w1off);
    load_w1<3>(w1d, w1off);
    if (w < 7) touch<0>(v4u{(unsigned)(u64)x, (unsigned)((u64)x >> 32) & 0xffffu, (unsigned)N * 512u, 0x00020000u},
                        (w - 4) * 64 + lane < 160 ? node0 * 512 + ((w - 4) * 64 + lane) * 128 : OOB);
    else touch<0>(w1d, OOB);
  }
  bar();
  STAMP(4);
  bool slow, timedout;
  int s0, nit;
  u64 pk = 0;
  const int n0 = bulk ? (10 - NCN) * w + (8 * NCN - 40) : NCN * w;
  const int nn = bulk ? 10 - NCN : NCN;
  {
    const unsigned cw = *(LAS unsigned*)(L + L_CNT + lane * 4);
    const unsigned flagv = __builtin_amdgcn_readfirstlane(*(LAS unsigned*)(L + L_MISC + 8));
    const unsigned deg = __builtin_amdgcn_sad_u8(cw, 0u, 0u), below = __builtin_amdgcn_sad_u8(cw & ((1u << (8 * (w & 3))) - 1u), 0u, 0u);
    const unsigned l4 = (deg + 4u) & ~3u;
    const bool lastn = lane < 4 * NCN ? (lane % NCN) == NCN - 1 : ((lane - 4 * NCN) % (10 - NCN)) == 9 - NCN;
    const unsigned plen = lane < TM ? l4 + (lastn ? 4u * (R - 1) : 0u) : 0u;
    const unsigned incl = wave_incl_scan(plen), start = incl - plen;
    timedout = (flagv & 2u) != 0u;
    slow = flagv != 0u || __builtin_amdgcn_readlane(incl, 63) > (unsigned)CAP;
    const unsigned tail = __builtin_amdgcn_readlane(incl, n0 + nn - 1) - 4 * (R - 1);
    s0 = __builtin_amdgcn_readlane(start, n0);
    nit = ((tail - s0) / 4 + R - 1) / R;
#pragma unroll
    for (int j = 0; j < 5; ++j) pk |= (u64)(((unsigned)__builtin_amdgcn_readlane(l4, n0 + j) >> 2) & 0xFFFu) << (12 * j);
    if (slow) s0 = 0;
    if (!slow) {
      const unsigned st2 = (unsigned)__builtin_amdgcn_ds_bpermute((n0 + (lane >> 3)) << 2, (int)(start | (deg << 16)));
      if (chain) {
        const unsigned B4 = L_FLAT + 4u * (lane < TM ? start + 1u + below : CAP + 64u);
        auto place = [&](int lo) __attribute__((always_inline)) {
          unsigned b[8];
#pragma unroll
          for (int i = 0; i < 8; ++i) b[i] = (unsigned)__builtin_amdgcn_ds_bpermute((int)((ent[lo + i] & 63u) << 2), (int)B4);
#pragma unroll
          for (int i = 0; i < 8; ++i) *(LAS unsigned*)(L + b[i] + 4u * rk[lo + i]) = ent[lo + i];
        };
        place(0);
        if (__builtin_expect(any2, 0)) place(8);
      }
      const unsigned dg = st2 >> 16, k = lane & 7, pp = k == 7 ? 0u : dg + 1u + k;
      const bool mine = (lane >> 3) < nn && pp < ((dg + 4u) & ~3u);
      const unsigned at = mine ? (st2 & 0xFFFFu) + pp : (lane >= 48 && lane - 48 < 4 * (R - 1) ? tail + lane - 48 : CAP + lane);
      flat[at] = (mine && k == 7) ? (unsigned)(node0 + n0 + (lane >> 3)) << 6 : EOOB;
    }
  }
  if (bulk) {
    wait_vm<9>(); xpose<2>(0x05040100u, 0x07060302u);
  }
  bar();
  STAMP(5);
  PRIO(2);
  if (w == 1 || w == 2) {
    asm volatile("s_waitcnt vmcnt(0)" ::: "memory");
    if (lane == 0 && tf < 256) __hip_atomic_store((GAS unsigned*)(ws + WS_WFLAG) + tf, (unsigned)tag, RLX_AGENT);
  }
  const unsigned l16 = m * 16;
  LAS unsigned* fp = flat + s0 + q;
  v4u bl[R], br[R];
  unsigned en[R];
#pragma unroll
  for (int i = 0; i < R; ++i) en[i] = fp[4 * i];
  const float epsf = bflo(*(LAS unsigned*)(L + L_MISC + 12));
#pragma unroll
  for (int i = 0; i < R; ++i) {
    const unsigned o = ((en[i] << 3) & ~511u) | l16;
    bl[i] = __builtin_amdgcn_raw_buffer_load_b128(xr, o, 0, 0);
    br[i] = __builtin_amdgcn_raw_buffer_load_b128(xr, o + 256, 0, 0);
    en[i] = fp[4 * (R + i)];
  }
  asm volatile("" ::: "memory");
  SB();
  if (chain) { PRIO(XPRIO); xpose<0>(0x05040100u, 0x07060302u); xpose<1>(0x05040100u, 0x07060302u); xpose<2>(0x05040100u, 0x07060302u); }
  wait_vm<2 * R + 1>();
  xpose<3>(0x05040100u, 0x07060302u);
  if (chain) PRIO(0); else PRIO(BPRIO);
  STAMP(6);
  v4u fl0;
  if (__builtin_expect(!slow, 1)) {
    const v4u sel = {m == 0 ? 0x3F80u : (m == 1 ? 0x3F800000u : 0u), m == 2 ? 0x3F80u : (m == 3 ? 0x3F800000u : 0u),
                     m == 4 ? 0x3F80u : (m == 5 ? 0x3F800000u : 0u), m == 6 ? 0x3F80u : (m == 7 ? 0x3F800000u : 0u)};
    const v4u self = {sel.x | (lane == 8 ? 0x3F80u : (lane == 9 ? 0x3F800000u : 0u)), sel.y | (lane == 10 ? 0x3F80u : (lane == 11 ? 0x3F800000u : 0u)),
                      sel.z | (lane == 12 ? 0x3F80u : (lane == 13 ? 0x3F800000u : 0u)), sel.w | (lane == 14 ? 0x3F80u : (lane == 15 ? 0x3F800000u : 0u))};
    v4u cur = self;
    fp += 8 * R;
    f32x4 a0 = {0.f, 0.f, 0.f, 0.f}, a1 = {0.f, 0.f, 0.f, 0.f};
    unsigned haddr = L_H + (lane >> 5) * 256 + m * 16 + (q & 1) * 8 + n0 * HP;
    int rem = (int)(pk & 0xFFF);
#define SLOT(i, REFILL) { \
        a0 = mfma(cur, bl[i], a0); \
        a1 = mfma(cur, br[i], a1); \
        cur = sel; \
        if (REFILL) { \
          const unsigned o = ((en[i] << 3) & ~511u) | l16; \
          bl[i] = __builtin_amdgcn_raw_buffer_load_b128(xr, o, 0, 0); \
          br[i] = __builtin_amdgcn_raw_buffer_load_b128(xr, o + 256, 0, 0); \
          en[i] = fp[4 * i]; \
        } \
        if (__builtin_expect(--rem == 0, 0)) { \
          float f[4]; \
          _Pragma("unroll") for (int r = 0; r < 4; ++r) { \
            auto s2 = __builtin_amdgcn_permlane32_swap(asu(a0[r]), asu(a1[r]), false, false); \
            const unsigned u0 = s2[0], u1 = s2[1]; \
            f[r] = fmaf(epsf, asf(u1), asf(u0)); \
          } \
          *(LAS v2u*)(L + haddr) = v2u{pack_bf16(f[0], f[1]), pack_bf16(f[2], f[3])}; \
          haddr += HP; \
          a0 = f32x4{0.f, 0.f, 0.f, 0.f}; a1 = a0; \
          cur = self; \
          pk >>= 12; \
          rem = (int)(pk & 0xFFF); \
        } }
    if (w == 6 || (w == 7 && share2 < 32)) touch<1>(wsd, WS_W2T + (w == 7 ? share2 : rank) * 8192 + lane * 128);
    for (; nit > 1; --nit) {
#pragma unroll
      for (int i = 0; i < R; ++i) SLOT(i, true)
      fp += 4 * R;
    }
    fl0 = __builtin_amdgcn_raw_buffer_load_b128(wsr, WS_WFLAG + lane * 16, 0, SC1);
#pragma unroll
    for (int i = 0; i < R; ++i) SLOT(i, false)
  } else {
#pragma unroll 1
    for (int j = 0; j < nn; ++j) {
      const int node = node0 + n0 + j;
      const v2u s2 = __builtin_amdgcn_raw_buffer_load_b64(xr, (unsigned)node * 512u + lane * 8, 0, 0);
      float a0 = bflo(s2.x), a1 = bfhi(s2.x), a2 = bflo(s2.y), a3 = bfhi(s2.y);
      if (node < N)
        for (int eb = 0; eb < E; eb += 64) {
          const int e = eb + lane;
          const int dv2 = (int)__builtin_amdgcn_raw_buffer_load_b32(rsrc(dst, E * 4), e * 4, 0, 0);
          const int sv2 = (int)__builtin_amdgcn_raw_buffer_load_b32(rsrc(src, E * 4), e * 4, 0, 0);
          u64 mkk = __ballot(dv2 == node && e < E);
          while (mkk) {
            const int b = __builtin_ctzll(mkk); mkk &= mkk - 1;
            const unsigned sidx = (unsigned)__builtin_amdgcn_readlane(sv2, b);
            const v2u rv = __builtin_amdgcn_raw_buffer_load_b64(xr, sidx < (unsigned)N ? sidx * 512u + lane * 8 : OOB, 0, 0);
            a0 += bflo(rv.x); a1 += bfhi(rv.x); a2 += bflo(rv.y); a3 += bfhi(rv.y);
          }
        }
      *(LAS v2u*)(L + L_H + (n0 + j) * HP + lane * 8) =
          v2u{pack_bf16(fmaf(epsf, bflo(s2.x), a0), fmaf(epsf, bfhi(s2.x), a1)), pack_bf16(fmaf(epsf, bflo(s2.y), a2), fmaf(epsf, bfhi(s2.y), a3))};
    }
    fl0 = __builtin_amdgcn_raw_buffer_load_b128(wsr, WS_WFLAG + lane * 16, 0, SC1);
  }
  STAMP(7);
  PRIO(0);
  {
    bool okw = false;
    const unsigned tl = (unsigned)tag;
    for (unsigned sp = 0; sp <= SPIN_CAP; ++sp) {
      if (__all(fl0.x == tl && fl0.y == tl && fl0.z == tl && fl0.w == tl)) { okw = true; break; }
      fl0 = __builtin_amdgcn_raw_buffer_load_b128(wsr, WS_WFLAG + lane * 16, 0, SC1);
    }
    if (__builtin_expect(!okw, 0)) {
#pragma unroll 1
      for (int i = 0; i < 32; ++i) { unsigned tv[8]; tile_w2_load(w2r, w * 32 + i, lane, tv); tile_w2_store(wsr, w * 32 + i, lane, tv); }
      asm volatile("s_waitcnt vmcnt(0)" ::: "memory");
    }
    __builtin_amdgcn_fence(__ATOMIC_ACQUIRE, "wavefront");
  }
  if (!timedout && tid == 192 && t == 0) __hip_atomic_store((GAS u64*)(ws + WS_EPOCH), tag, RLX_AGENT);
  const unsigned w2off = WS_W2T + w * 32768 + lane * 16;
  static_for<0, W2PRE>([&](auto I) { load_w2<I>(wsd, w2off); });
  f32x4 bias1[4];
#pragma unroll
  for (int c = 0; c < 4; ++c) bias1[c] = *(LAS f32x4*)(L + L_B1 + (64 * w + 16 * q + 4 * c) * 4);
  SB();
  bar();
  STAMP(8);
  f32x4 bias2[2];
  {
    LAS unsigned char* hb = L + L_H + m * HP + q * 16;
    LAS unsigned char* h1w = L + L_H1 + m * H1P + (64 * w + 16 * q) * 2;
    constexpr int P1 = 5;
    v4u b[P1];
    f32x4 acc[3][4];
    v4u bb2;
    auto epi = [&](int mt, int half) __attribute__((always_inline)) {
      unsigned o[4];
#pragma unroll
      for (int k = 0; k < 4; ++k) o[k] = relu_pack(acc[mt][2 * half + (k >> 1)][2 * (k & 1)], acc[mt][2 * half + (k >> 1)][2 * (k & 1) + 1]);
      *(LAS v4u*)(h1w + mt * 16 * H1P + 16 * half) = v4u{o[0], o[1], o[2], o[3]};
    };
#define U1_KS(u) ((u) < 12 ? (u) / 3 : 4 + ((u) - 12) % 4)
#define U1_MT(u) ((u) < 12 ? (u) % 3 : ((u) - 12) / 4)
#pragma unroll
    for (int u = 0; u < P1; ++u) b[u] = *(LAS v4u*)(hb + U1_MT(u) * 16 * HP + U1_KS(u) * 64);
    static_for<0, 24>([&](auto U) {
      constexpr int u = U, ks = U1_KS(u), mt = U1_MT(u);
      if constexpr (u % 6 == 0) PRIO(3 - u / 6);
      static_for<0, 4>([&](auto C) {
        if constexpr (ks == 0) mfma_p0<PB + 4 * C>(acc[mt][C], b[u % P1], bias1[C]);
        else mfma_p<PB + 16 * ks + 4 * C>(acc[mt][C], b[u % P1]);
      });
      if constexpr (u + P1 < 24) b[u % P1] = *(LAS v4u*)(hb + U1_MT(u + P1) * 16 * HP + U1_KS(u + P1) * 64);
      if constexpr (u >= 2 && u < 18) load_w2<6 + u>(wsd, w2off);
      if constexpr (u == 20) bb2 = *(LAS v4u*)(L + L_B2 + (32 * w + 8 * q) * 2);
      if constexpr (u == 17 || u == 21) epi(mt - 1, 0);
      if constexpr (u == 18 || u == 22) epi(mt - 1, 1);
      SB();
    });
    asm volatile("s_nop 7" : "+v"(acc[2][0]), "+v"(acc[2][1]), "+v"(acc[2][2]), "+v"(acc[2][3]));
    epi(2, 0); epi(2, 1);
#pragma unroll
    for (int nt = 0; nt < 2; ++nt) {
      bias2[nt] = f32x4{bflo(bb2[2 * nt]), bfhi(bb2[2 * nt]), bflo(bb2[2 * nt + 1]), bfhi(bb2[2 * nt + 1])};
      asm volatile("" : "+v"(bias2[nt]));
    }
  }
  SB();
  STAMP(9);
  bar();
  {
    f32x4 acc[3][2];
    LAS unsigned char* hb = L + L_H1 + m * H1P + q * 16;
    const __amdgpu_buffer_rsrc_t outr = rsrc(out, N * 512);
    auto put = [&](int mt) __attribute__((always_inline)) {
      const int r = 16 * mt + m;
      v4u o;
      o.x = pack_bf16(acc[mt][0][0], acc[mt][0][1]); o.y = pack_bf16(acc[mt][0][2], acc[mt][0][3]);
      o.z = pack_bf16(acc[mt][1][0], acc[mt][1][1]); o.w = pack_bf16(acc[mt][1][2], acc[mt][1][3]);
      __builtin_amdgcn_raw_buffer_store_b128(o, outr, r < TM ? (unsigned)(node0 + r) * 512u + 64 * w + 16 * q : OOB, 0, OUTAUX);
    };
    constexpr int P2 = 8;
    v4u b[P2];
#define U2_KS(u) ((u) < 24 ? (u) / 3 : 8 + ((u) - 24) % 8)
#define U2_MT(u) ((u) < 24 ? (u) % 3 : ((u) - 24) / 8)
#pragma unroll
    for (int u = 0; u < P2; ++u) b[u] = *(LAS v4u*)(hb + U2_MT(u) * 16 * H1P + U2_KS(u) * 64);
    static_for<0, 48>([&](auto U) {
      constexpr int u = U, ks = U2_KS(u), mt = U2_MT(u);
      if constexpr (u % 12 == 0) PRIO(3 - u / 12);
      if constexpr (u < 16 && (u & 1) == 0) load_w2<24 + u / 2>(wsd, w2off);
      constexpr int issued = u < 16 ? 25 + u / 2 : 32;
      if constexpr (mt == 0) wait_vm<issued - (2 * ks + 2)>();
      if constexpr (ks == 0) { mfma_p0<w2reg(0)>(acc[mt][0], b[u % P2], bias2[0]); mfma_p0<w2reg(1)>(acc[mt][1], b[u % P2], bias2[1]); }
      else { mfma_p<w2reg(2 * ks)>(acc[mt][0], b[u % P2]); mfma_p<w2reg(2 * ks + 1)>(acc[mt][1], b[u % P2]); }
      if constexpr (u + P2 < 48) b[u % P2] = *(LAS v4u*)(hb + U2_MT(u + P2) * 16 * H1P + U2_KS(u + P2) * 64);
      if constexpr (u == 36 || u == 44) put(mt - 1);
      SB();
    });
    asm volatile("s_nop 7" : "+v"(acc[2][0]), "+v"(acc[2][1]));
    put(2);
  }
  if (!timedout && w == 3 && __hip_atomic_load((GAS u64*)(ws + WS_EPOCH), RLX_AGENT) != tag && lane == 0) __hip_atomic_store((GAS u64*)(ws + WS_EPOCH), tag, RLX_AGENT);
  STAMP(10);
  STAMP_OUT((unsigned long long*)(ws + (64u << 20)), blockIdx.x * 8 + w);
  asm volatile(".Lgin_end:");
}

extern "C" void kernel_launch(void* const* d_in, const int* in_sizes, int n_in, void* d_out, int out_size, void* d_ws, size_t ws_size,
                              hipStream_t stream) {
  const int N = in_sizes[0] / D, E = in_sizes[1];
  const int T = (N + TM - 1) / TM;
  (void)hipFuncSetAttribute((const void*)gin_one, hipFuncAttributeMaxDynamicSharedMemorySize, L_END);
  gin_one<<<T, 512, L_END, stream>>>((const unsigned short*)d_in[0], (const int*)d_in[1], (const int*)d_in[2], (const unsigned short*)d_in[3],
                                 (const unsigned short*)d_in[4], (const unsigned short*)d_in[5], (const unsigned short*)d_in[6],
                                 (const unsigned short*)d_in[7], (unsigned short*)d_out, (unsigned char*)d_ws, N, E, T);
}
```

```cpp
#include <hip/hip_runtime.h>
#include <hip/hip_bf16.h>
#include <stdint.h>
#include <type_traits>

typedef unsigned v4u __attribute__((ext_vector_type(4)));
typedef unsigned v2u __attribute__((ext_vector_type(2)));
typedef float f32x4 __attribute__((ext_vector_type(4)));
typedef float f32x2 __attribute__((ext_vector_type(2)));
typedef short bf16x8 __attribute__((ext_vector_type(8)));
typedef __bf16 bf16x2_t __attribute__((ext_vector_type(2)));
typedef unsigned long long u64;
#define GAS __attribute__((address_space(1)))
#define LAS __attribute__((address_space(3)))
#define DI __device__ __forceinline__
#define RLX_AGENT __ATOMIC_RELAXED, __HIP_MEMORY_SCOPE_AGENT
#define RLX_WG __ATOMIC_RELAXED, __HIP_MEMORY_SCOPE_WORKGROUP
#define SB() __builtin_amdgcn_sched_barrier(0)
#define PRIO(p) __builtin_amdgcn_s_setprio(p)
#define STAMP(i)
#define STAMP_DECL
#define STAMP_OUT(base, idx)

constexpr int D = 256, H = 512, TM = 40;
constexpr int EPB = 640;
constexpr int CAP = 1536;
constexpr int HP = 544, H1P = 1056;
constexpr unsigned OOB = 0x40000000u, EOOB = OOB >> 3;
#define RING 3
constexpr int R = RING;
constexpr int SC1 = 16;
constexpr unsigned SPIN_CAP = 4096;
#define FAIR 1
#define XWARM 1
#define XP_EARLY 2
#define GATE 0
#define SPLIT 0
#define CELL_MODE 1
#define PRETOUCH 0
#define ZPAD 0
#define CPFS 128
#define NYD 4
#define P2D 8
#define BPRIO 0
#define BSCHED 0
#define XPRIO 0
#define ESLEEP 3
#define PSLEEP 24
#define NB1 8
#define NB2 16
#define NA 16
#define NCN 5
#define OUTAUX 16
#define W2PRE 8
constexpr unsigned WS_EPOCH = 0, WS_WFLAG = 4096, WS_W2T = 65536, WS_CELL = 1u << 20, WS_BYTES = WS_CELL + 256u * 16384u;
constexpr int L_H = 0, L_H1 = L_H + 48 * HP, L_FLAT = L_H1 + 48 * H1P, L_CNT = L_FLAT + (CAP + 128) * 4, L_MISC = L_CNT + 256,
              L_B1 = L_MISC + 32, L_B2 = L_B1 + 2048, L_END = L_B2 + 512;
constexpr int L_PSTG = L_H;
constexpr int L_PCELL = L_H1, L_PCNT = L_H1 + 320 * 64;

DI unsigned pack_bf16(float a, float b) { f32x2 v = {a, b}; bf16x2_t r = __builtin_convertvector(v, bf16x2_t); return __builtin_bit_cast(unsigned, r); }
DI float bflo(unsigned u) { return __builtin_bit_cast(float, u << 16); }
DI float bfhi(unsigned u) { return __builtin_bit_cast(float, u & 0xffff0000u); }
DI float asf(unsigned u) { return __builtin_bit_cast(float, u); }
DI unsigned asu(float u) { return __builtin_bit_cast(unsigned, u); }
DI f32x4 mfma(v4u a, v4u b, f32x4 c) {
  return __builtin_amdgcn_mfma_f32_16x16x32_bf16(__builtin_bit_cast(bf16x8, a), __builtin_bit_cast(bf16x8, b), c, 0, 0, 0);
}
typedef short s16x2 __attribute__((ext_vector_type(2)));
DI unsigned relu_pack(float a, float b) {
  const s16x2 v = __builtin_bit_cast(s16x2, pack_bf16(a, b));
  return __builtin_bit_cast(unsigned, __builtin_elementwise_max(v, s16x2{0, 0}));
}
DI void bar() { asm volatile("s_waitcnt lgkmcnt(0)" ::: "memory"); __builtin_amdgcn_s_barrier(); asm volatile("" ::: "memory"); }
template <int CTRL> DI unsigned dpp_add(unsigned x) { return x + (unsigned)__builtin_amdgcn_update_dpp(0, (int)x, CTRL, 0xF, 0xF, false); }
DI unsigned wave_incl_scan(unsigned x) {
  x = dpp_add<0x111>(x); x = dpp_add<0x112>(x); x = dpp_add<0x114>(x); x = dpp_add<0x118>(x);
  x = x + (unsigned)__builtin_amdgcn_update_dpp(0, (int)x, 0x142, 0xA, 0xF, false);
  x = x + (unsigned)__builtin_amdgcn_update_dpp(0, (int)x, 0x143, 0xC, 0xF, false);
  return x;
}
DI unsigned bsum(unsigned a, unsigned b) { return __builtin_amdgcn_sad_u8(a, 0u, __builtin_amdgcn_sad_u8(b, 0u, 0u)); }
DI __amdgpu_buffer_rsrc_t rsrc(const void* p, unsigned bytes) { return __builtin_amdgcn_make_buffer_rsrc((void*)p, 0, bytes, 0x00020000); }
DI unsigned tag_b(u64 tag) { return ((unsigned)tag + (unsigned)(tag >> 24) * 0x9E3779B1u) & 0x7FFFFu; }

DI void tile_w2_load(__amdgpu_buffer_rsrc_t w2r, int f, int lane, unsigned (&v)[8]) {
  const int ww = f >> 5, ks = (f >> 1) & 15, nt = f & 1, i = lane & 15, kg = lane >> 4;
  const int qo = (2 * ks + (kg >> 1)) & 3;
  const int row0 = 64 * (ks >> 1) + 32 * (qo & 1) + 4 * (qo >> 1) + 2 * (kg & 1);
  const unsigned o = f < 256 ? (row0 * D + 32 * ww + 8 * (i >> 2) + 4 * nt + (i & 3)) * 2 : OOB;
#pragma unroll
  for (int j = 0; j < 8; ++j) v[j] = __builtin_amdgcn_raw_buffer_load_b16(w2r, o + (j >> 2) * D * 2, (j & 3) * 8 * D * 2, 0);
}
DI void tile_w2_store(__amdgpu_buffer_rsrc_t wsr, int f, int lane, const unsigned (&v)[8]) {
  const v4u o = {v[0] | (v[1] << 16), v[2] | (v[3] << 16), v[4] | (v[5] << 16), v[6] | (v[7] << 16)};
  __builtin_amdgcn_raw_buffer_store_b128(o, wsr, f < 256 ? WS_W2T + f * 1024 + lane * 16 : OOB, 0, SC1);
}
constexpr int PB = 96;
template <int G> DI void load_w1(v4u srd, unsigned off) {
  unsigned so;
  constexpr int r = PB + 32 + 32 * G;
  asm volatile("s_mov_b32 %0, %3\n\t"
               "buffer_load_dwordx4 v[%c5:%c5+3], %1, %2, %0 offen\n\tbuffer_load_dwordx4 v[%c5+4:%c5+7], %1, %2, %0 offen offset:1024\n\t"
               "buffer_load_dwordx4 v[%c5+8:%c5+11], %1, %2, %0 offen offset:2048\n\tbuffer_load_dwordx4 v[%c5+12:%c5+15], %1, %2, %0 offen offset:3072\n\t"
               "s_mov_b32 %0, %4\n\t"
               "buffer_load_dwordx4 v[%c5+16:%c5+19], %1, %2, %0 offen\n\tbuffer_load_dwordx4 v[%c5+20:%c5+23], %1, %2, %0 offen offset:1024\n\t"
               "buffer_load_dwordx4 v[%c5+24:%c5+27], %1, %2, %0 offen offset:2048\n\tbuffer_load_dwordx4 v[%c5+28:%c5+31], %1, %2, %0 offen offset:3072"
               : "=&s"(so) : "v"(off), "s"(srd), "i"(G * 65536), "i"(G * 65536 + 4096), "i"(r) : "v255");
}
template <int G> DI void xpose(unsigned lo, unsigned hi) {
#pragma unroll
  for (int d = 0; d < 4; ++d)
#pragma unroll
    for (int p = 0; p < 2; ++p) {
      unsigned t0, t1;
      asm volatile("v_perm_b32 v[%c5], v[%c4+4], v[%c4], %2\n\tv_perm_b32 v[%c5+16], v[%c4+6], v[%c4+2], %2\n\t"
                   "v_perm_b32 v[%c5+4], v[%c4+4], v[%c4], %3\n\tv_perm_b32 v[%c5+20], v[%c4+6], v[%c4+2], %3\n\t"
                   "v_mov_b32 %0, v[%c5]\n\tv_mov_b32 %1, v[%c5+4]\n\t"
                   "v_mov_b32_dpp v[%c5], v[%c5+16] row_ror:8 row_mask:0xf bank_mask:0xc\n\t"
                   "v_mov_b32_dpp v[%c5+4], v[%c5+20] row_ror:8 row_mask:0xf bank_mask:0xc\n\t"
                   "v_mov_b32_dpp v[%c5+16], %0 row_ror:8 row_mask:0xf bank_mask:0x3\n\t"
                   "v_mov_b32_dpp v[%c5+20], %1 row_ror:8 row_mask:0xf bank_mask:0x3"
                   : "=&v"(t0), "=&v"(t1) : "s"(lo), "s"(hi), "i"(PB + 32 + 32 * G + 8 * d + p), "i"(PB + 32 * G + 8 * p + d));
    }
}
DI void xpose_all() { xpose<0>(0x05040100u, 0x07060302u); xpose<1>(0x05040100u, 0x07060302u); xpose<2>(0x05040100u, 0x07060302u); xpose<3>(0x05040100u, 0x07060302u); }
constexpr int NY = NYD;
constexpr int w2reg(int i) { return i < 8 ? PB + 128 + 4 * i : PB + 4 * (i - 8 - NY); }
template <int I> DI void load_w2(v4u srd, unsigned off) {
  unsigned so;
  asm volatile("s_mov_b32 %0, %3\n\tbuffer_load_dwordx4 v[%c4:%c4+3], %1, %2, %0 offen offset:%c5 sc1"
               : "=&s"(so) : "v"(off), "s"(srd), "i"((I >> 2) * 4096), "i"(w2reg(I)), "i"((I & 3) * 1024));
}
template <int A> DI void mfma_p(f32x4& acc, v4u b) { asm volatile("v_mfma_f32_16x16x32_bf16 %0, v[%c2:%c2+3], %1, %0" : "+v"(acc) : "v"(b), "i"(A)); }
template <int A> DI void mfma_p0(f32x4& acc, v4u b, f32x4 c) {
  asm volatile("v_mfma_f32_16x16x32_bf16 %0, v[%c3:%c3+3], %1, %2" : "=&v"(acc) : "v"(b), "v"(c), "i"(A));
}
template <int AUX> DI void touch(v4u srd, unsigned off) {
  if constexpr (AUX) asm volatile("buffer_load_dword v[%c2], %0, %1, 0 offen sc1" :: "v"(off), "s"(srd), "i"(PB - 1));
  else asm volatile("buffer_load_dword v[%c2], %0, %1, 0 offen" :: "v"(off), "s"(srd), "i"(PB - 1));
}
DI void mfma_r(f32x4& acc, v4u a, v4u b) { asm volatile("v_mfma_f32_16x16x32_bf16 %0, %1, %2, %0" : "+v"(acc) : "v"(a), "v"(b)); }
DI void count_row(unsigned a) {
  const unsigned one = 1;
  asm volatile("s_mov_b64 exec, 1\n\tds_add_u32 %0, %1\n\ts_mov_b64 exec, -1" :: "v"(a), "v"(one) : "memory");
}
template <int N> DI void wait_vm() { asm volatile("s_waitcnt vmcnt(%c0)" :: "i"(N)); }
template <int I, int E, typename F> DI void static_for(F&& f) { if constexpr (I < E) { f(std::integral_constant<int, I>{}); static_for<I + 1, E>(f); } }

__global__ __launch_bounds__(512, 2) __attribute__((amdgpu_num_vgpr(PB / 2 - 1))) void gin_one(const unsigned short* __restrict__ x, const int* __restrict__ src, const int* __restrict__ dst,
                                                  const unsigned short* __restrict__ epsp, const unsigned short* __restrict__ w1,
                                                  const unsigned short* __restrict__ b1, const unsigned short* __restrict__ w2,
                                                  const unsigned short* __restrict__ b2, unsigned short* __restrict__ out, unsigned char* ws,
                                                  int N, int E, int T) {
  extern __shared__ __attribute__((aligned(16))) unsigned char Ls[];
  LAS unsigned char* L = (LAS unsigned char*)Ls;
  const int tid = threadIdx.x, lane = tid & 63, w = __builtin_amdgcn_readfirstlane(tid >> 6);
  const int t = blockIdx.x, node0 = t * TM;
  const unsigned bulk = w >> 2, chain = bulk ^ 1;
  const int rank = (t >> 3) & 31;
  {
    u64 pc; unsigned len;
    asm volatile("s_getpc_b64 %0\n.Lgin_pc:\n\ts_mov_b32 %1, .Lgin_end-.Lgin_pc" : "=s"(pc), "=s"(len));
    touch<0>(v4u{(unsigned)pc, (unsigned)(pc >> 32) & 0xffffu, len, 0x00020000u}, (rank * 8 + (tid >> 6)) * CPFS + lane * 32768);
  }
  SB();
  asm volatile("" :: "s"(x), "s"(src), "s"(dst), "s"(epsp), "s"(w1), "s"(b1), "s"(w2), "s"(b2), "s"(out), "s"(ws), "s"(N), "s"(E), "s"(T));
  const int share2 = (T >> 3) + rank;
  const __amdgpu_buffer_rsrc_t wsr = rsrc(ws, WS_BYTES), xr = rsrc(x, N * 512), w1r = rsrc(w1, D * H * 2), w2r = rsrc(w2, D * H * 2);
  STAMP_DECL STAMP(0);
  const int q = lane >> 4, m = lane & 15;
  const v4u w1d = {(unsigned)(u64)w1, (unsigned)((u64)w1 >> 32) & 0xffffu, D * H * 2, 0x00020000u};
  const v4u wsd = {(unsigned)(u64)ws, (unsigned)((u64)ws >> 32) & 0xffffu, WS_BYTES, 0x00020000u};
  const unsigned w1off = (((lane >> 3) & 1) * 32 + q * 8) * 1024 + w * 128 + (lane & 7) * 16;
  LAS unsigned* pcnt = (LAS unsigned*)(L + L_PCNT);
  LAS unsigned* pcell = (LAS unsigned*)(L + L_PCELL);

  const int tf = t + (w - 1) * T;
  if (w == 4) {
    PRIO(3);
    const __amdgpu_buffer_rsrc_t dr = rsrc(dst, E * 4), sr = rsrc(src, E * 4);
    v4u dv[3], sv[3];
#pragma unroll
    for (int i = 0; i < 3; ++i) {
      const unsigned o = (i == 2 && lane >= 32) ? OOB : (unsigned)(t * EPB + (lane + 64 * i) * 4) * 4u;
      dv[i] = __builtin_amdgcn_raw_buffer_load_b128(dr, o, 0, 0);
      sv[i] = __builtin_amdgcn_raw_buffer_load_b128(sr, o, 0, 0);
    }
    const u64 ep0 = __hip_atomic_load((GAS u64*)(ws + WS_EPOCH), RLX_AGENT);
    SB();
    *(LAS v4u*)(pcnt + lane * 4) = v4u{0, 0, 0, 0};
    pcnt[256 + lane] = 0;
    const unsigned dummy = 256u + lane;
    unsigned tt[12], rk[12];
#pragma unroll
    for (int i = 0; i < 12; ++i) {
      const int e = t * EPB + (lane + 64 * (i >> 2)) * 4;
      const unsigned lo = (e < E && !(i >= 8 && lane >= 32)) ? 0u : dummy;
      tt[i] = max(min(__umul24(min(dv[i >> 2][i & 3], 16383u), 52429u) >> 21, dummy), lo);
      rk[i] = __hip_atomic_fetch_add(pcnt + tt[i], 1u, RLX_WG);
    }
#pragma unroll
    for (int i = 0; i < 12; ++i)
      pcell[tt[i] * 16 + (rk[i] & 15u)] = (dv[i >> 2][i & 3] - tt[i] * TM) | (min(sv[i >> 2][i & 3], 16383u) << 6);
    *(LAS u64*)(L + L_MISC) = ep0;
  } else {
  __builtin_amdgcn_s_sleep(ESLEEP);
  if (w == 0 || (w == 5 && share2 < 32)) touch<0>(w1d, (w ? share2 : rank) * 8192 + lane * 128);
   if (w == 3) {
    const v4u v1 = __builtin_amdgcn_raw_buffer_load_b128(rsrc(b1, H * 2), lane * 16, 0, 0);
    const v4u v2 = __builtin_amdgcn_raw_buffer_load_b128(rsrc(b2, D * 2), lane * 16, 0, 0);
    const unsigned ev = epsp[0];
    *(LAS unsigned*)(L + L_CNT + lane * 4) = 0;
#pragma unroll
    for (int i = 0; i < 8; ++i)
      *(LAS float*)(L + L_B1 + ((lane >> 3) * 64 + (((lane >> 2) & 1) + 2 * (i >> 2)) * 16 + 4 * (i & 3) + (lane & 3)) * 4) = (i & 1) ? bfhi(v1[i >> 1]) : bflo(v1[i >> 1]);
    *(LAS v4u*)(L + (lane < 32 ? L_B2 + lane * 16 : L_FLAT + lane * 16)) = v2;
    *(LAS v2u*)(L + L_MISC + 8) = v2u{0u, ev};
    *(LAS v4u*)(L + L_MISC + 16) = v4u{0u, 0u, 0u, 0u};
   } else if (w == 1 || w == 2) {
    unsigned tv[8];
    tile_w2_load(w2r, tf, lane, tv);
    tile_w2_store(wsr, tf, lane, tv);
   }
  }
  if (chain) { load_w1<0>(w1d, w1off); load_w1<1>(w1d, w1off); }
  PRIO(3);
  bar();
  STAMP(1);
  const u64 tag = *(LAS u64*)(L + L_MISC) + 1;
  const unsigned tagA = (unsigned)tag & 0xFFFFFFu, tagB = tag_b(tag);
  {
    v4u e[2]; unsigned c[2];
#pragma unroll
    for (int i = 0; i < 2; ++i) { e[i] = *(LAS v4u*)(pcell + (w * 2 + i) * 256 + lane * 4); c[i] = pcnt[(w * 2 + i) * 16 + (lane >> 2)]; }
#pragma unroll
    for (int i = 0; i < 2; ++i) {
      const unsigned tc = (w * 2 + i) * 16 + (lane >> 2), cc = min(c[i], 31u);
      const v4u g = {(e[i].x & 0xFFFFFu) | (e[i].y << 20), ((e[i].y >> 12) & 0xFFu) | (tagA << 8), (e[i].z & 0xFFFFFu) | (e[i].w << 20),
                     ((e[i].w >> 12) & 0xFFu) | (cc << 8) | (tagB << 13)};
      __builtin_amdgcn_raw_buffer_store_b128(g, wsr, tc < (unsigned)T ? WS_CELL + tc * 16384 + t * 64 + (lane & 3) * 16 : OOB, 0, SC1);
    }
  }
  SB();
  PRIO(1);
  if (chain) { load_w1<2>(w1d, w1off); load_w1<3>(w1d, w1off); } else __builtin_amdgcn_s_sleep(PSLEEP);
  STAMP(2);
  bool any2 = false;
  unsigned ent[16], rk[16], spins = 0;
  LAS unsigned* flat = (LAS unsigned*)(L + L_FLAT);
  if (chain) {
    PRIO(2);
    v4u g[4];
    const unsigned coff = WS_CELL + t * 16384 + w * 4096 + lane * 16;
    constexpr unsigned CSTEP = 1024;
#pragma unroll
    for (int i = 0; i < 4; ++i) g[i] = __builtin_amdgcn_raw_buffer_load_b128(wsr, coff + i * CSTEP, 0, SC1);
    bool tmo = false;
    const int pl = T - 64 * w - (lane >> 2);
#define GR_OK(i) (16 * (i) >= pl || ((g[i].y >> 8) == tagA && (g[i].w >> 13) == tagB))
    if (!__all(GR_OK(0) && GR_OK(1) && GR_OK(2) && GR_OK(3))) {
      for (;;) {
#pragma unroll
        for (int i = 0; i < 4; ++i) {
          const bool ok = GR_OK(i);
          const v4u nv = __builtin_amdgcn_raw_buffer_load_b128(wsr, ok ? OOB : coff + i * CSTEP, 0, SC1);
#pragma unroll
          for (int k = 0; k < 4; ++k) g[i][k] = ok ? g[i][k] : nv[k];
        }
        if (__all(GR_OK(0) && GR_OK(1) && GR_OK(2) && GR_OK(3))) break;
        if (++spins > SPIN_CAP) { tmo = true; break; }
      }
      ++spins;
    }
    STAMP(3);
    if (lane == 0) __hip_atomic_fetch_add((LAS unsigned*)(L + L_MISC + 16), 1u, RLX_WG);
    LAS unsigned char* stg = L + L_PSTG + w * 5120;
#pragma unroll
    for (int i = 0; i < 4; ++i) *(LAS v4u*)(stg + (16 * i + (lane >> 2)) * 80 + (lane & 3) * 16) = g[i];
#pragma unroll
    for (int i = 0; i < 4; ++i) g[i] = *(LAS v4u*)(stg + lane * 80 + i * 16);
    const int nval = (64 * w + lane < T && !tmo) ? (int)((g[0].w >> 8) & 31u) : 0;
    any2 = __any(nval > 8);
    bool bad = nval > 16;
    const unsigned dummy = 40u + (unsigned)(lane % 24);
    auto rank = [&](int lo) __attribute__((always_inline)) {
#pragma unroll
      for (int i = lo; i < lo + 8; ++i) {
        const unsigned a = g[i >> 2][i & 2], b = g[i >> 2][(i & 2) + 1];
        ent[i] = i < nval ? ((i & 1) ? __builtin_amdgcn_alignbit(b, a, 20) : a) & 0xFFFFFu : dummy;
        rk[i] = __hip_atomic_fetch_add((LAS unsigned*)(L + L_CNT + ((ent[i] & 63u) << 2)), 1u << (8 * w), RLX_WG);
      }
#pragma unroll
      for (int i = lo; i < lo + 8; ++i) { rk[i] = (rk[i] >> (8 * w)) & 255u; bad |= rk[i] == 255u; }
    };
    rank(0);
    if (__builtin_expect(any2, 0)) rank(8);
    if ((__any(bad) || tmo) && lane == 0) __hip_atomic_fetch_or((LAS unsigned*)(L + L_MISC + 8), tmo ? 3u : 1u, RLX_WG);
  } else {
    PRIO(0);
    load_w1<0>(w1d, w1off); load_w1<1>(w1d, w1off);
    wait_vm<8>(); xpose<0>(0x05040100u, 0x07060302u);
    wait_vm<0>(); xpose<1>(0x05040100u, 0x07060302u);
    load_w1<2>(w1d, w1off);
    load_w1<3>(w1d, w1off);
    if (w < 7) touch<0>(v4u{(unsigned)(u64)x, (unsigned)((u64)x >> 32) & 0xffffu, (unsigned)N * 512u, 0x00020000u},
                        (w - 4) * 64 + lane < 160 ? node0 * 512 + ((w - 4) * 64 + lane) * 128 : OOB);
    else touch<0>(w1d, OOB);
  }
  bar();
  STAMP(4);
  bool slow, timedout;
  int s0, nit;
  u64 pk = 0;
  const int n0 = bulk ? (10 - NCN) * w + (8 * NCN - 40) : NCN * w;
  const int nn = bulk ? 10 - NCN : NCN;
  {
    const unsigned cw = *(LAS unsigned*)(L + L_CNT + lane * 4);
    const unsigned flagv = __builtin_amdgcn_readfirstlane(*(LAS unsigned*)(L + L_MISC + 8));
    const unsigned deg = __builtin_amdgcn_sad_u8(cw, 0u, 0u), below = __builtin_amdgcn_sad_u8(cw & ((1u << (8 * (w & 3))) - 1u), 0u, 0u);
    const unsigned l4 = (deg + 4u) & ~3u;
    const bool lastn = lane < 4 * NCN ? (lane % NCN) == NCN - 1 : ((lane - 4 * NCN) % (10 - NCN)) == 9 - NCN;
    const unsigned plen = lane < TM ? l4 + (lastn ? 4u * (R - 1) : 0u) : 0u;
    const unsigned incl = wave_incl_scan(plen), start = incl - plen;
    timedout = (flagv & 2u) != 0u;
    slow = flagv != 0u || __builtin_amdgcn_readlane(incl, 63) > (unsigned)CAP;
    const unsigned tail = __builtin_amdgcn_readlane(incl, n0 + nn - 1) - 4 * (R - 1);
    s0 = __builtin_amdgcn_readlane(start, n0);
    nit = ((tail - s0) / 4 + R - 1) / R;
#pragma unroll
    for (int j = 0; j < 5; ++j) pk |= (u64)(((unsigned)__builtin_amdgcn_readlane(l4, n0 + j) >> 2) & 0xFFFu) << (12 * j);
    if (slow) s0 = 0;
    if (!slow) {
      const unsigned st2 = (unsigned)__builtin_amdgcn_ds_bpermute((n0 + (lane >> 3)) << 2, (int)(start | (deg << 16)));
      if (chain) {
        const unsigned B4 = L_FLAT + 4u * (lane < TM ? start + 1u + below : CAP + 64u);
        auto place = [&](int lo) __attribute__((always_inline)) {
          unsigned b[8];
#pragma unroll
          for (int i = 0; i < 8; ++i) b[i] = (unsigned)__builtin_amdgcn_ds_bpermute((int)((ent[lo + i] & 63u) << 2), (int)B4);
#pragma unroll
          for (int i = 0; i < 8; ++i) *(LAS unsigned*)(L + b[i] + 4u * rk[lo + i]) = ent[lo + i];
        };
        place(0);
        if (__builtin_expect(any2, 0)) place(8);
      }
      const unsigned dg = st2 >> 16, k = lane & 7, pp = k == 7 ? 0u : dg + 1u + k;
      const bool mine = (lane >> 3) < nn && pp < ((dg + 4u) & ~3u);
      const unsigned at = mine ? (st2 & 0xFFFFu) + pp : (lane >= 48 && lane - 48 < 4 * (R - 1) ? tail + lane - 48 : CAP + lane);
      flat[at] = (mine && k == 7) ? (unsigned)(node0 + n0 + (lane >> 3)) << 6 : EOOB;
    }
  }
  if (bulk) {
    wait_vm<9>(); xpose<2>(0x05040100u, 0x07060302u);
  }
  bar();
  STAMP(5);
  PRIO(2);
  if (w == 1 || w == 2) {
    asm volatile("s_waitcnt vmcnt(0)" ::: "memory");
    if (lane == 0 && tf < 256) __hip_atomic_store((GAS unsigned*)(ws + WS_WFLAG) + tf, (unsigned)tag, RLX_AGENT);
  }
  const unsigned l16 = m * 16;
  LAS unsigned* fp = flat + s0 + q;
  v4u bl[R], br[R];
  unsigned en[R];
#pragma unroll
  for (int i = 0; i < R; ++i) en[i] = fp[4 * i];
  const float epsf = bflo(*(LAS unsigned*)(L + L_MISC + 12));
#pragma unroll
  for (int i = 0; i < R; ++i) {
    const unsigned o = ((en[i] << 3) & ~511u) | l16;
    bl[i] = __builtin_amdgcn_raw_buffer_load_b128(xr, o, 0, 0);
    br[i] = __builtin_amdgcn_raw_buffer_load_b128(xr, o + 256, 0, 0);
    en[i] = fp[4 * (R + i)];
  }
  asm volatile("" ::: "memory");
  SB();
  if (chain) { PRIO(XPRIO); xpose<0>(0x05040100u, 0x07060302u); xpose<1>(0x05040100u, 0x07060302u); xpose<2>(0x05040100u, 0x07060302u); }
  wait_vm<2 * R + 1>();
  xpose<3>(0x05040100u, 0x07060302u);
  if (chain) PRIO(0); else PRIO(BPRIO);
  STAMP(6);
  v4u fl0;
  int stage = 0;
  bool xdone = false;
  const unsigned w2off = WS_W2T + w * 32768 + lane * 16;
  auto gemm1 = [&](int mt, auto LAST) __attribute__((always_inline)) {
    LAS unsigned char* hb = L + L_H + (mt * 16 + m) * HP + q * 16;
    LAS unsigned char* b1p = L + L_B1 + (64 * w + 16 * q) * 4;
    v4u b[3];
    f32x4 acc[4];
#pragma unroll
    for (int i = 0; i < 3; ++i) b[i] = *(LAS v4u*)(hb + i * 64);
#pragma unroll
    for (int c = 0; c < 4; ++c) acc[c] = *(LAS f32x4*)(b1p + 16 * c);
    static_for<0, 8>([&](auto KS) {
      constexpr int ks = KS;
      if constexpr (LAST && (ks & 1) == 0) PRIO(3 - ks / 2);
      static_for<0, 4>([&](auto C) { mfma_p<PB + 16 * ks + 4 * C>(acc[C], b[ks % 3]); });
      if constexpr (ks + 3 < 8) b[ks % 3] = *(LAS v4u*)(hb + (ks + 3) * 64);
      if constexpr (LAST) load_w2<8 + NY + ks>(wsd, w2off);
      SB();
    });
    asm volatile("s_nop 7" : "+v"(acc[0]), "+v"(acc[1]), "+v"(acc[2]), "+v"(acc[3]));
    LAS unsigned char* h1w = L + L_H1 + (mt * 16 + m) * H1P + (64 * w + 16 * q) * 2;
#pragma unroll
    for (int half = 0; half < 2; ++half) {
      unsigned o[4];
#pragma unroll
      for (int k = 0; k < 4; ++k) o[k] = relu_pack(acc[2 * half + (k >> 1)][2 * (k & 1)], acc[2 * half + (k >> 1)][2 * (k & 1) + 1]);
      *(LAS v4u*)(h1w + 16 * half) = v4u{o[0], o[1], o[2], o[3]};
    }
  };
  auto wait_rows = [&](int mt) __attribute__((always_inline)) {
    while (__builtin_amdgcn_readfirstlane(__hip_atomic_load((LAS unsigned*)(L + L_MISC + 20 + 4 * mt), RLX_WG)) < (mt < 2 ? 16u : 8u))
      __builtin_amdgcn_s_sleep(1);
    asm volatile("" ::: "memory");
  };
  auto early_stage = [&]() __attribute__((always_inline)) {
    if (stage == 0) {
      asm volatile("buffer_load_dwordx4 v[%c2:%c2+3], %0, %1, 0 offen sc1" :: "v"(WS_WFLAG + lane * 16), "s"(wsd), "i"(PB + 156));
    } else {
      unsigned d0, d1;
      asm volatile("v_xor_b32 %0, %2, v[%c3]\n\tv_xor_b32 %1, %2, v[%c3+1]\n\tv_or_b32 %0, %0, %1\n\tv_xor_b32 %1, %2, v[%c3+2]\n\t"
                   "v_or_b32 %0, %0, %1\n\tv_xor_b32 %1, %2, v[%c3+3]\n\tv_or_b32 %0, %0, %1"
                   : "=&v"(d0), "=&v"(d1) : "s"((unsigned)tag), "i"(PB + 156));
      xdone = __all(d0 == 0u);
      if (xdone)
      static_for<0, 8>([&](auto I) { load_w2<I>(wsd, w2off); });
    }
    gemm1(stage ? 0 : 2, std::false_type{});
    ++stage;
  };
  if (__builtin_expect(!slow, 1)) {
    const v4u sel = {m == 0 ? 0x3F80u : (m == 1 ? 0x3F800000u : 0u), m == 2 ? 0x3F80u : (m == 3 ? 0x3F800000u : 0u),
                     m == 4 ? 0x3F80u : (m == 5 ? 0x3F800000u : 0u), m == 6 ? 0x3F80u : (m == 7 ? 0x3F800000u : 0u)};
    const v4u self = {sel.x | (lane == 8 ? 0x3F80u : (lane == 9 ? 0x3F800000u : 0u)), sel.y | (lane == 10 ? 0x3F80u : (lane == 11 ? 0x3F800000u : 0u)),
                      sel.z | (lane == 12 ? 0x3F80u : (lane == 13 ? 0x3F800000u : 0u)), sel.w | (lane == 14 ? 0x3F80u : (lane == 15 ? 0x3F800000u : 0u))};
    v4u cur = self;
    fp += 8 * R;
    f32x4 a0 = {0.f, 0.f, 0.f, 0.f}, a1 = {0.f, 0.f, 0.f, 0.f};
    const unsigned hl = L_H + (lane >> 5) * 256 + m * 16 + (q & 1) * 8;
    unsigned rows = (32 + w) | ((2 * w) << 6) | ((2 * w + 1) << 12) | ((16 + 2 * w) << 18) | ((17 + 2 * w) << 24);
    unsigned cv = 0;
    bool seen = chain;
    int rem = (int)(pk & 0xFFF);
#define SLOT(i, REFILL) { \
        a0 = mfma(cur, bl[i], a0); \
        a1 = mfma(cur, br[i], a1); \
        cur = sel; \
        if (REFILL) { \
          const unsigned o = ((en[i] << 3) & ~511u) | l16; \
          bl[i] = __builtin_amdgcn_raw_buffer_load_b128(xr, o, 0, 0); \
          br[i] = __builtin_amdgcn_raw_buffer_load_b128(xr, o + 256, 0, 0); \
          en[i] = fp[4 * i]; \
        } \
        if (__builtin_expect(--rem == 0, 0)) { \
          float f[4]; \
          _Pragma("unroll") for (int r = 0; r < 4; ++r) { \
            auto s2 = __builtin_amdgcn_permlane32_swap(asu(a0[r]), asu(a1[r]), false, false); \
            const unsigned u0 = s2[0], u1 = s2[1]; \
            f[r] = fmaf(epsf, asf(u1), asf(u0)); \
          } \
          *(LAS v2u*)(L + hl + (rows & 63u) * HP) = v2u{pack_bf16(f[0], f[1]), pack_bf16(f[2], f[3])}; \
          count_row(L_MISC + 20 + ((rows & 48u) >> 2));       \
          rows >>= 6; \
          a0 = f32x4{0.f, 0.f, 0.f, 0.f}; a1 = a0; \
          cur = self; \
          pk >>= 12; \
          rem = (int)(pk & 0xFFF); \
        } }
    if (w == 6 || (w == 7 && share2 < 32)) touch<1>(wsd, WS_W2T + (w == 7 ? share2 : rank) * 8192 + lane * 128);
    for (; nit > 1; --nit) {
#pragma unroll
      for (int i = 0; i < R; ++i) SLOT(i, true)
      fp += 4 * R;
      if (stage < 2) {
        const bool full = __builtin_amdgcn_readfirstlane(cv) >= (stage ? 16u : 8u);
        if (full && seen) { asm volatile("" ::: "memory"); early_stage(); seen = chain; }
        else seen |= full;
        cv = __hip_atomic_load((LAS unsigned*)(L + L_MISC + 20 + (stage ? 0 : 8)), RLX_WG);
      }
    }
#pragma unroll
    for (int i = 0; i < R; ++i) SLOT(i, false)
  } else {
#pragma unroll 1
    for (int j = 0; j < nn; ++j) {
      const int node = node0 + n0 + j;
      const v2u s2 = __builtin_amdgcn_raw_buffer_load_b64(xr, (unsigned)node * 512u + lane * 8, 0, 0);
      float a0 = bflo(s2.x), a1 = bfhi(s2.x), a2 = bflo(s2.y), a3 = bfhi(s2.y);
      if (node < N)
        for (int eb = 0; eb < E; eb += 64) {
          const int e = eb + lane;
          const int dv2 = (int)__builtin_amdgcn_raw_buffer_load_b32(rsrc(dst, E * 4), e * 4, 0, 0);
          const int sv2 = (int)__builtin_amdgcn_raw_buffer_load_b32(rsrc(src, E * 4), e * 4, 0, 0);
          u64 mkk = __ballot(dv2 == node && e < E);
          while (mkk) {
            const int b = __builtin_ctzll(mkk); mkk &= mkk - 1;
            const unsigned sidx = (unsigned)__builtin_amdgcn_readlane(sv2, b);
            const v2u rv = __builtin_amdgcn_raw_buffer_load_b64(xr, sidx < (unsigned)N ? sidx * 512u + lane * 8 : OOB, 0, 0);
            a0 += bflo(rv.x); a1 += bfhi(rv.x); a2 += bflo(rv.y); a3 += bfhi(rv.y);
          }
        }
      const int row = j ? ((j - 1) >> 1) * 16 + 2 * w + ((j - 1) & 1) : 32 + w;
      *(LAS v2u*)(L + L_H + row * HP + lane * 8) =
          v2u{pack_bf16(fmaf(epsf, bflo(s2.x), a0), fmaf(epsf, bfhi(s2.x), a1)), pack_bf16(fmaf(epsf, bflo(s2.y), a2), fmaf(epsf, bfhi(s2.y), a3))};
      count_row(L_MISC + 20 + (row >> 4) * 4);
    }
  }
  STAMP(7);
  PRIO(0);
  if (!timedout && tid == 192 && t == 0) __hip_atomic_store((GAS u64*)(ws + WS_EPOCH), tag, RLX_AGENT);
#pragma unroll 1
  while (stage < 2) { wait_rows(stage ? 0 : 2); wait_vm<0>(); early_stage(); }
  if (__builtin_expect(!xdone, 0)) {
    bool okw = false;
    fl0 = __builtin_amdgcn_raw_buffer_load_b128(wsr, WS_WFLAG + lane * 16, 0, SC1);
    const unsigned tl = (unsigned)tag;
    for (unsigned sp = 0; sp <= SPIN_CAP; ++sp) {
      if (__all(fl0.x == tl && fl0.y == tl && fl0.z == tl && fl0.w == tl)) { okw = true; break; }
      fl0 = __builtin_amdgcn_raw_buffer_load_b128(wsr, WS_WFLAG + lane * 16, 0, SC1);
    }
    if (__builtin_expect(!okw, 0)) {
#pragma unroll 1
      for (int i = 0; i < 32; ++i) { unsigned tv[8]; tile_w2_load(w2r, w * 32 + i, lane, tv); tile_w2_store(wsr, w * 32 + i, lane, tv); }
      asm volatile("s_waitcnt vmcnt(0)" ::: "memory");
    }
    __builtin_amdgcn_fence(__ATOMIC_ACQUIRE, "wavefront");
    static_for<0, 8>([&](auto I) { load_w2<I>(wsd, w2off); });
  }
  v4u wy[NY];
#pragma unroll
  for (int i = 0; i < NY; ++i) wy[i] = __builtin_amdgcn_raw_buffer_load_b128(wsr, w2off + (i & 3) * 1024, (2 + (i >> 2)) * 4096, SC1);
  SB();
  const v4u bb2 = *(LAS v4u*)(L + L_B2 + (32 * w + 8 * q) * 2);
  wait_rows(1);
  STAMP(8);
#pragma unroll
  for (int i = 0; i < NY; ++i) asm volatile("" : "+v"(wy[i]));
  gemm1(1, std::true_type{});
  f32x4 bias2[2];
  {
#pragma unroll
    for (int nt = 0; nt < 2; ++nt) {
      bias2[nt] = f32x4{bflo(bb2[2 * nt]), bfhi(bb2[2 * nt]), bflo(bb2[2 * nt + 1]), bfhi(bb2[2 * nt + 1])};
      asm volatile("" : "+v"(bias2[nt]));
    }
  }
  SB();
  STAMP(9);
  bar();
  {
    f32x4 acc[3][2];
    LAS unsigned char* hb = L + L_H1 + m * H1P + q * 16;
    const __amdgpu_buffer_rsrc_t outr = rsrc(out, N * 512);
    auto put = [&](int mt) __attribute__((always_inline)) {
      const int r = mt < 2 ? 5 * (m >> 1) + 1 + 2 * mt + (m & 1) : (m < 8 ? 5 * m : TM);
      v4u o;
      o.x = pack_bf16(acc[mt][0][0], acc[mt][0][1]); o.y = pack_bf16(acc[mt][0][2], acc[mt][0][3]);
      o.z = pack_bf16(acc[mt][1][0], acc[mt][1][1]); o.w = pack_bf16(acc[mt][1][2], acc[mt][1][3]);
      __builtin_amdgcn_raw_buffer_store_b128(o, outr, r < TM ? (unsigned)(node0 + r) * 512u + 64 * w + 16 * q : OOB, 0, OUTAUX);
    };
    constexpr int P2 = P2D;
    v4u b[P2];
#define U2_KS(u) ((u) < 24 ? (u) / 3 : 8 + ((u) - 24) % 8)
#define U2_MT(u) ((u) < 24 ? (u) % 3 : ((u) - 24) / 8)
#pragma unroll
    for (int u = 0; u < P2; ++u) b[u] = *(LAS v4u*)(hb + U2_MT(u) * 16 * H1P + U2_KS(u) * 64);
    static_for<0, 48>([&](auto U) {
      constexpr int u = U, ks = U2_KS(u), mt = U2_MT(u);
      if constexpr (u % 12 == 0) PRIO(3 - u / 12);
      constexpr int F0 = 16 + NY;
      if constexpr (u < 2 * (32 - F0) && (u & 1) == 0) load_w2<F0 + u / 2>(wsd, w2off);
      constexpr int issued = u < 2 * (32 - F0) ? F0 + 1 + u / 2 : 32;
      if constexpr (mt == 0 && 2 * ks >= 8 + NY) wait_vm<issued - (2 * ks + 2)>();
      static_for<0, 2>([&](auto NT) {
        constexpr int f = 2 * ks + NT;
        if constexpr (ks == 0) mfma_p0<w2reg(f)>(acc[mt][NT], b[u % P2], bias2[NT]);
        else if constexpr (f >= 8 && f < 8 + NY) mfma_r(acc[mt][NT], wy[f - 8], b[u % P2]);
        else mfma_p<w2reg(f)>(acc[mt][NT], b[u % P2]);
      });
      if constexpr (u + P2 < 48) b[u % P2] = *(LAS v4u*)(hb + U2_MT(u + P2) * 16 * H1P + U2_KS(u + P2) * 64);
      if constexpr (u == 36 || u == 44) put(mt - 1);
      SB();
    });
    asm volatile("s_nop 7" : "+v"(acc[2][0]), "+v"(acc[2][1]));
    put(2);
  }
  if (!timedout && w == 3 && __hip_atomic_load((GAS u64*)(ws + WS_EPOCH), RLX_AGENT) != tag && lane == 0) __hip_atomic_store((GAS u64*)(ws + WS_EPOCH), tag, RLX_AGENT);
  STAMP(10);
  STAMP_OUT((unsigned long long*)(ws + (64u << 20)), blockIdx.x * 8 + w);
  asm volatile(".Lgin_end:");
}

extern "C" void kernel_launch(void* const* d_in, const int* in_sizes, int n_in, void* d_out, int out_size, void* d_ws, size_t ws_size,
                              hipStream_t stream) {
  const int N = in_sizes[0] / D, E = in_sizes[1];
  const int T = (N + TM - 1) / TM;
  (void)hipFuncSetAttribute((const void*)gin_one, hipFuncAttributeMaxDynamicSharedMemorySize, L_END);
  gin_one<<<T, 512, L_END, stream>>>((const unsigned short*)d_in[0], (const int*)d_in[1], (const int*)d_in[2], (const unsigned short*)d_in[3],
                                 (const unsigned short*)d_in[4], (const unsigned short*)d_in[5], (const unsigned short*)d_in[6],
                                 (const unsigned short*)d_in[7], (unsigned short*)d_out, (unsigned char*)d_ws, N, E, T);
}
```

```cpp
#define TLBW 0
#include <hip/hip_runtime.h>
#include <hip/hip_bf16.h>
#include <stdint.h>
#include <type_traits>

typedef unsigned v4u __attribute__((ext_vector_type(4)));
typedef unsigned v2u __attribute__((ext_vector_type(2)));
typedef float f32x4 __attribute__((ext_vector_type(4)));
typedef float f32x2 __attribute__((ext_vector_type(2)));
typedef short bf16x8 __attribute__((ext_vector_type(8)));
typedef __bf16 bf16x2_t __attribute__((ext_vector_type(2)));
typedef unsigned long long u64;
#define GAS __attribute__((address_space(1)))
#define LAS __attribute__((address_space(3)))
#define DI __device__ __forceinline__
#define RLX_AGENT __ATOMIC_RELAXED, __HIP_MEMORY_SCOPE_AGENT
#define RLX_WG __ATOMIC_RELAXED, __HIP_MEMORY_SCOPE_WORKGROUP
#define SB() __builtin_amdgcn_sched_barrier(0)
#define PRIO(p) __builtin_amdgcn_s_setprio(p)
#define STAMP(i)
#define STAMP_DECL
#define STAMP_OUT(base, idx)

constexpr int D = 256, H = 512, TM = 40;
constexpr int EPB = 640;
constexpr int CAP = 1536;
constexpr int HP = 544, H1P = 1056;
constexpr unsigned OOB = 0x40000000u, EOOB = OOB >> 3;
#define RING 3
constexpr int R = RING;
constexpr int SC1 = 16;
constexpr unsigned SPIN_CAP = 4096;
#define FAIR 1
#define XWARM 1
#define XP_EARLY 2
#define GATE 0
#define SPLIT 0
#define CELL_MODE 1
#define PRETOUCH 0
#define ZPAD 0
#define CPFS 128
#define NYD 4
#define P2D 8
#define STAG 0
#define BPRIO 0
#define BSCHED 0
#define XPRIO 0
#define ESLEEP 3
#define PSLEEP 24
#define NB1 8
#define NB2 16
#define NA 16
#define NCN 5
#define OUTAUX 16
#define W2PRE 8
constexpr unsigned WS_EPOCH = 0, WS_WFLAG = 4096, WS_W2T = 65536, WS_CELL = 1u << 20, WS_BYTES = WS_CELL + 256u * 16384u;
constexpr int L_H = 0, L_H1 = L_H + 48 * HP, L_FLAT = L_H1 + 48 * H1P, L_CNT = L_FLAT + (CAP + 128) * 4, L_MISC = L_CNT + 256,
              L_B1 = L_MISC + 32, L_B2 = L_B1 + 2048, L_END = L_B2 + 512;
constexpr int L_PSTG = L_H;
constexpr int L_PCELL = L_H1, L_PCNT = L_H1 + 320 * 64;

DI unsigned pack_bf16(float a, float b) { f32x2 v = {a, b}; bf16x2_t r = __builtin_convertvector(v, bf16x2_t); return __builtin_bit_cast(unsigned, r); }
DI float bflo(unsigned u) { return __builtin_bit_cast(float, u << 16); }
DI float bfhi(unsigned u) { return __builtin_bit_cast(float, u & 0xffff0000u); }
DI float asf(unsigned u) { return __builtin_bit_cast(float, u); }
DI unsigned asu(float u) { return __builtin_bit_cast(unsigned, u); }
DI f32x4 mfma(v4u a, v4u b, f32x4 c) {
  return __builtin_amdgcn_mfma_f32_16x16x32_bf16(__builtin_bit_cast(bf16x8, a), __builtin_bit_cast(bf16x8, b), c, 0, 0, 0);
}
typedef short s16x2 __attribute__((ext_vector_type(2)));
DI unsigned relu_pack(float a, float b) {
  const s16x2 v = __builtin_bit_cast(s16x2, pack_bf16(a, b));
  return __builtin_bit_cast(unsigned, __builtin_elementwise_max(v, s16x2{0, 0}));
}
DI void bar() { asm volatile("s_waitcnt lgkmcnt(0)" ::: "memory"); __builtin_amdgcn_s_barrier(); asm volatile("" ::: "memory"); }
template <int CTRL> DI unsigned dpp_add(unsigned x) { return x + (unsigned)__builtin_amdgcn_update_dpp(0, (int)x, CTRL, 0xF, 0xF, false); }
DI unsigned wave_incl_scan(unsigned x) {
  x = dpp_add<0x111>(x); x = dpp_add<0x112>(x); x = dpp_add<0x114>(x); x = dpp_add<0x118>(x);
  x = x + (unsigned)__builtin_amdgcn_update_dpp(0, (int)x, 0x142, 0xA, 0xF, false);
  x = x + (unsigned)__builtin_amdgcn_update_dpp(0, (int)x, 0x143, 0xC, 0xF, false);
  return x;
}
DI unsigned bsum(unsigned a, unsigned b) { return __builtin_amdgcn_sad_u8(a, 0u, __builtin_amdgcn_sad_u8(b, 0u, 0u)); }
DI __amdgpu_buffer_rsrc_t rsrc(const void* p, unsigned bytes) { return __builtin_amdgcn_make_buffer_rsrc((void*)p, 0, bytes, 0x00020000); }
DI unsigned tag_b(u64 tag) { return ((unsigned)tag + (unsigned)(tag >> 24) * 0x9E3779B1u) & 0x7FFFFu; }

DI void tile_w2_load(__amdgpu_buffer_rsrc_t w2r, int f, int lane, unsigned (&v)[8]) {
  const int ww = f >> 5, ks = (f >> 1) & 15, nt = f & 1, i = lane & 15, kg = lane >> 4;
  const int qo = (2 * ks + (kg >> 1)) & 3;
  const int row0 = 64 * (ks >> 1) + 32 * (qo & 1) + 4 * (qo >> 1) + 2 * (kg & 1);
  const unsigned o = f < 256 ? (row0 * D + 32 * ww + 8 * (i >> 2) + 4 * nt + (i & 3)) * 2 : OOB;
#pragma unroll
  for (int j = 0; j < 8; ++j) v[j] = __builtin_amdgcn_raw_buffer_load_b16(w2r, o + (j >> 2) * D * 2, (j & 3) * 8 * D * 2, 0);
}
DI void tile_w2_store(__amdgpu_buffer_rsrc_t wsr, int f, int lane, const unsigned (&v)[8]) {
  const v4u o = {v[0] | (v[1] << 16), v[2] | (v[3] << 16), v[4] | (v[5] << 16), v[6] | (v[7] << 16)};
  __builtin_amdgcn_raw_buffer_store_b128(o, wsr, f < 256 ? WS_W2T + f * 1024 + lane * 16 : OOB, 0, SC1);
}
constexpr int PB = 96;
template <int G> DI void load_w1(v4u srd, unsigned off) {
  unsigned so;
  constexpr int r = PB + 32 + 32 * G;
  asm volatile("s_mov_b32 %0, %3\n\t"
               "buffer_load_dwordx4 v[%c5:%c5+3], %1, %2, %0 offen\n\tbuffer_load_dwordx4 v[%c5+4:%c5+7], %1, %2, %0 offen offset:1024\n\t"
               "buffer_load_dwordx4 v[%c5+8:%c5+11], %1, %2, %0 offen offset:2048\n\tbuffer_load_dwordx4 v[%c5+12:%c5+15], %1, %2, %0 offen offset:3072\n\t"
               "s_mov_b32 %0, %4\n\t"
               "buffer_load_dwordx4 v[%c5+16:%c5+19], %1, %2, %0 offen\n\tbuffer_load_dwordx4 v[%c5+20:%c5+23], %1, %2, %0 offen offset:1024\n\t"
               "buffer_load_dwordx4 v[%c5+24:%c5+27], %1, %2, %0 offen offset:2048\n\tbuffer_load_dwordx4 v[%c5+28:%c5+31], %1, %2, %0 offen offset:3072"
               : "=&s"(so) : "v"(off), "s"(srd), "i"(G * 65536), "i"(G * 65536 + 4096), "i"(r) : "v255");
}
template <int G> DI void xpose(unsigned lo, unsigned hi) {
#pragma unroll
  for (int d = 0; d < 4; ++d)
#pragma unroll
    for (int p = 0; p < 2; ++p) {
      unsigned t0, t1;
      asm volatile("v_perm_b32 v[%c5], v[%c4+4], v[%c4], %2\n\tv_perm_b32 v[%c5+16], v[%c4+6], v[%c4+2], %2\n\t"
                   "v_perm_b32 v[%c5+4], v[%c4+4], v[%c4], %3\n\tv_perm_b32 v[%c5+20], v[%c4+6], v[%c4+2], %3\n\t"
                   "v_mov_b32 %0, v[%c5]\n\tv_mov_b32 %1, v[%c5+4]\n\t"
                   "v_mov_b32_dpp v[%c5], v[%c5+16] row_ror:8 row_mask:0xf bank_mask:0xc\n\t"
                   "v_mov_b32_dpp v[%c5+4], v[%c5+20] row_ror:8 row_mask:0xf bank_mask:0xc\n\t"
                   "v_mov_b32_dpp v[%c5+16], %0 row_ror:8 row_mask:0xf bank_mask:0x3\n\t"
                   "v_mov_b32_dpp v[%c5+20], %1 row_ror:8 row_mask:0xf bank_mask:0x3"
                   : "=&v"(t0), "=&v"(t1) : "s"(lo), "s"(hi), "i"(PB + 32 + 32 * G + 8 * d + p), "i"(PB + 32 * G + 8 * p + d));
    }
}
DI void xpose_all() { xpose<0>(0x05040100u, 0x07060302u); xpose<1>(0x05040100u, 0x07060302u); xpose<2>(0x05040100u, 0x07060302u); xpose<3>(0x05040100u, 0x07060302u); }
constexpr int NY = NYD;
constexpr int w2reg(int i) { return i < 8 ? PB + 128 + 4 * i : PB + 4 * (i - 8 - NY); }
template <int I> DI void load_w2(v4u srd, unsigned off) {
  unsigned so;
  asm volatile("s_mov_b32 %0, %3\n\tbuffer_load_dwordx4 v[%c4:%c4+3], %1, %2, %0 offen offset:%c5 sc1"
               : "=&s"(so) : "v"(off), "s"(srd), "i"((I >> 2) * 4096), "i"(w2reg(I)), "i"((I & 3) * 1024));
}
template <int A> DI void mfma_p(f32x4& acc, v4u b) { asm volatile("v_mfma_f32_16x16x32_bf16 %0, v[%c2:%c2+3], %1, %0" : "+v"(acc) : "v"(b), "i"(A)); }
template <int A> DI void mfma_p0(f32x4& acc, v4u b, f32x4 c) {
  asm volatile("v_mfma_f32_16x16x32_bf16 %0, v[%c3:%c3+3], %1, %2" : "=&v"(acc) : "v"(b), "v"(c), "i"(A));
}
template <int AUX> DI void touch(v4u srd, unsigned off) {
  if constexpr (AUX) asm volatile("buffer_load_dword v[%c2], %0, %1, 0 offen sc1" :: "v"(off), "s"(srd), "i"(PB - 1));
  else asm volatile("buffer_load_dword v[%c2], %0, %1, 0 offen" :: "v"(off), "s"(srd), "i"(PB - 1));
}
DI void mfma_r(f32x4& acc, v4u a, v4u b) { asm volatile("v_mfma_f32_16x16x32_bf16 %0, %1, %2, %0" : "+v"(acc) : "v"(a), "v"(b)); }
DI void count_row(unsigned a) {
  const unsigned one = 1;
  asm volatile("s_mov_b64 exec, 1\n\tds_add_u32 %0, %1\n\ts_mov_b64 exec, -1" :: "v"(a), "v"(one) : "memory");
}
template <int N> DI void wait_vm() { asm volatile("s_waitcnt vmcnt(%c0)" :: "i"(N)); }
template <int I, int E, typename F> DI void static_for(F&& f) { if constexpr (I < E) { f(std::integral_constant<int, I>{}); static_for<I + 1, E>(f); } }

__global__ __launch_bounds__(512, 2) __attribute__((amdgpu_num_vgpr(PB / 2 - 1))) void gin_one(const unsigned short* __restrict__ x, const int* __restrict__ src, const int* __restrict__ dst,
                                                  const unsigned short* __restrict__ epsp, const unsigned short* __restrict__ w1,
                                                  const unsigned short* __restrict__ b1, const unsigned short* __restrict__ w2,
                                                  const unsigned short* __restrict__ b2, unsigned short* __restrict__ out, unsigned char* ws,
                                                  int N, int E, int T) {
  extern __shared__ __attribute__((aligned(16))) unsigned char Ls[];
  LAS unsigned char* L = (LAS unsigned char*)Ls;
  const int tid = threadIdx.x, lane = tid & 63, w = __builtin_amdgcn_readfirstlane(tid >> 6);
  const int t = blockIdx.x, node0 = t * TM;
  const unsigned bulk = w >> 2, chain = bulk ^ 1;
  const int rank = (t >> 3) & 31;
  {
    u64 pc; unsigned len;
    asm volatile("s_getpc_b64 %0\n.Lgin_pc:\n\ts_mov_b32 %1, .Lgin_end-.Lgin_pc" : "=s"(pc), "=s"(len));
    touch<0>(v4u{(unsigned)pc, (unsigned)(pc >> 32) & 0xffffu, len, 0x00020000u}, (rank * 8 + (tid >> 6)) * CPFS + lane * 32768);
  }
  SB();
  asm volatile("" :: "s"(x), "s"(src), "s"(dst), "s"(epsp), "s"(w1), "s"(b1), "s"(w2), "s"(b2), "s"(out), "s"(ws), "s"(N), "s"(E), "s"(T));
  const int share2 = (T >> 3) + rank;
  const __amdgpu_buffer_rsrc_t wsr = rsrc(ws, WS_BYTES), xr = rsrc(x, N * 512), w1r = rsrc(w1, D * H * 2), w2r = rsrc(w2, D * H * 2);
  STAMP_DECL STAMP(0);
  const int q = lane >> 4, m = lane & 15;
  const v4u w1d = {(unsigned)(u64)w1, (unsigned)((u64)w1 >> 32) & 0xffffu, D * H * 2, 0x00020000u};
  const v4u wsd = {(unsigned)(u64)ws, (unsigned)((u64)ws >> 32) & 0xffffu, WS_BYTES, 0x00020000u};
  const unsigned w1off = (((lane >> 3) & 1) * 32 + q * 8) * 1024 + w * 128 + (lane & 7) * 16;
  LAS unsigned* pcnt = (LAS unsigned*)(L + L_PCNT);
  LAS unsigned* pcell = (LAS unsigned*)(L + L_PCELL);

  const int tf = t + (w - 1) * T;
  if (w == 4) {
    PRIO(3);
    const __amdgpu_buffer_rsrc_t dr = rsrc(dst, E * 4), sr = rsrc(src, E * 4);
    v4u dv[3], sv[3];
#pragma unroll
    for (int i = 0; i < 3; ++i) {
      const unsigned o = (i == 2 && lane >= 32) ? OOB : (unsigned)(t * EPB + (lane + 64 * i) * 4) * 4u;
      dv[i] = __builtin_amdgcn_raw_buffer_load_b128(dr, o, 0, 0);
      sv[i] = __builtin_amdgcn_raw_buffer_load_b128(sr, o, 0, 0);
    }
    const u64 ep0 = __hip_atomic_load((GAS u64*)(ws + WS_EPOCH), RLX_AGENT);
    SB();
    *(LAS v4u*)(pcnt + lane * 4) = v4u{0, 0, 0, 0};
    pcnt[256 + lane] = 0;
    const unsigned dummy = 256u + lane;
    unsigned tt[12], rk[12];
#pragma unroll
    for (int i = 0; i < 12; ++i) {
      const int e = t * EPB + (lane + 64 * (i >> 2)) * 4;
      const unsigned lo = (e < E && !(i >= 8 && lane >= 32)) ? 0u : dummy;
      tt[i] = max(min(__umul24(min(dv[i >> 2][i & 3], 16383u), 52429u) >> 21, dummy), lo);
      rk[i] = __hip_atomic_fetch_add(pcnt + tt[i], 1u, RLX_WG);
    }
#pragma unroll
    for (int i = 0; i < 12; ++i)
      pcell[tt[i] * 16 + (rk[i] & 15u)] = (dv[i >> 2][i & 3] - tt[i] * TM) | (min(sv[i >> 2][i & 3], 16383u) << 6);
    *(LAS u64*)(L + L_MISC) = ep0;
  } else {
  __builtin_amdgcn_s_sleep(ESLEEP);
  if (w == 0 || (w == 5 && share2 < 32)) touch<0>(w1d, (w ? share2 : rank) * 8192 + lane * 128);
   if (w == 3) {
    const v4u v1 = __builtin_amdgcn_raw_buffer_load_b128(rsrc(b1, H * 2), lane * 16, 0, 0);
    const v4u v2 = __builtin_amdgcn_raw_buffer_load_b128(rsrc(b2, D * 2), lane * 16, 0, 0);
    const unsigned ev = epsp[0];
    *(LAS unsigned*)(L + L_CNT + lane * 4) = 0;
#pragma unroll
    for (int i = 0; i < 8; ++i)
      *(LAS float*)(L + L_B1 + ((lane >> 3) * 64 + (((lane >> 2) & 1) + 2 * (i >> 2)) * 16 + 4 * (i & 3) + (lane & 3)) * 4) = (i & 1) ? bfhi(v1[i >> 1]) : bflo(v1[i >> 1]);
    *(LAS v4u*)(L + (lane < 32 ? L_B2 + lane * 16 : L_FLAT + lane * 16)) = v2;
    *(LAS v2u*)(L + L_MISC + 8) = v2u{0u, ev};
    *(LAS v4u*)(L + L_MISC + 16) = v4u{0u, 0u, 0u, 0u};
   } else if (w == 1 || w == 2) {
    unsigned tv[8];
    tile_w2_load(w2r, tf, lane, tv);
    tile_w2_store(wsr, tf, lane, tv);
   }
  }
  if (chain) { load_w1<0>(w1d, w1off); load_w1<1>(w1d, w1off); }
  PRIO(3);
  bar();
  STAMP(1);
  const u64 tag = *(LAS u64*)(L + L_MISC) + 1;
  const unsigned tagA = (unsigned)tag & 0xFFFFFFu, tagB = tag_b(tag);
  {
    v4u e[2]; unsigned c[2];
#pragma unroll
    for (int i = 0; i < 2; ++i) { e[i] = *(LAS v4u*)(pcell + (w * 2 + i) * 256 + lane * 4); c[i] = pcnt[(w * 2 + i) * 16 + (lane >> 2)]; }
#pragma unroll
    for (int i = 0; i < 2; ++i) {
      const unsigned tc = (w * 2 + i) * 16 + (lane >> 2), cc = min(c[i], 31u);
      const v4u g = {(e[i].x & 0xFFFFFu) | (e[i].y << 20), ((e[i].y >> 12) & 0xFFu) | (tagA << 8), (e[i].z & 0xFFFFFu) | (e[i].w << 20),
                     ((e[i].w >> 12) & 0xFFu) | (cc << 8) | (tagB << 13)};
      __builtin_amdgcn_raw_buffer_store_b128(g, wsr, tc < (unsigned)T ? WS_CELL + tc * 16384 + t * 64 + (lane & 3) * 16 : OOB, 0, SC1);
    }
  }
  SB();
  PRIO(1);
  if (chain) { load_w1<2>(w1d, w1off); load_w1<3>(w1d, w1off); } else __builtin_amdgcn_s_sleep(PSLEEP);
  STAMP(2);
  bool any2 = false;
  unsigned ent[16], rk[16], spins = 0;
  LAS unsigned* flat = (LAS unsigned*)(L + L_FLAT);
  if (chain) {
    PRIO(2);
    v4u g[4];
    const unsigned coff = WS_CELL + t * 16384 + w * 4096 + lane * 16;
    constexpr unsigned CSTEP = 1024;
#pragma unroll
    for (int i = 0; i < 4; ++i) g[i] = __builtin_amdgcn_raw_buffer_load_b128(wsr, coff + i * CSTEP, 0, SC1);
    bool tmo = false;
    const int pl = T - 64 * w - (lane >> 2);
#define GR_OK(i) (16 * (i) >= pl || ((g[i].y >> 8) == tagA && (g[i].w >> 13) == tagB))
    if (!__all(GR_OK(0) && GR_OK(1) && GR_OK(2) && GR_OK(3))) {
      for (;;) {
#pragma unroll
        for (int i = 0; i < 4; ++i) {
          const bool ok = GR_OK(i);
          const v4u nv = __builtin_amdgcn_raw_buffer_load_b128(wsr, ok ? OOB : coff + i * CSTEP, 0, SC1);
#pragma unroll
          for (int k = 0; k < 4; ++k) g[i][k] = ok ? g[i][k] : nv[k];
        }
        if (__all(GR_OK(0) && GR_OK(1) && GR_OK(2) && GR_OK(3))) break;
        if (++spins > SPIN_CAP) { tmo = true; break; }
      }
      ++spins;
    }
    STAMP(3);
    if (lane == 0) __hip_atomic_fetch_add((LAS unsigned*)(L + L_MISC + 16), 1u, RLX_WG);
    LAS unsigned char* stg = L + L_PSTG + w * 5120;
#pragma unroll
    for (int i = 0; i < 4; ++i) *(LAS v4u*)(stg + (16 * i + (lane >> 2)) * 80 + (lane & 3) * 16) = g[i];
#pragma unroll
    for (int i = 0; i < 4; ++i) g[i] = *(LAS v4u*)(stg + lane * 80 + i * 16);
    const int nval = (64 * w + lane < T && !tmo) ? (int)((g[0].w >> 8) & 31u) : 0;
    any2 = __any(nval > 8);
    bool bad = nval > 16;
    const unsigned dummy = 40u + (unsigned)(lane % 24);
    auto rank = [&](int lo) __attribute__((always_inline)) {
#pragma unroll
      for (int i = lo; i < lo + 8; ++i) {
        const unsigned a = g[i >> 2][i & 2], b = g[i >> 2][(i & 2) + 1];
        ent[i] = i < nval ? ((i & 1) ? __builtin_amdgcn_alignbit(b, a, 20) : a) & 0xFFFFFu : dummy;
        rk[i] = __hip_atomic_fetch_add((LAS unsigned*)(L + L_CNT + ((ent[i] & 63u) << 2)), 1u << (8 * w), RLX_WG);
      }
#pragma unroll
      for (int i = lo; i < lo + 8; ++i) { rk[i] = (rk[i] >> (8 * w)) & 255u; bad |= rk[i] == 255u; }
    };
    rank(0);
    if (__builtin_expect(any2, 0)) rank(8);
    if ((__any(bad) || tmo) && lane == 0) __hip_atomic_fetch_or((LAS unsigned*)(L + L_MISC + 8), tmo ? 3u : 1u, RLX_WG);
  } else {
    PRIO(0);
    load_w1<0>(w1d, w1off); load_w1<1>(w1d, w1off);
    wait_vm<8>(); xpose<0>(0x05040100u, 0x07060302u);
    wait_vm<0>(); xpose<1>(0x05040100u, 0x07060302u);
    load_w1<2>(w1d, w1off);
    load_w1<3>(w1d, w1off);
    if (w < 7) touch<0>(v4u{(unsigned)(u64)x, (unsigned)((u64)x >> 32) & 0xffffu, (unsigned)N * 512u, 0x00020000u},
                        (w - 4) * 64 + lane < 160 ? node0 * 512 + ((w - 4) * 64 + lane) * 128 : OOB);
    else touch<0>(w1d, OOB);
  }
  bar();
  STAMP(4);
  bool slow, timedout;
  int s0, nit;
  u64 pk = 0;
  const int n0 = bulk ? (10 - NCN) * w + (8 * NCN - 40) : NCN * w;
  const int nn = bulk ? 10 - NCN : NCN;
  {
    const unsigned cw = *(LAS unsigned*)(L + L_CNT + lane * 4);
    const unsigned flagv = __builtin_amdgcn_readfirstlane(*(LAS unsigned*)(L + L_MISC + 8));
    const unsigned deg = __builtin_amdgcn_sad_u8(cw, 0u, 0u), below = __builtin_amdgcn_sad_u8(cw & ((1u << (8 * (w & 3))) - 1u), 0u, 0u);
    const unsigned l4 = (deg + 4u) & ~3u;
    const bool lastn = lane < 4 * NCN ? (lane % NCN) == NCN - 1 : ((lane - 4 * NCN) % (10 - NCN)) == 9 - NCN;
    const unsigned plen = lane < TM ? l4 + (lastn ? 4u * (R - 1) : 0u) : 0u;
    const unsigned incl = wave_incl_scan(plen), start = incl - plen;
    timedout = (flagv & 2u) != 0u;
    slow = flagv != 0u || __builtin_amdgcn_readlane(incl, 63) > (unsigned)CAP;
    const unsigned tail = __builtin_amdgcn_readlane(incl, n0 + nn - 1) - 4 * (R - 1);
    s0 = __builtin_amdgcn_readlane(start, n0);
    nit = ((tail - s0) / 4 + R - 1) / R;
#pragma unroll
    for (int j = 0; j < 5; ++j) pk |= (u64)(((unsigned)__builtin_amdgcn_readlane(l4, n0 + j) >> 2) & 0xFFFu) << (12 * j);
    if (slow) s0 = 0;
    if (!slow) {
      const unsigned st2 = (unsigned)__builtin_amdgcn_ds_bpermute((n0 + (lane >> 3)) << 2, (int)(start | (deg << 16)));
      if (chain) {
        const unsigned B4 = L_FLAT + 4u * (lane < TM ? start + 1u + below : CAP + 64u);
        auto place = [&](int lo) __attribute__((always_inline)) {
          unsigned b[8];
#pragma unroll
          for (int i = 0; i < 8; ++i) b[i] = (unsigned)__builtin_amdgcn_ds_bpermute((int)((ent[lo + i] & 63u) << 2), (int)B4);
#pragma unroll
          for (int i = 0; i < 8; ++i) *(LAS unsigned*)(L + b[i] + 4u * rk[lo + i]) = ent[lo + i];
        };
        place(0);
        if (__builtin_expect(any2, 0)) place(8);
      }
      const unsigned dg = st2 >> 16, k = lane & 7, pp = k == 7 ? 0u : dg + 1u + k;
      const bool mine = (lane >> 3) < nn && pp < ((dg + 4u) & ~3u);
      const unsigned at = mine ? (st2 & 0xFFFFu) + pp : (lane >= 48 && lane - 48 < 4 * (R - 1) ? tail + lane - 48 : CAP + lane);
      flat[at] = (mine && k == 7) ? (unsigned)(node0 + n0 + (lane >> 3)) << 6 : EOOB;
    }
  }
  if (bulk) {
    wait_vm<9>(); xpose<2>(0x05040100u, 0x07060302u);
  }
  bar();
  STAMP(5);
  PRIO(2);
  if (w == 1 || w == 2) {
    asm volatile("s_waitcnt vmcnt(0)" ::: "memory");
    if (lane == 0 && tf < 256) __hip_atomic_store((GAS unsigned*)(ws + WS_WFLAG) + tf, (unsigned)tag, RLX_AGENT);
  }
  const unsigned l16 = m * 16;
  LAS unsigned* fp = flat + s0 + q;
  v4u bl[R], br[R];
  unsigned en[R];
#pragma unroll
  for (int i = 0; i < R; ++i) en[i] = fp[4 * i];
  const float epsf = bflo(*(LAS unsigned*)(L + L_MISC + 12));
#pragma unroll
  for (int i = 0; i < R; ++i) {
    const unsigned o = ((en[i] << 3) & ~511u) | l16;
    bl[i] = __builtin_amdgcn_raw_buffer_load_b128(xr, o, 0, 0);
    br[i] = __builtin_amdgcn_raw_buffer_load_b128(xr, o + 256, 0, 0);
    en[i] = fp[4 * (R + i)];
  }
  asm volatile("" ::: "memory");
  SB();
  if (chain) { PRIO(XPRIO); xpose<0>(0x05040100u, 0x07060302u); xpose<1>(0x05040100u, 0x07060302u); xpose<2>(0x05040100u, 0x07060302u); }
  wait_vm<2 * R + 1>();
  xpose<3>(0x05040100u, 0x07060302u);
  if (chain) PRIO(0); else PRIO(BPRIO);
  STAMP(6);
  v4u fl0;
  int stage = 0;
  bool xdone = false;
  const unsigned w2off = WS_W2T + w * 32768 + lane * 16;
  v4u gb[2];
  f32x4 gacc[4];
  unsigned ghb = 0;
  auto gemm1 = [&](int mt, auto LAST, auto PART) __attribute__((always_inline)) {
    constexpr int part = PART;
    if constexpr (part == 0) {
      ghb = L_H + (mt * 16 + m) * HP + q * 16;
#pragma unroll
      for (int i = 0; i < 2; ++i) gb[i] = *(LAS v4u*)(L + ghb + i * 64);
#pragma unroll
      for (int c = 0; c < 4; ++c) gacc[c] = *(LAS f32x4*)(L + L_B1 + (64 * w + 16 * q) * 4 + 16 * c);
    }
    static_for<3 * part, (part == 2 ? 8 : 3 * part + 3)>([&](auto KS) {
      constexpr int ks = KS;
      if constexpr (LAST && (ks & 1) == 0) PRIO(3 - ks / 2);
      static_for<0, 4>([&](auto C) { mfma_p<PB + 16 * ks + 4 * C>(gacc[C], gb[ks & 1]); });
      if constexpr (ks + 2 < 8) gb[ks & 1] = *(LAS v4u*)(L + ghb + (ks + 2) * 64);
      if constexpr (LAST) load_w2<8 + NY + ks>(wsd, w2off);
      SB();
    });
    if constexpr (part == 2) {
      asm volatile("s_nop 7" : "+v"(gacc[0]), "+v"(gacc[1]), "+v"(gacc[2]), "+v"(gacc[3]));
      LAS unsigned char* h1w = L + L_H1 + (mt * 16 + m) * H1P + (64 * w + 16 * q) * 2;
#pragma unroll
      for (int half = 0; half < 2; ++half) {
        unsigned o[4];
#pragma unroll
        for (int k = 0; k < 4; ++k) o[k] = relu_pack(gacc[2 * half + (k >> 1)][2 * (k & 1)], gacc[2 * half + (k >> 1)][2 * (k & 1) + 1]);
        *(LAS v4u*)(h1w + 16 * half) = v4u{o[0], o[1], o[2], o[3]};
      }
    }
  };
  typedef std::integral_constant<int, 0> I0; typedef std::integral_constant<int, 1> I1; typedef std::integral_constant<int, 2> I2;
  auto wait_rows = [&](int mt) __attribute__((always_inline)) {
    while (__builtin_amdgcn_readfirstlane(__hip_atomic_load((LAS unsigned*)(L + L_MISC + 20 + 4 * mt), RLX_WG)) < (mt < 2 ? 16u : 8u))
      __builtin_amdgcn_s_sleep(1);
    asm volatile("" ::: "memory");
  };
  auto early_head = [&]() __attribute__((always_inline)) {
    if (stage == 0) {
      asm volatile("buffer_load_dwordx4 v[%c2:%c2+3], %0, %1, 0 offen sc1" :: "v"(WS_WFLAG + lane * 16), "s"(wsd), "i"(PB + 156));
    } else {
      unsigned d0, d1;
      asm volatile("v_xor_b32 %0, %2, v[%c3]\n\tv_xor_b32 %1, %2, v[%c3+1]\n\tv_or_b32 %0, %0, %1\n\tv_xor_b32 %1, %2, v[%c3+2]\n\t"
                   "v_or_b32 %0, %0, %1\n\tv_xor_b32 %1, %2, v[%c3+3]\n\tv_or_b32 %0, %0, %1"
                   : "=&v"(d0), "=&v"(d1) : "s"((unsigned)tag), "i"(PB + 156));
      xdone = __all(d0 == 0u);
      if (xdone)
      static_for<0, 8>([&](auto I) { load_w2<I>(wsd, w2off); });
    }
  };
  if (__builtin_expect(!slow, 1)) {
    const v4u sel = {m == 0 ? 0x3F80u : (m == 1 ? 0x3F800000u : 0u), m == 2 ? 0x3F80u : (m == 3 ? 0x3F800000u : 0u),
                     m == 4 ? 0x3F80u : (m == 5 ? 0x3F800000u : 0u), m == 6 ? 0x3F80u : (m == 7 ? 0x3F800000u : 0u)};
#define SELF(k) (unsigned)__builtin_amdgcn_update_dpp((int)sel[k], (int)sel[k], 0x128, 0x1, 0xC, false)
    v4u cur = {SELF(0), SELF(1), SELF(2), SELF(3)};
    fp += 8 * R;
    f32x4 a0 = {0.f, 0.f, 0.f, 0.f}, a1 = {0.f, 0.f, 0.f, 0.f};
    const unsigned hl = L_H + (lane >> 5) * 256 + m * 16 + (q & 1) * 8;
    unsigned rows = (32 + w) | ((2 * w) << 6) | ((2 * w + 1) << 12) | ((16 + 2 * w) << 18) | ((17 + 2 * w) << 24);
    unsigned cv = 0;
    bool seen = chain | !STAG;
    int rem = (int)(pk & 0xFFF);
#define SLOT(i, REFILL) { \
        a0 = mfma(cur, bl[i], a0); \
        a1 = mfma(cur, br[i], a1); \
        cur = sel; \
        if (REFILL) { \
          const unsigned o = ((en[i] << 3) & ~511u) | l16; \
          bl[i] = __builtin_amdgcn_raw_buffer_load_b128(xr, o, 0, 0); \
          br[i] = __builtin_amdgcn_raw_buffer_load_b128(xr, o + 256, 0, 0); \
          en[i] = fp[4 * i]; \
        } \
        if (__builtin_expect(--rem == 0, 0)) { \
          float f[4]; \
          _Pragma("unroll") for (int r = 0; r < 4; ++r) { \
            auto s2 = __builtin_amdgcn_permlane32_swap(asu(a0[r]), asu(a1[r]), false, false); \
            const unsigned u0 = s2[0], u1 = s2[1]; \
            f[r] = fmaf(epsf, asf(u1), asf(u0)); \
          } \
          *(LAS v2u*)(L + hl + (rows & 63u) * HP) = v2u{pack_bf16(f[0], f[1]), pack_bf16(f[2], f[3])}; \
          count_row(L_MISC + 20 + ((rows & 48u) >> 2));       \
          rows >>= 6; \
          a0 = f32x4{0.f, 0.f, 0.f, 0.f}; a1 = a0; \
          cur = v4u{SELF(0), SELF(1), SELF(2), SELF(3)}; \
          pk >>= 12; \
          rem = (int)(pk & 0xFFF); \
        } }
    if (w == 6 || (w == 7 && share2 < 32)) touch<1>(wsd, WS_W2T + (w == 7 ? share2 : rank) * 8192 + lane * 128);
    static_assert(R == 3, "GEMM1 comes in three parts, one behind each slot of a round");
    for (; nit > 1; --nit) {
      bool go = false;
      if (stage < 2) {
        const bool full = __builtin_amdgcn_readfirstlane(cv) >= (stage ? 16u : 8u);
        go = full && seen;
        seen = go ? (bool)(chain | !STAG) : seen || full;
      }
      asm volatile("" ::: "memory");
      SLOT(0, true)
      if (go) { early_head(); gemm1(stage ? 0 : 2, std::false_type{}, I0{}); }
      SLOT(1, true)
      if (go) gemm1(0, std::false_type{}, I1{});
      SLOT(2, true)
      if (go) { gemm1(stage ? 0 : 2, std::false_type{}, I2{}); ++stage; }
      fp += 4 * R;
      cv = __hip_atomic_load((LAS unsigned*)(L + L_MISC + 20 + (stage ? 0 : 8)), RLX_WG);
    }
#pragma unroll
    for (int i = 0; i < R; ++i) SLOT(i, false)
  } else {
#pragma unroll 1
    for (int j = 0; j < nn; ++j) {
      const int node = node0 + n0 + j;
      const v2u s2 = __builtin_amdgcn_raw_buffer_load_b64(xr, (unsigned)node * 512u + lane * 8, 0, 0);
      float a0 = bflo(s2.x), a1 = bfhi(s2.x), a2 = bflo(s2.y), a3 = bfhi(s2.y);
      if (node < N)
        for (int eb = 0; eb < E; eb += 64) {
          const int e = eb + lane;
          const int dv2 = (int)__builtin_amdgcn_raw_buffer_load_b32(rsrc(dst, E * 4), e * 4, 0, 0);
          const int sv2 = (int)__builtin_amdgcn_raw_buffer_load_b32(rsrc(src, E * 4), e * 4, 0, 0);
          u64 mkk = __ballot(dv2 == node && e < E);
          while (mkk) {
            const int b = __builtin_ctzll(mkk); mkk &= mkk - 1;
            const unsigned sidx = (unsigned)__builtin_amdgcn_readlane(sv2, b);
            const v2u rv = __builtin_amdgcn_raw_buffer_load_b64(xr, sidx < (unsigned)N ? sidx * 512u + lane * 8 : OOB, 0, 0);
            a0 += bflo(rv.x); a1 += bfhi(rv.x); a2 += bflo(rv.y); a3 += bfhi(rv.y);
          }
        }
      const int row = j ? ((j - 1) >> 1) * 16 + 2 * w + ((j - 1) & 1) : 32 + w;
      *(LAS v2u*)(L + L_H + row * HP + lane * 8) =
          v2u{pack_bf16(fmaf(epsf, bflo(s2.x), a0), fmaf(epsf, bfhi(s2.x), a1)), pack_bf16(fmaf(epsf, bflo(s2.y), a2), fmaf(epsf, bfhi(s2.y), a3))};
      count_row(L_MISC + 20 + (row >> 4) * 4);
    }
  }
  STAMP(7);
  PRIO(0);
  if (!timedout && tid == 192 && t == 0) __hip_atomic_store((GAS u64*)(ws + WS_EPOCH), tag, RLX_AGENT);
#pragma unroll 1
  while (stage < 2) { wait_rows(stage ? 0 : 2); wait_vm<0>(); early_head();
    gemm1(stage ? 0 : 2, std::false_type{}, I0{}); gemm1(0, std::false_type{}, I1{}); gemm1(stage ? 0 : 2, std::false_type{}, I2{}); ++stage; }
  if (__builtin_expect(!xdone, 0)) {
    bool okw = false;
    fl0 = __builtin_amdgcn_raw_buffer_load_b128(wsr, WS_WFLAG + lane * 16, 0, SC1);
    const unsigned tl = (unsigned)tag;
    for (unsigned sp = 0; sp <= SPIN_CAP; ++sp) {
      if (__all(fl0.x == tl && fl0.y == tl && fl0.z == tl && fl0.w == tl)) { okw = true; break; }
      fl0 = __builtin_amdgcn_raw_buffer_load_b128(wsr, WS_WFLAG + lane * 16, 0, SC1);
    }
    if (__builtin_expect(!okw, 0)) {
#pragma unroll 1
      for (int i = 0; i < 32; ++i) { unsigned tv[8]; tile_w2_load(w2r, w * 32 + i, lane, tv); tile_w2_store(wsr, w * 32 + i, lane, tv); }
      asm volatile("s_waitcnt vmcnt(0)" ::: "memory");
    }
    __builtin_amdgcn_fence(__ATOMIC_ACQUIRE, "wavefront");
    static_for<0, 8>([&](auto I) { load_w2<I>(wsd, w2off); });
  }
  v4u wy[NY];
#pragma unroll
  for (int i = 0; i < NY; ++i) wy[i] = __builtin_amdgcn_raw_buffer_load_b128(wsr, w2off + (i & 3) * 1024, (2 + (i >> 2)) * 4096, SC1);
  SB();
  const v4u bb2 = *(LAS v4u*)(L + L_B2 + (32 * w + 8 * q) * 2);
  wait_rows(1);
  STAMP(8);
#pragma unroll
  for (int i = 0; i < NY; ++i) asm volatile("" : "+v"(wy[i]));
  gemm1(1, std::true_type{}, I0{}); gemm1(1, std::true_type{}, I1{}); gemm1(1, std::true_type{}, I2{});
  f32x4 bias2[2];
  {
#pragma unroll
    for (int nt = 0; nt < 2; ++nt) {
      bias2[nt] = f32x4{bflo(bb2[2 * nt]), bfhi(bb2[2 * nt]), bflo(bb2[2 * nt + 1]), bfhi(bb2[2 * nt + 1])};
      asm volatile("" : "+v"(bias2[nt]));
    }
  }
  SB();
  STAMP(9);
  bar();
  {
    f32x4 acc[3][2];
    LAS unsigned char* hb = L + L_H1 + m * H1P + q * 16;
    const __amdgpu_buffer_rsrc_t outr = rsrc(out, N * 512);
    auto put = [&](int mt) __attribute__((always_inline)) {
      const int r = mt < 2 ? 5 * (m >> 1) + 1 + 2 * mt + (m & 1) : (m < 8 ? 5 * m : TM);
      v4u o;
      o.x = pack_bf16(acc[mt][0][0], acc[mt][0][1]); o.y = pack_bf16(acc[mt][0][2], acc[mt][0][3]);
      o.z = pack_bf16(acc[mt][1][0], acc[mt][1][1]); o.w = pack_bf16(acc[mt][1][2], acc[mt][1][3]);
      __builtin_amdgcn_raw_buffer_store_b128(o, outr, r < TM ? (unsigned)(node0 + r) * 512u + 64 * w + 16 * q : OOB, 0, OUTAUX);
    };
    constexpr int P2 = P2D;
    v4u b[P2];
#define U2_KS(u) ((u) < 24 ? (u) / 3 : 8 + ((u) - 24) % 8)
#define U2_MT(u) ((u) < 24 ? (u) % 3 : ((u) - 24) / 8)
#pragma unroll
    for (int u = 0; u < P2; ++u) b[u] = *(LAS v4u*)(hb + U2_MT(u) * 16 * H1P + U2_KS(u) * 64);
    static_for<0, 48>([&](auto U) {
      constexpr int u = U, ks = U2_KS(u), mt = U2_MT(u);
      if constexpr (u % 12 == 0) PRIO(3 - u / 12);
      constexpr int F0 = 16 + NY;
      if constexpr (u < 2 * (32 - F0) && (u & 1) == 0) load_w2<F0 + u / 2>(wsd, w2off);
      constexpr int issued = u < 2 * (32 - F0) ? F0 + 1 + u / 2 : 32;
      if constexpr (mt == 0 && 2 * ks >= 8 + NY) wait_vm<issued - (2 * ks + 2)>();
      static_for<0, 2>([&](auto NT) {
        constexpr int f = 2 * ks + NT;
        if constexpr (ks == 0) mfma_p0<w2reg(f)>(acc[mt][NT], b[u % P2], bias2[NT]);
        else if constexpr (f >= 8 && f < 8 + NY) mfma_r(acc[mt][NT], wy[f - 8], b[u % P2]);
        else mfma_p<w2reg(f)>(acc[mt][NT], b[u % P2]);
      });
      if constexpr (u + P2 < 48) b[u % P2] = *(LAS v4u*)(hb + U2_MT(u + P2) * 16 * H1P + U2_KS(u + P2) * 64);
      if constexpr (u == 36 || u == 44) put(mt - 1);
      SB();
    });
    asm volatile("s_nop 7" : "+v"(acc[2][0]), "+v"(acc[2][1]));
    put(2);
  }
  if (!timedout && w == 3 && __hip_atomic_load((GAS u64*)(ws + WS_EPOCH), RLX_AGENT) != tag && lane == 0) __hip_atomic_store((GAS u64*)(ws + WS_EPOCH), tag, RLX_AGENT);
  STAMP(10);
  STAMP_OUT((unsigned long long*)(ws + (64u << 20)), blockIdx.x * 8 + w);
  asm volatile(".Lgin_end:");
}

extern "C" void kernel_launch(void* const* d_in, const int* in_sizes, int n_in, void* d_out, int out_size, void* d_ws, size_t ws_size,
                              hipStream_t stream) {
  const int N = in_sizes[0] / D, E = in_sizes[1];
  const int T = (N + TM - 1) / TM;
  (void)hipFuncSetAttribute((const void*)gin_one, hipFuncAttributeMaxDynamicSharedMemorySize, L_END);
  gin_one<<<T, 512, L_END, stream>>>((const unsigned short*)d_in[0], (const int*)d_in[1], (const int*)d_in[2], (const unsigned short*)d_in[3],
                                 (const unsigned short*)d_in[4], (const unsigned short*)d_in[5], (const unsigned short*)d_in[6],
                                 (const unsigned short*)d_in[7], (unsigned short*)d_out, (unsigned char*)d_ws, N, E, T);
}
```

```cpp
#include <hip/hip_runtime.h>
#include <hip/hip_bf16.h>
#include <stdint.h>
#include <type_traits>

typedef unsigned v4u __attribute__((ext_vector_type(4)));
typedef unsigned v2u __attribute__((ext_vector_type(2)));
typedef float f32x4 __attribute__((ext_vector_type(4)));
typedef float f32x2 __attribute__((ext_vector_type(2)));
typedef short bf16x8 __attribute__((ext_vector_type(8)));
typedef short s16x2 __attribute__((ext_vector_type(2)));
typedef __bf16 bf16x2_t __attribute__((ext_vector_type(2)));
typedef unsigned long long u64;
#define GAS __attribute__((address_space(1)))
#define LAS __attribute__((address_space(3)))
#define DI __device__ __forceinline__
#define INL __attribute__((always_inline))
#define RLX_AGENT __ATOMIC_RELAXED, __HIP_MEMORY_SCOPE_AGENT
#define RLX_WG __ATOMIC_RELAXED, __HIP_MEMORY_SCOPE_WORKGROUP
#define SB() __builtin_amdgcn_sched_barrier(0)
#define PRIO(p) __builtin_amdgcn_s_setprio(p)

constexpr int D = 256, H = 512, TM = 40;
constexpr int EPB = 640;
constexpr int CAP = 1536;
constexpr int HP = 544, H1P = 1056;
constexpr int R = 3;
constexpr unsigned OOB = 0x40000000u;
constexpr unsigned EOOB = OOB >> 3;
constexpr int SC1 = 16;
constexpr unsigned SPIN_CAP = 4096;
constexpr unsigned WS_EPOCH = 0, WS_WFLAG = 4096, WS_W2T = 65536, WS_CELL = 1u << 20, WS_BYTES = WS_CELL + 256u * 16384u;
constexpr int L_H = 0, L_H1 = L_H + 48 * HP, L_FLAT = L_H1 + 48 * H1P, L_CNT = L_FLAT + (CAP + 128) * 4, L_MISC = L_CNT + 256,
              L_B1 = L_MISC + 32, L_B2 = L_B1 + 2048, L_END = L_B2 + 512;
constexpr int L_PSTG = L_H;
constexpr int L_PCELL = L_H1, L_PCNT = L_H1 + 320 * 64;

DI unsigned pack_bf16(float a, float b) { f32x2 v = {a, b}; bf16x2_t r = __builtin_convertvector(v, bf16x2_t); return __builtin_bit_cast(unsigned, r); }
DI float bflo(unsigned u) { return __builtin_bit_cast(float, u << 16); }
DI float bfhi(unsigned u) { return __builtin_bit_cast(float, u & 0xffff0000u); }
DI float asf(unsigned u) { return __builtin_bit_cast(float, u); }
DI unsigned asu(float u) { return __builtin_bit_cast(unsigned, u); }
DI f32x4 mfma(v4u a, v4u b, f32x4 c) {
  return __builtin_amdgcn_mfma_f32_16x16x32_bf16(__builtin_bit_cast(bf16x8, a), __builtin_bit_cast(bf16x8, b), c, 0, 0, 0);
}
DI unsigned relu_pack(float a, float b) {
  const s16x2 v = __builtin_bit_cast(s16x2, pack_bf16(a, b));
  return __builtin_bit_cast(unsigned, __builtin_elementwise_max(v, s16x2{0, 0}));
}
DI void bar() { asm volatile("s_waitcnt lgkmcnt(0)" ::: "memory"); __builtin_amdgcn_s_barrier(); asm volatile("" ::: "memory"); }
template <int CTRL, int ROWS> DI unsigned dpp_add(unsigned x) { return x + (unsigned)__builtin_amdgcn_update_dpp(0, (int)x, CTRL, ROWS, 0xF, false); }
DI unsigned wave_incl_scan(unsigned x) {
  x = dpp_add<0x111, 0xF>(x); x = dpp_add<0x112, 0xF>(x); x = dpp_add<0x114, 0xF>(x); x = dpp_add<0x118, 0xF>(x);
  x = dpp_add<0x142, 0xA>(x); x = dpp_add<0x143, 0xC>(x);
  return x;
}
DI __amdgpu_buffer_rsrc_t rsrc(const void* p, unsigned bytes) { return __builtin_amdgcn_make_buffer_rsrc((void*)p, 0, bytes, 0x00020000); }
DI v4u srd(const void* p, unsigned bytes) { return v4u{(unsigned)(u64)p, (unsigned)((u64)p >> 32) & 0xffffu, bytes, 0x00020000u}; }
template <int I, int E, typename F> DI void static_for(F&& f) { if constexpr (I < E) { f(std::integral_constant<int, I>{}); static_for<I + 1, E>(f); } }

DI void tile_w2(__amdgpu_buffer_rsrc_t w2r, __amdgpu_buffer_rsrc_t wsr, int f, int lane) {
  const int ww = f >> 5, ks = (f >> 1) & 15, nt = f & 1, i = lane & 15, kg = lane >> 4;
  const int qo = (2 * ks + (kg >> 1)) & 3;
  const int row0 = 64 * (ks >> 1) + 32 * (qo & 1) + 4 * (qo >> 1) + 2 * (kg & 1);
  const unsigned o = f < 256 ? (row0 * D + 32 * ww + 8 * (i >> 2) + 4 * nt + (i & 3)) * 2 : OOB;
  unsigned v[8];
#pragma unroll
  for (int j = 0; j < 8; ++j) v[j] = __builtin_amdgcn_raw_buffer_load_b16(w2r, o + (j >> 2) * D * 2, (j & 3) * 8 * D * 2, 0);
  const v4u p = {v[0] | (v[1] << 16), v[2] | (v[3] << 16), v[4] | (v[5] << 16), v[6] | (v[7] << 16)};
  __builtin_amdgcn_raw_buffer_store_b128(p, wsr, f < 256 ? WS_W2T + f * 1024 + lane * 16 : OOB, 0, SC1);
}

constexpr int PB = 96, NY = 4;
template <int G> DI void load_w1(v4u d, unsigned off) {
  unsigned so;
  asm volatile("s_mov_b32 %0, %3\n\t"
               "buffer_load_dwordx4 v[%c5:%c5+3], %1, %2, %0 offen\n\tbuffer_load_dwordx4 v[%c5+4:%c5+7], %1, %2, %0 offen offset:1024\n\t"
               "buffer_load_dwordx4 v[%c5+8:%c5+11], %1, %2, %0 offen offset:2048\n\tbuffer_load_dwordx4 v[%c5+12:%c5+15], %1, %2, %0 offen offset:3072\n\t"
               "s_mov_b32 %0, %4\n\t"
               "buffer_load_dwordx4 v[%c5+16:%c5+19], %1, %2, %0 offen\n\tbuffer_load_dwordx4 v[%c5+20:%c5+23], %1, %2, %0 offen offset:1024\n\t"
               "buffer_load_dwordx4 v[%c5+24:%c5+27], %1, %2, %0 offen offset:2048\n\tbuffer_load_dwordx4 v[%c5+28:%c5+31], %1, %2, %0 offen offset:3072"
               : "=&s"(so) : "v"(off), "s"(d), "i"(G * 65536), "i"(G * 65536 + 4096), "i"(PB + 32 + 32 * G) : "v255");
}
template <int G> DI void xpose() {
#pragma unroll
  for (int d = 0; d < 4; ++d)
#pragma unroll
    for (int p = 0; p < 2; ++p) {
      unsigned t0, t1;
      asm volatile("v_perm_b32 v[%c5], v[%c4+4], v[%c4], %2\n\tv_perm_b32 v[%c5+16], v[%c4+6], v[%c4+2], %2\n\t"
                   "v_perm_b32 v[%c5+4], v[%c4+4], v[%c4], %3\n\tv_perm_b32 v[%c5+20], v[%c4+6], v[%c4+2], %3\n\t"
                   "v_mov_b32 %0, v[%c5]\n\tv_mov_b32 %1, v[%c5+4]\n\t"
                   "v_mov_b32_dpp v[%c5], v[%c5+16] row_ror:8 row_mask:0xf bank_mask:0xc\n\t"
                   "v_mov_b32_dpp v[%c5+4], v[%c5+20] row_ror:8 row_mask:0xf bank_mask:0xc\n\t"
                   "v_mov_b32_dpp v[%c5+16], %0 row_ror:8 row_mask:0xf bank_mask:0x3\n\t"
                   "v_mov_b32_dpp v[%c5+20], %1 row_ror:8 row_mask:0xf bank_mask:0x3"
                   : "=&v"(t0), "=&v"(t1) : "s"(0x05040100u), "s"(0x07060302u), "i"(PB + 32 + 32 * G + 8 * d + p), "i"(PB + 32 * G + 8 * p + d));
    }
}
constexpr int w2reg(int i) { return i < 8 ? PB + 128 + 4 * i : PB + 4 * (i - 8 - NY); }
template <int I> DI void load_w2(v4u d, unsigned off) {
  unsigned so;
  asm volatile("s_mov_b32 %0, %3\n\tbuffer_load_dwordx4 v[%c4:%c4+3], %1, %2, %0 offen offset:%c5 sc1"
               : "=&s"(so) : "v"(off), "s"(d), "i"((I >> 2) * 4096), "i"(w2reg(I)), "i"((I & 3) * 1024));
}
template <int A> DI void mfma_p(f32x4& acc, v4u b) { asm volatile("v_mfma_f32_16x16x32_bf16 %0, v[%c2:%c2+3], %1, %0" : "+v"(acc) : "v"(b), "i"(A)); }
template <int A> DI void mfma_p0(f32x4& acc, v4u b, f32x4 c) {
  asm volatile("v_mfma_f32_16x16x32_bf16 %0, v[%c3:%c3+3], %1, %2" : "=&v"(acc) : "v"(b), "v"(c), "i"(A));
}
DI void mfma_r(f32x4& acc, v4u a, v4u b) { asm volatile("v_mfma_f32_16x16x32_bf16 %0, %1, %2, %0" : "+v"(acc) : "v"(a), "v"(b)); }
template <bool DEV> DI void touch(v4u d, unsigned off) {
  if constexpr (DEV) asm volatile("buffer_load_dword v[%c2], %0, %1, 0 offen sc1" :: "v"(off), "s"(d), "i"(PB - 1));
  else asm volatile("buffer_load_dword v[%c2], %0, %1, 0 offen" :: "v"(off), "s"(d), "i"(PB - 1));
}
DI void count_row(unsigned a) {
  asm volatile("s_mov_b64 exec, 1\n\tds_add_u32 %0, %1\n\ts_mov_b64 exec, -1" :: "v"(a), "v"(1u) : "memory");
}
template <int N> DI void wait_vm() { asm volatile("s_waitcnt vmcnt(%c0)" :: "i"(N) : "memory"); }

__global__ __launch_bounds__(512, 2) __attribute__((amdgpu_num_vgpr(PB / 2 - 1)))
void gin_one(const unsigned short* __restrict__ x, const int* __restrict__ src, const int* __restrict__ dst,
             const unsigned short* __restrict__ epsp, const unsigned short* __restrict__ w1, const unsigned short* __restrict__ b1,
             const unsigned short* __restrict__ w2, const unsigned short* __restrict__ b2, unsigned short* __restrict__ out,
             unsigned char* ws, int N, int E, int T) {
  extern __shared__ __attribute__((aligned(16))) unsigned char Ls[];
  LAS unsigned char* L = (LAS unsigned char*)Ls;
  const int tid = threadIdx.x, lane = tid & 63, w = __builtin_amdgcn_readfirstlane(tid >> 6);
  const int t = blockIdx.x, node0 = t * TM;
  const unsigned bulk = w >> 2, chain = bulk ^ 1;
  const int rank = (t >> 3) & 31;
  {
    u64 pc; unsigned len;
    asm volatile("s_getpc_b64 %0\n.Lgin_pc:\n\ts_mov_b32 %1, .Lgin_end-.Lgin_pc" : "=s"(pc), "=s"(len));
    touch<false>(srd((const void*)pc, len), (rank * 8 + (tid >> 6)) * 128 + lane * 32768);
  }
  SB();
  asm volatile("" :: "s"(x), "s"(src), "s"(dst), "s"(epsp), "s"(w1), "s"(b1), "s"(w2), "s"(b2), "s"(out), "s"(ws), "s"(N), "s"(E), "s"(T));
  const int share2 = (T >> 3) + rank;
  const __amdgpu_buffer_rsrc_t wsr = rsrc(ws, WS_BYTES), xr = rsrc(x, N * 512), w2r = rsrc(w2, D * H * 2);
  const v4u w1d = srd(w1, D * H * 2), wsd = srd(ws, WS_BYTES);
  const int q = lane >> 4, m = lane & 15;
  const unsigned w1off = (((lane >> 3) & 1) * 32 + q * 8) * 1024 + w * 128 + (lane & 7) * 16;
  LAS unsigned* pcnt = (LAS unsigned*)(L + L_PCNT);
  LAS unsigned* pcell = (LAS unsigned*)(L + L_PCELL);
  const int tf = t + (w - 1) * T;

  if (w == 4) {
    PRIO(3);
    const __amdgpu_buffer_rsrc_t dr = rsrc(dst, E * 4), sr = rsrc(src, E * 4);
    v4u dv[3], sv[3];
#pragma unroll
    for (int i = 0; i < 3; ++i) {
      const unsigned o = (i == 2 && lane >= 32) ? OOB : (unsigned)(t * EPB + (lane + 64 * i) * 4) * 4u;
      dv[i] = __builtin_amdgcn_raw_buffer_load_b128(dr, o, 0, 0);
      sv[i] = __builtin_amdgcn_raw_buffer_load_b128(sr, o, 0, 0);
    }
    const u64 ep0 = __hip_atomic_load((GAS u64*)(ws + WS_EPOCH), RLX_AGENT);
    SB();
    *(LAS v4u*)(pcnt + lane * 4) = v4u{0, 0, 0, 0};
    pcnt[256 + lane] = 0;
    const unsigned dummy = 256u + lane;
    unsigned tt[12], rk[12];
#pragma unroll
    for (int i = 0; i < 12; ++i) {
      const int e = t * EPB + (lane + 64 * (i >> 2)) * 4;
      const unsigned lo = (e < E && !(i >= 8 && lane >= 32)) ? 0u : dummy;
      tt[i] = max(min(__umul24(min(dv[i >> 2][i & 3], 16383u), 52429u) >> 21, dummy), lo);
      rk[i] = __hip_atomic_fetch_add(pcnt + tt[i], 1u, RLX_WG);
    }
#pragma unroll
    for (int i = 0; i < 12; ++i)
      pcell[tt[i] * 16 + (rk[i] & 15u)] = (dv[i >> 2][i & 3] - tt[i] * TM) | (min(sv[i >> 2][i & 3], 16383u) << 6);
    *(LAS u64*)(L + L_MISC) = ep0;
  } else {
    __builtin_amdgcn_s_sleep(3);
    if (w == 0 || (w == 5 && share2 < 32)) touch<false>(w1d, (w ? share2 : rank) * 8192 + lane * 128);
    if (w == 3) {
      const v4u v1 = __builtin_amdgcn_raw_buffer_load_b128(rsrc(b1, H * 2), lane * 16, 0, 0);
      const v4u v2 = __builtin_amdgcn_raw_buffer_load_b128(rsrc(b2, D * 2), lane * 16, 0, 0);
      const unsigned ev = epsp[0];
      *(LAS unsigned*)(L + L_CNT + lane * 4) = 0;
#pragma unroll
      for (int i = 0; i < 8; ++i)
        *(LAS float*)(L + L_B1 + ((lane >> 3) * 64 + (((lane >> 2) & 1) + 2 * (i >> 2)) * 16 + 4 * (i & 3) + (lane & 3)) * 4) =
            (i & 1) ? bfhi(v1[i >> 1]) : bflo(v1[i >> 1]);
      *(LAS v4u*)(L + (lane < 32 ? L_B2 + lane * 16 : L_FLAT + lane * 16)) = v2;
      *(LAS v2u*)(L + L_MISC + 8) = v2u{0u, ev};
      *(LAS v4u*)(L + L_MISC + 16) = v4u{0u, 0u, 0u, 0u};
    } else if (w == 1 || w == 2) {
      tile_w2(w2r, wsr, tf, lane);
    }
  }
  if (chain) { load_w1<0>(w1d, w1off); load_w1<1>(w1d, w1off); }
  PRIO(3);
  bar();
  const u64 tag = *(LAS u64*)(L + L_MISC) + 1;
  const unsigned tagA = (unsigned)tag & 0xFFFFFFu, tagB = ((unsigned)tag + (unsigned)(tag >> 24) * 0x9E3779B1u) & 0x7FFFFu;
  {
    v4u e[2]; unsigned c[2];
#pragma unroll
    for (int i = 0; i < 2; ++i) { e[i] = *(LAS v4u*)(pcell + (w * 2 + i) * 256 + lane * 4); c[i] = pcnt[(w * 2 + i) * 16 + (lane >> 2)]; }
#pragma unroll
    for (int i = 0; i < 2; ++i) {
      const unsigned tc = (w * 2 + i) * 16 + (lane >> 2), cc = min(c[i], 31u);
      const v4u g = {(e[i].x & 0xFFFFFu) | (e[i].y << 20), ((e[i].y >> 12) & 0xFFu) | (tagA << 8), (e[i].z & 0xFFFFFu) | (e[i].w << 20),
                     ((e[i].w >> 12) & 0xFFu) | (cc << 8) | (tagB << 13)};
      __builtin_amdgcn_raw_buffer_store_b128(g, wsr, tc < (unsigned)T ? WS_CELL + tc * 16384 + t * 64 + (lane & 3) * 16 : OOB, 0, SC1);
    }
  }
  SB();
  PRIO(1);
  if (chain) { load_w1<2>(w1d, w1off); load_w1<3>(w1d, w1off); } else __builtin_amdgcn_s_sleep(24);
  bool any2 = false;
  unsigned ent[16], rk[16];
  LAS unsigned* flat = (LAS unsigned*)(L + L_FLAT);
  if (chain) {
    PRIO(2);
    v4u g[4];
    const unsigned coff = WS_CELL + t * 16384 + w * 4096 + lane * 16;
#pragma unroll
    for (int i = 0; i < 4; ++i) g[i] = __builtin_amdgcn_raw_buffer_load_b128(wsr, coff + i * 1024, 0, SC1);
    bool tmo = false;
    const int pl = T - 64 * w - (lane >> 2);
#define GR_OK(i) (16 * (i) >= pl || ((g[i].y >> 8) == tagA && (g[i].w >> 13) == tagB))
    if (!__all(GR_OK(0) && GR_OK(1) && GR_OK(2) && GR_OK(3))) {
      for (unsigned spins = 0;;) {
#pragma unroll
        for (int i = 0; i < 4; ++i) {
          const bool ok = GR_OK(i);
          const v4u nv = __builtin_amdgcn_raw_buffer_load_b128(wsr, ok ? OOB : coff + i * 1024, 0, SC1);
#pragma unroll
          for (int k = 0; k < 4; ++k) g[i][k] = ok ? g[i][k] : nv[k];
        }
        if (__all(GR_OK(0) && GR_OK(1) && GR_OK(2) && GR_OK(3))) break;
        if (++spins > SPIN_CAP) { tmo = true; break; }
      }
    }
    LAS unsigned char* stg = L + L_PSTG + w * 5120;
#pragma unroll
    for (int i = 0; i < 4; ++i) *(LAS v4u*)(stg + (16 * i + (lane >> 2)) * 80 + (lane & 3) * 16) = g[i];
#pragma unroll
    for (int i = 0; i < 4; ++i) g[i] = *(LAS v4u*)(stg + lane * 80 + i * 16);
    const int nval = (64 * w + lane < T && !tmo) ? (int)((g[0].w >> 8) & 31u) : 0;
    any2 = __any(nval > 8);
    bool bad = nval > 16;
    const unsigned dummy = 40u + (unsigned)(lane % 24);
    auto rank8 = [&](int lo) INL {
#pragma unroll
      for (int i = lo; i < lo + 8; ++i) {
        const unsigned a = g[i >> 2][i & 2], b = g[i >> 2][(i & 2) + 1];
        ent[i] = i < nval ? ((i & 1) ? __builtin_amdgcn_alignbit(b, a, 20) : a) & 0xFFFFFu : dummy;
        rk[i] = __hip_atomic_fetch_add((LAS unsigned*)(L + L_CNT + ((ent[i] & 63u) << 2)), 1u << (8 * w), RLX_WG);
      }
#pragma unroll
      for (int i = lo; i < lo + 8; ++i) { rk[i] = (rk[i] >> (8 * w)) & 255u; bad |= rk[i] == 255u; }
    };
    rank8(0);
    if (__builtin_expect(any2, 0)) rank8(8);
    if ((__any(bad) || tmo) && lane == 0) __hip_atomic_fetch_or((LAS unsigned*)(L + L_MISC + 8), tmo ? 3u : 1u, RLX_WG);
  } else {
    PRIO(0);
    load_w1<0>(w1d, w1off); load_w1<1>(w1d, w1off);
    wait_vm<8>(); xpose<0>();
    wait_vm<0>(); xpose<1>();
    load_w1<2>(w1d, w1off); load_w1<3>(w1d, w1off);
    touch<false>(srd(x, N * 512), (w - 4) * 64 + lane < 160 ? node0 * 512 + ((w - 4) * 64 + lane) * 128 : OOB);
  }
  bar();
  bool slow, timedout;
  int s0, nit;
  u64 pk = 0;
  {
    const unsigned cw = *(LAS unsigned*)(L + L_CNT + lane * 4);
    const unsigned flagv = __builtin_amdgcn_readfirstlane(*(LAS unsigned*)(L + L_MISC + 8));
    const unsigned deg = __builtin_amdgcn_sad_u8(cw, 0u, 0u), below = __builtin_amdgcn_sad_u8(cw & ((1u << (8 * (w & 3))) - 1u), 0u, 0u);
    const unsigned l4 = (deg + 4u) & ~3u;
    const unsigned plen = lane < TM ? l4 + (lane % 5 == 4 ? 4u * (R - 1) : 0u) : 0u;
    const unsigned incl = wave_incl_scan(plen), start = incl - plen;
    timedout = (flagv & 2u) != 0u;
    slow = flagv != 0u || __builtin_amdgcn_readlane(incl, 63) > (unsigned)CAP;
    const unsigned tail = __builtin_amdgcn_readlane(incl, 5 * w + 4) - 4 * (R - 1);
    s0 = __builtin_amdgcn_readlane(start, 5 * w);
    nit = ((tail - s0) / 4 + R - 1) / R;
#pragma unroll
    for (int j = 0; j < 5; ++j) pk |= (u64)(((unsigned)__builtin_amdgcn_readlane(l4, 5 * w + j) >> 2) & 0xFFFu) << (12 * j);
    if (slow) s0 = 0;
    if (!slow) {
      const unsigned st2 = (unsigned)__builtin_amdgcn_ds_bpermute((5 * w + (lane >> 3)) << 2, (int)(start | (deg << 16)));
      if (chain) {
        const unsigned B4 = L_FLAT + 4u * (lane < TM ? start + 1u + below : CAP + 64u);
        auto place8 = [&](int lo) INL {
          unsigned b[8];
#pragma unroll
          for (int i = 0; i < 8; ++i) b[i] = (unsigned)__builtin_amdgcn_ds_bpermute((int)((ent[lo + i] & 63u) << 2), (int)B4);
#pragma unroll
          for (int i = 0; i < 8; ++i) *(LAS unsigned*)(L + b[i] + 4u * rk[lo + i]) = ent[lo + i];
        };
        place8(0);
        if (__builtin_expect(any2, 0)) place8(8);
      }
      const unsigned dg = st2 >> 16, k = lane & 7, pp = k == 7 ? 0u : dg + 1u + k;
      const bool mine = lane < 40 && pp < ((dg + 4u) & ~3u);
      const unsigned at = mine ? (st2 & 0xFFFFu) + pp : (lane >= 48 && lane - 48 < 4 * (R - 1) ? tail + lane - 48 : CAP + lane);
      flat[at] = (mine && k == 7) ? (unsigned)(node0 + 5 * w + (lane >> 3)) << 6 : EOOB;
    }
  }
  if (bulk) { wait_vm<9>(); xpose<2>(); }
  bar();
  PRIO(2);
  if (w == 1 || w == 2) {
    wait_vm<0>();
    if (lane == 0 && tf < 256) __hip_atomic_store((GAS unsigned*)(ws + WS_WFLAG) + tf, (unsigned)tag, RLX_AGENT);
  }
  const unsigned l16 = m * 16;
  LAS unsigned* fp = flat + s0 + q;
  v4u bl[R], br[R];
  unsigned en[R];
#pragma unroll
  for (int i = 0; i < R; ++i) en[i] = fp[4 * i];
  const float epsf = bflo(*(LAS unsigned*)(L + L_MISC + 12));
#pragma unroll
  for (int i = 0; i < R; ++i) {
    const unsigned o = ((en[i] << 3) & ~511u) | l16;
    bl[i] = __builtin_amdgcn_raw_buffer_load_b128(xr, o, 0, 0);
    br[i] = __builtin_amdgcn_raw_buffer_load_b128(xr, o + 256, 0, 0);
    en[i] = fp[4 * (R + i)];
  }
  asm volatile("" ::: "memory");
  SB();
  if (chain) { PRIO(0); xpose<0>(); xpose<1>(); xpose<2>(); }
  wait_vm<2 * R + 1>();
  xpose<3>();
  PRIO(0);
  int stage = 0;
  bool xdone = false;
  const unsigned w2off = WS_W2T + w * 32768 + lane * 16;
  v4u gb[2];
  f32x4 gacc[4];
  unsigned ghb = 0;
  auto gemm1 = [&](int mt, auto LAST, auto PART) INL {
    constexpr int part = PART;
    if constexpr (part == 0) {
      ghb = L_H + (mt * 16 + m) * HP + q * 16;
#pragma unroll
      for (int i = 0; i < 2; ++i) gb[i] = *(LAS v4u*)(L + ghb + i * 64);
#pragma unroll
      for (int c = 0; c < 4; ++c) gacc[c] = *(LAS f32x4*)(L + L_B1 + (64 * w + 16 * q) * 4 + 16 * c);
    }
    static_for<3 * part, (part == 2 ? 8 : 3 * part + 3)>([&](auto KS) {
      constexpr int ks = KS;
      if constexpr (LAST && (ks & 1) == 0) PRIO(3 - ks / 2);
      static_for<0, 4>([&](auto C) { mfma_p<PB + 16 * ks + 4 * C>(gacc[C], gb[ks & 1]); });
      if constexpr (ks + 2 < 8) gb[ks & 1] = *(LAS v4u*)(L + ghb + (ks + 2) * 64);
      if constexpr (LAST) load_w2<8 + NY + ks>(wsd, w2off);
      SB();
    });
    if constexpr (part == 2) {
      asm volatile("s_nop 7" : "+v"(gacc[0]), "+v"(gacc[1]), "+v"(gacc[2]), "+v"(gacc[3]));
      LAS unsigned char* h1w = L + L_H1 + (mt * 16 + m) * H1P + (64 * w + 16 * q) * 2;
#pragma unroll
      for (int half = 0; half < 2; ++half) {
        unsigned o[4];
#pragma unroll
        for (int k = 0; k < 4; ++k) o[k] = relu_pack(gacc[2 * half + (k >> 1)][2 * (k & 1)], gacc[2 * half + (k >> 1)][2 * (k & 1) + 1]);
        *(LAS v4u*)(h1w + 16 * half) = v4u{o[0], o[1], o[2], o[3]};
      }
    }
  };
  typedef std::integral_constant<int, 0> I0; typedef std::integral_constant<int, 1> I1; typedef std::integral_constant<int, 2> I2;
  auto wait_rows = [&](int mt) INL {
    while (__builtin_amdgcn_readfirstlane(__hip_atomic_load((LAS unsigned*)(L + L_MISC + 20 + 4 * mt), RLX_WG)) < (mt < 2 ? 16u : 8u))
      __builtin_amdgcn_s_sleep(1);
    asm volatile("" ::: "memory");
  };
  auto early_head = [&]() INL {
    if (stage == 0) {
      asm volatile("buffer_load_dwordx4 v[%c2:%c2+3], %0, %1, 0 offen sc1" :: "v"(WS_WFLAG + lane * 16), "s"(wsd), "i"(PB + 156));
    } else {
      unsigned d0, d1;
      asm volatile("v_xor_b32 %0, %2, v[%c3]\n\tv_xor_b32 %1, %2, v[%c3+1]\n\tv_or_b32 %0, %0, %1\n\tv_xor_b32 %1, %2, v[%c3+2]\n\t"
                   "v_or_b32 %0, %0, %1\n\tv_xor_b32 %1, %2, v[%c3+3]\n\tv_or_b32 %0, %0, %1"
                   : "=&v"(d0), "=&v"(d1) : "s"((unsigned)tag), "i"(PB + 156));
      xdone = __all(d0 == 0u);
      if (xdone) static_for<0, 8>([&](auto I) { load_w2<I>(wsd, w2off); });
    }
  };
  if (__builtin_expect(!slow, 1)) {
    const v4u sel = {m == 0 ? 0x3F80u : (m == 1 ? 0x3F800000u : 0u), m == 2 ? 0x3F80u : (m == 3 ? 0x3F800000u : 0u),
                     m == 4 ? 0x3F80u : (m == 5 ? 0x3F800000u : 0u), m == 6 ? 0x3F80u : (m == 7 ? 0x3F800000u : 0u)};
#define SELF(k) (unsigned)__builtin_amdgcn_update_dpp((int)sel[k], (int)sel[k], 0x128, 0x1, 0xC, false)
    v4u cur = {SELF(0), SELF(1), SELF(2), SELF(3)};
    fp += 8 * R;
    f32x4 a0 = {0.f, 0.f, 0.f, 0.f}, a1 = {0.f, 0.f, 0.f, 0.f};
    const unsigned hl = L_H + (lane >> 5) * 256 + m * 16 + (q & 1) * 8;
    unsigned rows = (32 + w) | ((2 * w) << 6) | ((2 * w + 1) << 12) | ((16 + 2 * w) << 18) | ((17 + 2 * w) << 24);
    unsigned cv = 0;
    int rem = (int)(pk & 0xFFF);
#define SLOT(i, REFILL) { \
        a0 = mfma(cur, bl[i], a0); \
        a1 = mfma(cur, br[i], a1); \
        cur = sel; \
        if (REFILL) { \
          const unsigned o = ((en[i] << 3) & ~511u) | l16; \
          bl[i] = __builtin_amdgcn_raw_buffer_load_b128(xr, o, 0, 0); \
          br[i] = __builtin_amdgcn_raw_buffer_load_b128(xr, o + 256, 0, 0); \
          en[i] = fp[4 * i]; \
        } \
        if (__builtin_expect(--rem == 0, 0)) { \
          float f[4]; \
          _Pragma("unroll") for (int r = 0; r < 4; ++r) { \
            auto s2 = __builtin_amdgcn_permlane32_swap(asu(a0[r]), asu(a1[r]), false, false); \
            const unsigned u0 = s2[0], u1 = s2[1]; \
            f[r] = fmaf(epsf, asf(u1), asf(u0)); \
          } \
          *(LAS v2u*)(L + hl + (rows & 63u) * HP) = v2u{pack_bf16(f[0], f[1]), pack_bf16(f[2], f[3])}; \
          count_row(L_MISC + 20 + ((rows & 48u) >> 2));       \
          rows >>= 6; \
          a0 = f32x4{0.f, 0.f, 0.f, 0.f}; a1 = a0; \
          cur = v4u{SELF(0), SELF(1), SELF(2), SELF(3)}; \
          pk >>= 12; \
          rem = (int)(pk & 0xFFF); \
        } }
    if (w == 6 || (w == 7 && share2 < 32)) touch<true>(wsd, WS_W2T + (w == 7 ? share2 : rank) * 8192 + lane * 128);
    static_assert(R == 3, "GEMM1 comes in three parts, one behind each slot of a round");
    for (; nit > 1; --nit) {
      const bool go = stage < 2 && __builtin_amdgcn_readfirstlane(cv) >= (stage ? 16u : 8u);
      asm volatile("" ::: "memory");
      SLOT(0, true)
      if (go) { early_head(); gemm1(stage ? 0 : 2, std::false_type{}, I0{}); }
      SLOT(1, true)
      if (go) gemm1(0, std::false_type{}, I1{});
      SLOT(2, true)
      if (go) { gemm1(stage ? 0 : 2, std::false_type{}, I2{}); ++stage; }
      fp += 4 * R;
      cv = __hip_atomic_load((LAS unsigned*)(L + L_MISC + 20 + (stage ? 0 : 8)), RLX_WG);
    }
#pragma unroll
    for (int i = 0; i < R; ++i) SLOT(i, false)
  } else {
#pragma unroll 1
    for (int j = 0; j < 5; ++j) {
      const int node = node0 + 5 * w + j;
      const v2u s2 = __builtin_amdgcn_raw_buffer_load_b64(xr, (unsigned)node * 512u + lane * 8, 0, 0);
      float a0 = bflo(s2.x), a1 = bfhi(s2.x), a2 = bflo(s2.y), a3 = bfhi(s2.y);
      if (node < N)
        for (int eb = 0; eb < E; eb += 64) {
          const int e = eb + lane;
          const int dv2 = (int)__builtin_amdgcn_raw_buffer_load_b32(rsrc(dst, E * 4), e * 4, 0, 0);
          const int sv2 = (int)__builtin_amdgcn_raw_buffer_load_b32(rsrc(src, E * 4), e * 4, 0, 0);
          u64 mkk = __ballot(dv2 == node && e < E);
          while (mkk) {
            const int b = __builtin_ctzll(mkk); mkk &= mkk - 1;
            const unsigned sidx = (unsigned)__builtin_amdgcn_readlane(sv2, b);
            const v2u rv = __builtin_amdgcn_raw_buffer_load_b64(xr, sidx < (unsigned)N ? sidx * 512u + lane * 8 : OOB, 0, 0);
            a0 += bflo(rv.x); a1 += bfhi(rv.x); a2 += bflo(rv.y); a3 += bfhi(rv.y);
          }
        }
      const int row = j ? ((j - 1) >> 1) * 16 + 2 * w + ((j - 1) & 1) : 32 + w;
      *(LAS v2u*)(L + L_H + row * HP + lane * 8) =
          v2u{pack_bf16(fmaf(epsf, bflo(s2.x), a0), fmaf(epsf, bfhi(s2.x), a1)), pack_bf16(fmaf(epsf, bflo(s2.y), a2), fmaf(epsf, bfhi(s2.y), a3))};
      count_row(L_MISC + 20 + (row >> 4) * 4);
    }
  }
  if (!timedout && tid == 192 && t == 0) __hip_atomic_store((GAS u64*)(ws + WS_EPOCH), tag, RLX_AGENT);
#pragma unroll 1
  for (; stage < 2; ++stage) {
    wait_rows(stage ? 0 : 2); wait_vm<0>(); early_head();
    gemm1(stage ? 0 : 2, std::false_type{}, I0{}); gemm1(0, std::false_type{}, I1{}); gemm1(stage ? 0 : 2, std::false_type{}, I2{});
  }
  if (__builtin_expect(!xdone, 0)) {
    bool okw = false;
    for (unsigned sp = 0; sp <= SPIN_CAP && !okw; ++sp) {
      const v4u fl = __builtin_amdgcn_raw_buffer_load_b128(wsr, WS_WFLAG + lane * 16, 0, SC1);
      okw = __all(fl.x == (unsigned)tag && fl.y == (unsigned)tag && fl.z == (unsigned)tag && fl.w == (unsigned)tag);
    }
    if (!okw) {
#pragma unroll 1
      for (int i = 0; i < 32; ++i) tile_w2(w2r, wsr, w * 32 + i, lane);
    }
    wait_vm<0>();
    static_for<0, 8>([&](auto I) { load_w2<I>(wsd, w2off); });
  }
  v4u wy[NY];
#pragma unroll
  for (int i = 0; i < NY; ++i) wy[i] = __builtin_amdgcn_raw_buffer_load_b128(wsr, w2off + (i & 3) * 1024, (2 + (i >> 2)) * 4096, SC1);
  SB();
  const v4u bb2 = *(LAS v4u*)(L + L_B2 + (32 * w + 8 * q) * 2);
  wait_rows(1);
#pragma unroll
  for (int i = 0; i < NY; ++i) asm volatile("" : "+v"(wy[i]));
  gemm1(1, std::true_type{}, I0{}); gemm1(1, std::true_type{}, I1{}); gemm1(1, std::true_type{}, I2{});
  f32x4 bias2[2];
#pragma unroll
  for (int nt = 0; nt < 2; ++nt) {
    bias2[nt] = f32x4{bflo(bb2[2 * nt]), bfhi(bb2[2 * nt]), bflo(bb2[2 * nt + 1]), bfhi(bb2[2 * nt + 1])};
    asm volatile("" : "+v"(bias2[nt]));
  }
  SB();
  bar();
  {
    f32x4 acc[3][2];
    LAS unsigned char* hb = L + L_H1 + m * H1P + q * 16;
    const __amdgpu_buffer_rsrc_t outr = rsrc(out, N * 512);
    auto put = [&](int mt) INL {
      const int r = mt < 2 ? 5 * (m >> 1) + 1 + 2 * mt + (m & 1) : (m < 8 ? 5 * m : TM);
      const v4u o = {pack_bf16(acc[mt][0][0], acc[mt][0][1]), pack_bf16(acc[mt][0][2], acc[mt][0][3]),
                     pack_bf16(acc[mt][1][0], acc[mt][1][1]), pack_bf16(acc[mt][1][2], acc[mt][1][3])};
      __builtin_amdgcn_raw_buffer_store_b128(o, outr, r < TM ? (unsigned)(node0 + r) * 512u + 64 * w + 16 * q : OOB, 0, SC1);
    };
    constexpr int P2 = 8, F0 = 16 + NY;
    v4u b[P2];
#define U2_KS(u) ((u) < 24 ? (u) / 3 : 8 + ((u) - 24) % 8)
#define U2_MT(u) ((u) < 24 ? (u) % 3 : ((u) - 24) / 8)
#pragma unroll
    for (int u = 0; u < P2; ++u) b[u] = *(LAS v4u*)(hb + U2_MT(u) * 16 * H1P + U2_KS(u) * 64);
    static_for<0, 48>([&](auto U) {
      constexpr int u = U, ks = U2_KS(u), mt = U2_MT(u);
      if constexpr (u % 12 == 0) PRIO(3 - u / 12);
      if constexpr (u < 2 * (32 - F0) && (u & 1) == 0) load_w2<F0 + u / 2>(wsd, w2off);
      constexpr int issued = u < 2 * (32 - F0) ? F0 + 1 + u / 2 : 32;
      if constexpr (mt == 0 && 2 * ks >= 8 + NY) wait_vm<issued - (2 * ks + 2)>();
      static_for<0, 2>([&](auto NT) {
        constexpr int f = 2 * ks + NT;
        if constexpr (ks == 0) mfma_p0<w2reg(f)>(acc[mt][NT], b[u % P2], bias2[NT]);
        else if constexpr (f >= 8 && f < 8 + NY) mfma_r(acc[mt][NT], wy[f - 8], b[u % P2]);
        else mfma_p<w2reg(f)>(acc[mt][NT], b[u % P2]);
      });
      if constexpr (u + P2 < 48) b[u % P2] = *(LAS v4u*)(hb + U2_MT(u + P2) * 16 * H1P + U2_KS(u + P2) * 64);
      if constexpr (u == 36 || u == 44) put(mt - 1);
      SB();
    });
    asm volatile("s_nop 7" : "+v"(acc[2][0]), "+v"(acc[2][1]));
    put(2);
  }
  if (!timedout && w == 3 && __hip_atomic_load((GAS u64*)(ws + WS_EPOCH), RLX_AGENT) != tag && lane == 0)
    __hip_atomic_store((GAS u64*)(ws + WS_EPOCH), tag, RLX_AGENT);
  asm volatile(".Lgin_end:");
}

__global__ __launch_bounds__(256) void gin_any(const unsigned short* __restrict__ x, const int* __restrict__ src, const int* __restrict__ dst,
                                               const unsigned short* __restrict__ epsp, const unsigned short* __restrict__ w1,
                                               const unsigned short* __restrict__ b1, const unsigned short* __restrict__ w2,
                                               const unsigned short* __restrict__ b2, unsigned short* __restrict__ out, int N, int E) {
  __shared__ float h[D], h1[H];
  const int node = blockIdx.x, c = threadIdx.x, lane = c & 63;
  const float xs = bflo(x[(size_t)node * D + c]);
  float a = xs;
  for (int eb = 0; eb < E; eb += 64) {
    const int e = eb + lane, dv = e < E ? dst[e] : -1, sv = e < E ? src[e] : 0;
    u64 mk = __ballot(dv == node);
    while (mk) {
      const int s = __builtin_amdgcn_readlane(sv, __builtin_ctzll(mk));
      mk &= mk - 1;
      if ((unsigned)s < (unsigned)N) a += bflo(x[(size_t)s * D + c]);
    }
  }
  h[c] = bflo(pack_bf16(fmaf(bflo(epsp[0]), xs, a), 0.f));
  __syncthreads();
  for (int j = c; j < H; j += 256) {
    float s = bflo(b1[j]);
    for (int k = 0; k < D; ++k) s = fmaf(h[k], bflo(w1[k * H + j]), s);
    h1[j] = bflo(relu_pack(s, 0.f));
  }
  __syncthreads();
  float s = bflo(b2[c]);
  for (int k = 0; k < H; ++k) s = fmaf(h1[k], bflo(w2[k * D + c]), s);
  out[(size_t)node * D + c] = (unsigned short)pack_bf16(s, 0.f);
}

extern "C" void kernel_launch(void* const* d_in, const int* in_sizes, int n_in, void* d_out, int out_size, void* d_ws, size_t ws_size,
                              hipStream_t stream) {
  const unsigned short* x = (const unsigned short*)d_in[0];
  const int* src = (const int*)d_in[1];
  const int* dst = (const int*)d_in[2];
  const unsigned short* eps = (const unsigned short*)d_in[3];
  const unsigned short* w1 = (const unsigned short*)d_in[4];
  const unsigned short* b1 = (const unsigned short*)d_in[5];
  const unsigned short* w2 = (const unsigned short*)d_in[6];
  const unsigned short* b2 = (const unsigned short*)d_in[7];
  const int N = in_sizes[0] / D, E = in_sizes[1], T = (N + TM - 1) / TM;
  if (T >= 128 && T <= 256 && E <= EPB * T && E % 4 == 0 && ws_size >= WS_BYTES) {
    (void)hipFuncSetAttribute((const void*)gin_one, hipFuncAttributeMaxDynamicSharedMemorySize, L_END);
    gin_one<<<T, 512, L_END, stream>>>(x, src, dst, eps, w1, b1, w2, b2, (unsigned short*)d_out, (unsigned char*)d_ws, N, E, T);
  } else if (N > 0) {
    gin_any<<<N, 256, 0, stream>>>(x, src, dst, eps, w1, b1, w2, b2, (unsigned short*)d_out, N, E);
  }
}
```

```cpp
#include <hip/hip_runtime.h>
#include <hip/hip_bf16.h>
#include <stdint.h>
#include <type_traits>

typedef unsigned v4u __attribute__((ext_vector_type(4)));
typedef unsigned v2u __attribute__((ext_vector_type(2)));
typedef float f32x4 __attribute__((ext_vector_type(4)));
typedef float f32x2 __attribute__((ext_vector_type(2)));
typedef short bf16x8 __attribute__((ext_vector_type(8)));
typedef short s16x2 __attribute__((ext_vector_type(2)));
typedef __bf16 bf16x2_t __attribute__((ext_vector_type(2)));
typedef unsigned long long u64;
#define GAS __attribute__((address_space(1)))
#define LAS __attribute__((address_space(3)))
#define DI __device__ __forceinline__
#define INL __attribute__((always_inline))
#define RLX_AGENT __ATOMIC_RELAXED, __HIP_MEMORY_SCOPE_AGENT
#define RLX_WG __ATOMIC_RELAXED, __HIP_MEMORY_SCOPE_WORKGROUP
#define SB() __builtin_amdgcn_sched_barrier(0)
#define PRIO(p) __builtin_amdgcn_s_setprio(p)

constexpr int D = 256, H = 512, TM = 40;
constexpr int EPB = 640;
constexpr int CAP = 1536;
constexpr int HP = 544, H1P = 1056;
constexpr int R = 3;
constexpr unsigned OOB = 0x40000000u;
constexpr unsigned EOOB = OOB >> 3;
constexpr int SC1 = 16;
constexpr unsigned SPIN_CAP = 4096;
constexpr unsigned WS_EPOCH = 0, WS_WFLAG = 4096, WS_W2T = 65536, WS_CELL = 1u << 20, WS_BYTES = WS_CELL + 256u * 16384u;
constexpr int L_H = 0, L_H1 = L_H + 48 * HP, L_FLAT = L_H1 + 48 * H1P, L_CNT = L_FLAT + (CAP + 128) * 4, L_MISC = L_CNT + 256,
              L_B1 = L_MISC + 32, L_B2 = L_B1 + 2048, L_END = L_B2 + 512;
constexpr int L_PSTG = L_H;
constexpr int L_PCELL = L_H1, L_PCNT = L_H1 + 320 * 64;

DI unsigned pack_bf16(float a, float b) { f32x2 v = {a, b}; bf16x2_t r = __builtin_convertvector(v, bf16x2_t); return __builtin_bit_cast(unsigned, r); }
DI float bflo(unsigned u) { return __builtin_bit_cast(float, u << 16); }
DI float bfhi(unsigned u) { return __builtin_bit_cast(float, u & 0xffff0000u); }
DI float asf(unsigned u) { return __builtin_bit_cast(float, u); }
DI unsigned asu(float u) { return __builtin_bit_cast(unsigned, u); }
DI f32x4 mfma(v4u a, v4u b, f32x4 c) {
  return __builtin_amdgcn_mfma_f32_16x16x32_bf16(__builtin_bit_cast(bf16x8, a), __builtin_bit_cast(bf16x8, b), c, 0, 0, 0);
}
DI unsigned relu_pack(float a, float b) {
  const s16x2 v = __builtin_bit_cast(s16x2, pack_bf16(a, b));
  return __builtin_bit_cast(unsigned, __builtin_elementwise_max(v, s16x2{0, 0}));
}
DI void bar() { asm volatile("s_waitcnt lgkmcnt(0)" ::: "memory"); __builtin_amdgcn_s_barrier(); asm volatile("" ::: "memory"); }
template <int CTRL, int ROWS> DI unsigned dpp_add(unsigned x) { return x + (unsigned)__builtin_amdgcn_update_dpp(0, (int)x, CTRL, ROWS, 0xF, false); }
DI unsigned wave_incl_scan(unsigned x) {
  x = dpp_add<0x111, 0xF>(x); x = dpp_add<0x112, 0xF>(x); x = dpp_add<0x114, 0xF>(x); x = dpp_add<0x118, 0xF>(x);
  x = dpp_add<0x142, 0xA>(x); x = dpp_add<0x143, 0xC>(x);
  return x;
}
DI __amdgpu_buffer_rsrc_t rsrc(const void* p, unsigned bytes) { return __builtin_amdgcn_make_buffer_rsrc((void*)p, 0, bytes, 0x00020000); }
DI v4u srd(const void* p, unsigned bytes) { return v4u{(unsigned)(u64)p, (unsigned)((u64)p >> 32) & 0xffffu, bytes, 0x00020000u}; }
template <int I, int E, typename F> DI void static_for(F&& f) { if constexpr (I < E) { f(std::integral_constant<int, I>{}); static_for<I + 1, E>(f); } }

DI void tile_w2(__amdgpu_buffer_rsrc_t w2r, __amdgpu_buffer_rsrc_t wsr, int f, int lane) {
  const int ww = f >> 5, ks = (f >> 1) & 15, nt = f & 1, i = lane & 15, kg = lane >> 4;
  const int qo = (2 * ks + (kg >> 1)) & 3;
  const int row0 = 64 * (ks >> 1) + 32 * (qo & 1) + 4 * (qo >> 1) + 2 * (kg & 1);
  const unsigned o = (row0 * D + 32 * ww + 8 * (i >> 2) + 4 * nt + (i & 3)) * 2;
  unsigned v[8];
#pragma unroll
  for (int j = 0; j < 8; ++j) v[j] = __builtin_amdgcn_raw_buffer_load_b16(w2r, o + (j >> 2) * D * 2, (j & 3) * 8 * D * 2, 0);
  const v4u p = {v[0] | (v[1] << 16), v[2] | (v[3] << 16), v[4] | (v[5] << 16), v[6] | (v[7] << 16)};
  __builtin_amdgcn_raw_buffer_store_b128(p, wsr, WS_W2T + f * 1024 + lane * 16, 0, SC1);
}

constexpr int PB = 96, NY = 4;
template <int G> DI void load_w1(v4u d, unsigned off) {
  unsigned so;
  asm volatile("s_mov_b32 %0, %3\n\t"
               "buffer_load_dwordx4 v[%c5:%c5+3], %1, %2, %0 offen\n\tbuffer_load_dwordx4 v[%c5+4:%c5+7], %1, %2, %0 offen offset:1024\n\t"
               "buffer_load_dwordx4 v[%c5+8:%c5+11], %1, %2, %0 offen offset:2048\n\tbuffer_load_dwordx4 v[%c5+12:%c5+15], %1, %2, %0 offen offset:3072\n\t"
               "s_mov_b32 %0, %4\n\t"
               "buffer_load_dwordx4 v[%c5+16:%c5+19], %1, %2, %0 offen\n\tbuffer_load_dwordx4 v[%c5+20:%c5+23], %1, %2, %0 offen offset:1024\n\t"
               "buffer_load_dwordx4 v[%c5+24:%c5+27], %1, %2, %0 offen offset:2048\n\tbuffer_load_dwordx4 v[%c5+28:%c5+31], %1, %2, %0 offen offset:3072"
               : "=&s"(so) : "v"(off), "s"(d), "i"(G * 65536), "i"(G * 65536 + 4096), "i"(PB + 32 + 32 * G) : "v255");
}
template <int G> DI void xpose() {
#pragma unroll
  for (int d = 0; d < 4; ++d)
#pragma unroll
    for (int p = 0; p < 2; ++p) {
      unsigned t0, t1;
      asm volatile("v_perm_b32 v[%c5], v[%c4+4], v[%c4], %2\n\tv_perm_b32 v[%c5+16], v[%c4+6], v[%c4+2], %2\n\t"
                   "v_perm_b32 v[%c5+4], v[%c4+4], v[%c4], %3\n\tv_perm_b32 v[%c5+20], v[%c4+6], v[%c4+2], %3\n\t"
                   "v_mov_b32 %0, v[%c5]\n\tv_mov_b32 %1, v[%c5+4]\n\t"
                   "v_mov_b32_dpp v[%c5], v[%c5+16] row_ror:8 row_mask:0xf bank_mask:0xc\n\t"
                   "v_mov_b32_dpp v[%c5+4], v[%c5+20] row_ror:8 row_mask:0xf bank_mask:0xc\n\t"
                   "v_mov_b32_dpp v[%c5+16], %0 row_ror:8 row_mask:0xf bank_mask:0x3\n\t"
                   "v_mov_b32_dpp v[%c5+20], %1 row_ror:8 row_mask:0xf bank_mask:0x3"
                   : "=&v"(t0), "=&v"(t1) : "s"(0x05040100u), "s"(0x07060302u), "i"(PB + 32 + 32 * G + 8 * d + p), "i"(PB + 32 * G + 8 * p + d));
    }
}
constexpr int w2reg(int i) { return i < 8 ? PB + 128 + 4 * i : PB + 4 * (i - 8 - NY); }
template <int I> DI void load_w2(v4u d, unsigned off) {
  unsigned so;
  asm volatile("s_mov_b32 %0, %3\n\tbuffer_load_dwordx4 v[%c4:%c4+3], %1, %2, %0 offen offset:%c5 sc1"
               : "=&s"(so) : "v"(off), "s"(d), "i"((I >> 2) * 4096), "i"(w2reg(I)), "i"((I & 3) * 1024));
}
template <int A> DI void mfma_p(f32x4& acc, v4u b) { asm volatile("v_mfma_f32_16x16x32_bf16 %0, v[%c2:%c2+3], %1, %0" : "+v"(acc) : "v"(b), "i"(A)); }
template <int A> DI void mfma_p0(f32x4& acc, v4u b, f32x4 c) {
  asm volatile("v_mfma_f32_16x16x32_bf16 %0, v[%c3:%c3+3], %1, %2" : "=&v"(acc) : "v"(b), "v"(c), "i"(A));
}
DI void mfma_r(f32x4& acc, v4u a, v4u b) { asm volatile("v_mfma_f32_16x16x32_bf16 %0, %1, %2, %0" : "+v"(acc) : "v"(a), "v"(b)); }
template <bool DEV> DI void touch(v4u d, unsigned off) {
  if constexpr (DEV) asm volatile("buffer_load_dword v[%c2], %0, %1, 0 offen sc1" :: "v"(off), "s"(d), "i"(PB - 1));
  else asm volatile("buffer_load_dword v[%c2], %0, %1, 0 offen" :: "v"(off), "s"(d), "i"(PB - 1));
}
DI void count_row(unsigned a) {
  asm volatile("s_mov_b64 exec, 1\n\tds_add_u32 %0, %1\n\ts_mov_b64 exec, -1" :: "v"(a), "v"(1u) : "memory");
}
template <int N> DI void wait_vm() { asm volatile("s_waitcnt vmcnt(%c0)" :: "i"(N) : "memory"); }

__global__ __launch_bounds__(512, 2) __attribute__((amdgpu_num_vgpr(PB / 2 - 1)))
void gin_one(const unsigned short* __restrict__ x, const int* __restrict__ src, const int* __restrict__ dst,
             const unsigned short* __restrict__ epsp, const unsigned short* __restrict__ w1, const unsigned short* __restrict__ b1,
             const unsigned short* __restrict__ w2, const unsigned short* __restrict__ b2, unsigned short* __restrict__ out,
             unsigned char* ws, int N, int E, int T) {
  extern __shared__ __attribute__((aligned(16))) unsigned char Ls[];
  LAS unsigned char* L = (LAS unsigned char*)Ls;
  const int tid = threadIdx.x, lane = tid & 63, tid0 = __builtin_amdgcn_readfirstlane(tid), w = tid0 >> 6;
  const int t = blockIdx.x, node0 = t * TM;
  const unsigned bulk = w >> 2, chain = bulk ^ 1;
  const int rank = (t >> 3) & 31;
  {
    u64 pc; unsigned len;
    asm volatile("s_getpc_b64 %0\n.Lgin_pc:\n\ts_mov_b32 %1, .Lgin_end-.Lgin_pc" : "=s"(pc), "=s"(len));
    asm volatile("s_cmp_gt_u32 %3, 63\n\ts_cbranch_scc1 1f\n\tbuffer_load_dword v[%c2], %0, %1, 0 offen\n1:"
                 :: "v"(lane < 8 ? (rank * 8 + lane) * 128 : OOB), "s"(srd((const void*)pc, len)), "i"(PB - 1), "s"(tid0) : "scc");
  }
  SB();
  asm volatile("" :: "s"(x), "s"(src), "s"(dst), "s"(epsp), "s"(w1), "s"(b1), "s"(w2), "s"(b2), "s"(out), "s"(ws), "s"(N), "s"(E), "s"(T));
  const int share2 = (T >> 3) + rank;
  const __amdgpu_buffer_rsrc_t wsr = rsrc(ws, WS_BYTES), xr = rsrc(x, N * 512), w2r = rsrc(w2, D * H * 2);
  const v4u w1d = srd(w1, D * H * 2), wsd = srd(ws, WS_BYTES);
  const int q = lane >> 4, m = lane & 15;
  const unsigned w1off = (((lane >> 3) & 1) * 32 + q * 8) * 1024 + w * 128 + (lane & 7) * 16;
  LAS unsigned* pcnt = (LAS unsigned*)(L + L_PCNT);
  LAS unsigned* pcell = (LAS unsigned*)(L + L_PCELL);
  const int tf = t + (w - 1) * T;

  if (w == 4) {
    PRIO(3);
    const __amdgpu_buffer_rsrc_t dr = rsrc(dst, E * 4), sr = rsrc(src, E * 4);
    v4u dv[3], sv[3];
#pragma unroll
    for (int i = 0; i < 3; ++i) {
      const unsigned o = (i == 2 && lane >= 32) ? OOB : (unsigned)(t * EPB + (lane + 64 * i) * 4) * 4u;
      dv[i] = __builtin_amdgcn_raw_buffer_load_b128(dr, o, 0, 0);
      sv[i] = __builtin_amdgcn_raw_buffer_load_b128(sr, o, 0, 0);
    }
    const u64 ep0 = __hip_atomic_load((GAS u64*)(ws + WS_EPOCH), RLX_AGENT);
    SB();
    *(LAS v4u*)(pcnt + lane * 4) = v4u{0, 0, 0, 0};
    pcnt[256 + lane] = 0;
    const unsigned dummy = 256u + lane;
    unsigned tt[12], rk[12];
#pragma unroll
    for (int i = 0; i < 12; ++i) {
      const int e = t * EPB + (lane + 64 * (i >> 2)) * 4;
      const unsigned lo = (e < E && !(i >= 8 && lane >= 32)) ? 0u : dummy;
      tt[i] = max(min(__umul24(min(dv[i >> 2][i & 3], 16383u), 52429u) >> 21, dummy), lo);
      rk[i] = __hip_atomic_fetch_add(pcnt + tt[i], 1u, RLX_WG);
    }
#pragma unroll
    for (int i = 0; i < 12; ++i)
      pcell[tt[i] * 16 + (rk[i] & 15u)] = (dv[i >> 2][i & 3] - tt[i] * TM) | (min(sv[i >> 2][i & 3], 16383u) << 6);
    *(LAS u64*)(L + L_MISC) = ep0;
  } else {
    __builtin_amdgcn_s_sleep(3);
    if (w == 0 || (w == 5 && share2 < 32)) touch<false>(w1d, (w ? share2 : rank) * 8192 + lane * 128);
    if (w == 3) {
      const v4u v1 = __builtin_amdgcn_raw_buffer_load_b128(rsrc(b1, H * 2), lane * 16, 0, 0);
      const v4u v2 = __builtin_amdgcn_raw_buffer_load_b128(rsrc(b2, D * 2), lane * 16, 0, 0);
      const unsigned ev = epsp[0];
      *(LAS unsigned*)(L + L_CNT + lane * 4) = 0;
#pragma unroll
      for (int i = 0; i < 8; ++i)
        *(LAS float*)(L + L_B1 + ((lane >> 3) * 64 + (((lane >> 2) & 1) + 2 * (i >> 2)) * 16 + 4 * (i & 3) + (lane & 3)) * 4) =
            (i & 1) ? bfhi(v1[i >> 1]) : bflo(v1[i >> 1]);
      *(LAS v4u*)(L + (lane < 32 ? L_B2 + lane * 16 : L_FLAT + lane * 16)) = v2;
      *(LAS v2u*)(L + L_MISC + 8) = v2u{0u, ev};
      *(LAS v4u*)(L + L_MISC + 16) = v4u{0u, 0u, 0u, 0u};
    } else if ((w == 1 || w == 2) && tf < 256) {
      tile_w2(w2r, wsr, tf, lane);
    }
  }
  if (chain) { load_w1<0>(w1d, w1off); load_w1<1>(w1d, w1off); }
  PRIO(3);
  bar();
  const u64 tag = *(LAS u64*)(L + L_MISC) + 1;
  const unsigned tagA = (unsigned)tag & 0xFFFFFFu, tagB = ((unsigned)tag + (unsigned)(tag >> 24) * 0x9E3779B1u) & 0x7FFFFu;
  {
    v4u e[2]; unsigned c[2];
#pragma unroll
    for (int i = 0; i < 2; ++i) { e[i] = *(LAS v4u*)(pcell + (w * 2 + i) * 256 + lane * 4); c[i] = pcnt[(w * 2 + i) * 16 + (lane >> 2)]; }
#pragma unroll
    for (int i = 0; i < 2; ++i) {
      const unsigned tc = (w * 2 + i) * 16 + (lane >> 2), cc = min(c[i], 31u);
      const v4u g = {(e[i].x & 0xFFFFFu) | (e[i].y << 20), ((e[i].y >> 12) & 0xFFu) | (tagA << 8), (e[i].z & 0xFFFFFu) | (e[i].w << 20),
                     ((e[i].w >> 12) & 0xFFu) | (cc << 8) | (tagB << 13)};
      __builtin_amdgcn_raw_buffer_store_b128(g, wsr, tc < (unsigned)T ? WS_CELL + tc * 16384 + t * 64 + (lane & 3) * 16 : OOB, 0, SC1);
    }
  }
  SB();
  PRIO(1);
  if (chain) { load_w1<2>(w1d, w1off); load_w1<3>(w1d, w1off); } else __builtin_amdgcn_s_sleep(20);
  bool any2 = false;
  unsigned ent[16], rk[16];
  LAS unsigned* flat = (LAS unsigned*)(L + L_FLAT);
  if (chain) {
    PRIO(2);
    v4u g[4];
    const unsigned coff = WS_CELL + t * 16384 + w * 4096 + lane * 16;
#pragma unroll
    for (int i = 0; i < 4; ++i) g[i] = __builtin_amdgcn_raw_buffer_load_b128(wsr, coff + i * 1024, 0, SC1);
    bool tmo = false;
    const int pl = T - 64 * w - (lane >> 2);
#define GR_OK(i) (16 * (i) >= pl || ((g[i].y >> 8) == tagA && (g[i].w >> 13) == tagB))
    if (!__all(GR_OK(0) && GR_OK(1) && GR_OK(2) && GR_OK(3))) {
      for (unsigned spins = 0;;) {
#pragma unroll
        for (int i = 0; i < 4; ++i) {
          const bool ok = GR_OK(i);
          const v4u nv = __builtin_amdgcn_raw_buffer_load_b128(wsr, ok ? OOB : coff + i * 1024, 0, SC1);
#pragma unroll
          for (int k = 0; k < 4; ++k) g[i][k] = ok ? g[i][k] : nv[k];
        }
        if (__all(GR_OK(0) && GR_OK(1) && GR_OK(2) && GR_OK(3))) break;
        if (++spins > SPIN_CAP) { tmo = true; break; }
      }
    }
    LAS unsigned char* stg = L + L_PSTG + w * 5120;
#pragma unroll
    for (int i = 0; i < 4; ++i) *(LAS v4u*)(stg + (16 * i + (lane >> 2)) * 80 + (lane & 3) * 16) = g[i];
#pragma unroll
    for (int i = 0; i < 4; ++i) g[i] = *(LAS v4u*)(stg + lane * 80 + i * 16);
    const int nval = (64 * w + lane < T && !tmo) ? (int)((g[0].w >> 8) & 31u) : 0;
    any2 = __any(nval > 8);
    bool bad = nval > 16;
    const unsigned dummy = 40u + (unsigned)(lane % 24);
    auto rank8 = [&](int lo) INL {
#pragma unroll
      for (int i = lo; i < lo + 8; ++i) {
        const unsigned a = g[i >> 2][i & 2], b = g[i >> 2][(i & 2) + 1];
        ent[i] = i < nval ? ((i & 1) ? __builtin_amdgcn_alignbit(b, a, 20) : a) & 0xFFFFFu : dummy;
        rk[i] = __hip_atomic_fetch_add((LAS unsigned*)(L + L_CNT + ((ent[i] & 63u) << 2)), 1u << (8 * w), RLX_WG);
      }
#pragma unroll
      for (int i = lo; i < lo + 8; ++i) { rk[i] = (rk[i] >> (8 * w)) & 255u; bad |= rk[i] == 255u; }
    };
    rank8(0);
    if (__builtin_expect(any2, 0)) rank8(8);
    if ((__any(bad) || tmo) && lane == 0) __hip_atomic_fetch_or((LAS unsigned*)(L + L_MISC + 8), tmo ? 3u : 1u, RLX_WG);
  } else {
    PRIO(0);
    load_w1<0>(w1d, w1off); load_w1<1>(w1d, w1off);
    wait_vm<8>(); xpose<0>();
    wait_vm<0>(); xpose<1>();
    load_w1<2>(w1d, w1off); load_w1<3>(w1d, w1off);
    if (w < 7) touch<false>(srd(x, N * 512), (w - 4) * 64 + lane < 160 ? node0 * 512 + ((w - 4) * 64 + lane) * 128 : OOB);
  }
  bar();
  bool slow, timedout;
  int s0, nit;
  u64 pk = 0;
  {
    const unsigned cw = *(LAS unsigned*)(L + L_CNT + lane * 4);
    const unsigned flagv = __builtin_amdgcn_readfirstlane(*(LAS unsigned*)(L + L_MISC + 8));
    const unsigned deg = __builtin_amdgcn_sad_u8(cw, 0u, 0u), below = __builtin_amdgcn_sad_u8(cw & ((1u << (8 * (w & 3))) - 1u), 0u, 0u);
    const unsigned l4 = (deg + 4u) & ~3u;
    const unsigned plen = lane < TM ? l4 + (lane % 5 == 4 ? 4u * (R - 1) : 0u) : 0u;
    const unsigned incl = wave_incl_scan(plen), start = incl - plen;
    timedout = (flagv & 2u) != 0u;
    slow = flagv != 0u || __builtin_amdgcn_readlane(incl, 63) > (unsigned)CAP;
    const unsigned tail = __builtin_amdgcn_readlane(incl, 5 * w + 4) - 4 * (R - 1);
    s0 = __builtin_amdgcn_readlane(start, 5 * w);
    nit = ((tail - s0) / 4 + R - 1) / R;
#pragma unroll
    for (int j = 0; j < 5; ++j) pk |= (u64)(((unsigned)__builtin_amdgcn_readlane(l4, 5 * w + j) >> 2) & 0xFFFu) << (12 * j);
    if (slow) s0 = 0;
    if (!slow) {
      const unsigned st2 = (unsigned)__builtin_amdgcn_ds_bpermute((5 * w + (lane >> 3)) << 2, (int)(start | (deg << 16)));
      if (chain) {
        const unsigned B4 = L_FLAT + 4u * (lane < TM ? start + 1u + below : CAP + 64u);
        auto place8 = [&](int lo) INL {
          unsigned b[8];
#pragma unroll
          for (int i = 0; i < 8; ++i) b[i] = (unsigned)__builtin_amdgcn_ds_bpermute((int)((ent[lo + i] & 63u) << 2), (int)B4);
#pragma unroll
          for (int i = 0; i < 8; ++i) *(LAS unsigned*)(L + b[i] + 4u * rk[lo + i]) = ent[lo + i];
        };
        place8(0);
        if (__builtin_expect(any2, 0)) place8(8);
      }
      const unsigned dg = st2 >> 16, k = lane & 7, pp = k == 7 ? 0u : dg + 1u + k;
      const bool mine = lane < 40 && pp < ((dg + 4u) & ~3u);
      const unsigned at = mine ? (st2 & 0xFFFFu) + pp : (lane >= 48 && lane - 48 < 4 * (R - 1) ? tail + lane - 48 : CAP + lane);
      flat[at] = (mine && k == 7) ? (unsigned)(node0 + 5 * w + (lane >> 3)) << 6 : EOOB;
    }
  }
  if (bulk) { wait_vm<8>(); xpose<2>(); }
  bar();
  PRIO(2);
  if (w == 1 || w == 2) {
    wait_vm<0>();
    if (lane == 0 && tf < 256) __hip_atomic_store((GAS unsigned*)(ws + WS_WFLAG) + tf, (unsigned)tag, RLX_AGENT);
  }
  const unsigned l16 = m * 16;
  LAS unsigned* fp = flat + s0 + q;
  v4u bl[R], br[R];
  unsigned en[R];
#pragma unroll
  for (int i = 0; i < R; ++i) en[i] = fp[4 * i];
  const float epsf = bflo(*(LAS unsigned*)(L + L_MISC + 12));
#pragma unroll
  for (int i = 0; i < R; ++i) {
    const unsigned o = ((en[i] << 3) & ~511u) | l16;
    bl[i] = __builtin_amdgcn_raw_buffer_load_b128(xr, o, 0, 0);
    br[i] = __builtin_amdgcn_raw_buffer_load_b128(xr, o + 256, 0, 0);
    en[i] = fp[4 * (R + i)];
  }
  asm volatile("" ::: "memory");
  SB();
  if (chain) { PRIO(0); xpose<0>(); xpose<1>(); xpose<2>(); }
  if (w == 7) wait_vm<2 * R>(); else wait_vm<2 * R + 1>();
  xpose<3>();
  PRIO(0);
  int stage = 0;
  bool xdone = false;
  const unsigned w2off = WS_W2T + w * 32768 + lane * 16;
  v4u gb[2];
  f32x4 gacc[4];
  unsigned ghb = 0;
  auto gemm1 = [&](int mt, auto LAST, auto PART) INL {
    constexpr int part = PART;
    if constexpr (part == 0) {
      ghb = L_H + (mt * 16 + m) * HP + q * 16;
#pragma unroll
      for (int i = 0; i < 2; ++i) gb[i] = *(LAS v4u*)(L + ghb + i * 64);
#pragma unroll
      for (int c = 0; c < 4; ++c) gacc[c] = *(LAS f32x4*)(L + L_B1 + (64 * w + 16 * q) * 4 + 16 * c);
    }
    static_for<3 * part, (part == 2 ? 8 : 3 * part + 3)>([&](auto KS) {
      constexpr int ks = KS;
      if constexpr (LAST && (ks & 1) == 0) PRIO(3 - ks / 2);
      static_for<0, 4>([&](auto C) { mfma_p<PB + 16 * ks + 4 * C>(gacc[C], gb[ks & 1]); });
      if constexpr (ks + 2 < 8) gb[ks & 1] = *(LAS v4u*)(L + ghb + (ks + 2) * 64);
      if constexpr (LAST) load_w2<8 + NY + ks>(wsd, w2off);
      SB();
    });
    if constexpr (part == 2) {
      asm volatile("s_nop 7" : "+v"(gacc[0]), "+v"(gacc[1]), "+v"(gacc[2]), "+v"(gacc[3]));
      LAS unsigned char* h1w = L + L_H1 + (mt * 16 + m) * H1P + (64 * w + 16 * q) * 2;
#pragma unroll
      for (int half = 0; half < 2; ++half) {
        unsigned o[4];
#pragma unroll
        for (int k = 0; k < 4; ++k) o[k] = relu_pack(gacc[2 * half + (k >> 1)][2 * (k & 1)], gacc[2 * half + (k >> 1)][2 * (k & 1) + 1]);
        *(LAS v4u*)(h1w + 16 * half) = v4u{o[0], o[1], o[2], o[3]};
      }
    }
  };
  typedef std::integral_constant<int, 0> I0; typedef std::integral_constant<int, 1> I1; typedef std::integral_constant<int, 2> I2;
  auto wait_rows = [&](int mt) INL {
    while (__builtin_amdgcn_readfirstlane(__hip_atomic_load((LAS unsigned*)(L + L_MISC + 20 + 4 * mt), RLX_WG)) < (mt < 2 ? 16u : 8u))
      __builtin_amdgcn_s_sleep(1);
    asm volatile("" ::: "memory");
  };
  auto early_head = [&]() INL {
    if (stage == 0) {
      asm volatile("buffer_load_dwordx4 v[%c2:%c2+3], %0, %1, 0 offen sc1" :: "v"(WS_WFLAG + lane * 16), "s"(wsd), "i"(PB + 156));
    } else {
      unsigned d0, d1;
      asm volatile("v_xor_b32 %0, %2, v[%c3]\n\tv_xor_b32 %1, %2, v[%c3+1]\n\tv_or_b32 %0, %0, %1\n\tv_xor_b32 %1, %2, v[%c3+2]\n\t"
                   "v_or_b32 %0, %0, %1\n\tv_xor_b32 %1, %2, v[%c3+3]\n\tv_or_b32 %0, %0, %1"
                   : "=&v"(d0), "=&v"(d1) : "s"((unsigned)tag), "i"(PB + 156));
      xdone = __all(d0 == 0u);
      if (xdone) static_for<0, 8>([&](auto I) { load_w2<I>(wsd, w2off); });
    }
  };
  if (__builtin_expect(!slow, 1)) {
    const v4u sel = {m == 0 ? 0x3F80u : (m == 1 ? 0x3F800000u : 0u), m == 2 ? 0x3F80u : (m == 3 ? 0x3F800000u : 0u),
                     m == 4 ? 0x3F80u : (m == 5 ? 0x3F800000u : 0u), m == 6 ? 0x3F80u : (m == 7 ? 0x3F800000u : 0u)};
#define SELF(k) (unsigned)__builtin_amdgcn_update_dpp((int)sel[k], (int)sel[k], 0x128, 0x1, 0xC, false)
    v4u cur = {SELF(0), SELF(1), SELF(2), SELF(3)};
    fp += 8 * R;
    f32x4 a0 = {0.f, 0.f, 0.f, 0.f}, a1 = {0.f, 0.f, 0.f, 0.f};
    const unsigned hl = L_H + (lane >> 5) * 256 + m * 16 + (q & 1) * 8;
    unsigned rows = (32 + w) | ((2 * w) << 6) | ((2 * w + 1) << 12) | ((16 + 2 * w) << 18) | ((17 + 2 * w) << 24);
    unsigned cv = 0;
    int rem = (int)(pk & 0xFFF);
#define SLOT(i, REFILL) { \
        a0 = mfma(cur, bl[i], a0); \
        a1 = mfma(cur, br[i], a1); \
        cur = sel; \
        if (REFILL) { \
          const unsigned o = ((en[i] << 3) & ~511u) | l16; \
          bl[i] = __builtin_amdgcn_raw_buffer_load_b128(xr, o, 0, 0); \
          br[i] = __builtin_amdgcn_raw_buffer_load_b128(xr, o + 256, 0, 0); \
          en[i] = fp[4 * i]; \
        } \
        if (__builtin_expect(--rem == 0, 0)) { \
          float f[4]; \
          _Pragma("unroll") for (int r = 0; r < 4; ++r) { \
            auto s2 = __builtin_amdgcn_permlane32_swap(asu(a0[r]), asu(a1[r]), false, false); \
            const unsigned u0 = s2[0], u1 = s2[1]; \
            f[r] = fmaf(epsf, asf(u1), asf(u0)); \
          } \
          *(LAS v2u*)(L + hl + (rows & 63u) * HP) = v2u{pack_bf16(f[0], f[1]), pack_bf16(f[2], f[3])}; \
          count_row(L_MISC + 20 + ((rows & 48u) >> 2));       \
          rows >>= 6; \
          a0 = f32x4{0.f, 0.f, 0.f, 0.f}; a1 = a0; \
          cur = v4u{SELF(0), SELF(1), SELF(2), SELF(3)}; \
          pk >>= 12; \
          rem = (int)(pk & 0xFFF); \
        } }
    if (w == 6 || (w == 7 && share2 < 32)) touch<true>(wsd, WS_W2T + (w == 7 ? share2 : rank) * 8192 + lane * 128);
    static_assert(R == 3, "GEMM1 comes in three parts, one behind each slot of a round");
    for (; nit > 1; --nit) {
      const bool go = stage < 2 && __builtin_amdgcn_readfirstlane(cv) >= (stage ? 16u : 8u);
      asm volatile("" ::: "memory");
      SLOT(0, true)
      if (go) { early_head(); gemm1(stage ? 0 : 2, std::false_type{}, I0{}); }
      SLOT(1, true)
      if (go) gemm1(0, std::false_type{}, I1{});
      SLOT(2, true)
      if (go) { gemm1(stage ? 0 : 2, std::false_type{}, I2{}); ++stage; }
      fp += 4 * R;
      cv = __hip_atomic_load((LAS unsigned*)(L + L_MISC + 20 + (stage ? 0 : 8)), RLX_WG);
    }
#pragma unroll
    for (int i = 0; i < R; ++i) SLOT(i, false)
  } else {
#pragma unroll 1
    for (int j = 0; j < 5; ++j) {
      const int node = node0 + 5 * w + j;
      const v2u s2 = __builtin_amdgcn_raw_buffer_load_b64(xr, (unsigned)node * 512u + lane * 8, 0, 0);
      float a0 = bflo(s2.x), a1 = bfhi(s2.x), a2 = bflo(s2.y), a3 = bfhi(s2.y);
      if (node < N)
        for (int eb = 0; eb < E; eb += 64) {
          const int e = eb + lane;
          const int dv2 = (int)__builtin_amdgcn_raw_buffer_load_b32(rsrc(dst, E * 4), e * 4, 0, 0);
          const int sv2 = (int)__builtin_amdgcn_raw_buffer_load_b32(rsrc(src, E * 4), e * 4, 0, 0);
          u64 mkk = __ballot(dv2 == node && e < E);
          while (mkk) {
            const int b = __builtin_ctzll(mkk); mkk &= mkk - 1;
            const unsigned sidx = (unsigned)__builtin_amdgcn_readlane(sv2, b);
            const v2u rv = __builtin_amdgcn_raw_buffer_load_b64(xr, sidx < (unsigned)N ? sidx * 512u + lane * 8 : OOB, 0, 0);
            a0 += bflo(rv.x); a1 += bfhi(rv.x); a2 += bflo(rv.y); a3 += bfhi(rv.y);
          }
        }
      const int row = j ? ((j - 1) >> 1) * 16 + 2 * w + ((j - 1) & 1) : 32 + w;
      *(LAS v2u*)(L + L_H + row * HP + lane * 8) =
          v2u{pack_bf16(fmaf(epsf, bflo(s2.x), a0), fmaf(epsf, bfhi(s2.x), a1)), pack_bf16(fmaf(epsf, bflo(s2.y), a2), fmaf(epsf, bfhi(s2.y), a3))};
      count_row(L_MISC + 20 + (row >> 4) * 4);
    }
  }
  if (!timedout && tid == 192 && t == 0) __hip_atomic_store((GAS u64*)(ws + WS_EPOCH), tag, RLX_AGENT);
#pragma unroll 1
  for (; stage < 2; ++stage) {
    wait_rows(stage ? 0 : 2); wait_vm<0>(); early_head();
    gemm1(stage ? 0 : 2, std::false_type{}, I0{}); gemm1(0, std::false_type{}, I1{}); gemm1(stage ? 0 : 2, std::false_type{}, I2{});
  }
  if (__builtin_expect(!xdone, 0)) {
    bool okw = false;
    for (unsigned sp = 0; sp <= SPIN_CAP && !okw; ++sp) {
      const v4u fl = __builtin_amdgcn_raw_buffer_load_b128(wsr, WS_WFLAG + lane * 16, 0, SC1);
      okw = __all(fl.x == (unsigned)tag && fl.y == (unsigned)tag && fl.z == (unsigned)tag && fl.w == (unsigned)tag);
    }
    if (!okw) {
#pragma unroll 1
      for (int i = 0; i < 32; ++i) tile_w2(w2r, wsr, w * 32 + i, lane);
    }
    wait_vm<0>();
    static_for<0, 8>([&](auto I) { load_w2<I>(wsd, w2off); });
  }
  v4u wy[NY];
#pragma unroll
  for (int i = 0; i < NY; ++i) wy[i] = __builtin_amdgcn_raw_buffer_load_b128(wsr, w2off + (i & 3) * 1024, (2 + (i >> 2)) * 4096, SC1);
  SB();
  const v4u bb2 = *(LAS v4u*)(L + L_B2 + (32 * w + 8 * q) * 2);
  wait_rows(1);
#pragma unroll
  for (int i = 0; i < NY; ++i) asm volatile("" : "+v"(wy[i]));
  gemm1(1, std::true_type{}, I0{}); gemm1(1, std::true_type{}, I1{}); gemm1(1, std::true_type{}, I2{});
  f32x4 bias2[2];
#pragma unroll
  for (int nt = 0; nt < 2; ++nt) {
    bias2[nt] = f32x4{bflo(bb2[2 * nt]), bfhi(bb2[2 * nt]), bflo(bb2[2 * nt + 1]), bfhi(bb2[2 * nt + 1])};
    asm volatile("" : "+v"(bias2[nt]));
  }
  SB();
  bar();
  {
    f32x4 acc[3][2];
    LAS unsigned char* hb = L + L_H1 + m * H1P + q * 16;
    const __amdgpu_buffer_rsrc_t outr = rsrc(out, N * 512);
    auto put = [&](int mt) INL {
      const int r = mt < 2 ? 5 * (m >> 1) + 1 + 2 * mt + (m & 1) : (m < 8 ? 5 * m : TM);
      const v4u o = {pack_bf16(acc[mt][0][0], acc[mt][0][1]), pack_bf16(acc[mt][0][2], acc[mt][0][3]),
                     pack_bf16(acc[mt][1][0], acc[mt][1][1]), pack_bf16(acc[mt][1][2], acc[mt][1][3])};
      __builtin_amdgcn_raw_buffer_store_b128(o, outr, r < TM ? (unsigned)(node0 + r) * 512u + 64 * w + 16 * q : OOB, 0, SC1);
    };
    constexpr int P2 = 8, F0 = 16 + NY;
    v4u b[P2];
#define U2_KS(u) ((u) < 24 ? (u) / 3 : 8 + ((u) - 24) % 8)
#define U2_MT(u) ((u) < 24 ? (u) % 3 : ((u) - 24) / 8)
#pragma unroll
    for (int u = 0; u < P2; ++u) b[u] = *(LAS v4u*)(hb + U2_MT(u) * 16 * H1P + U2_KS(u) * 64);
    static_for<0, 48>([&](auto U) {
      constexpr int u = U, ks = U2_KS(u), mt = U2_MT(u);
      if constexpr (u % 12 == 0) PRIO(3 - u / 12);
      if constexpr (u < 2 * (32 - F0) && (u & 1) == 0) load_w2<F0 + u / 2>(wsd, w2off);
      constexpr int issued = u < 2 * (32 - F0) ? F0 + 1 + u / 2 : 32;
      if constexpr (mt == 0 && 2 * ks >= 8 + NY) wait_vm<issued - (2 * ks + 2)>();
      static_for<0, 2>([&](auto NT) {
        constexpr int f = 2 * ks + NT;
        if constexpr (ks == 0) mfma_p0<w2reg(f)>(acc[mt][NT], b[u % P2], bias2[NT]);
        else if constexpr (f >= 8 && f < 8 + NY) mfma_r(acc[mt][NT], wy[f - 8], b[u % P2]);
        else mfma_p<w2reg(f)>(acc[mt][NT], b[u % P2]);
      });
      if constexpr (u + P2 < 48) b[u % P2] = *(LAS v4u*)(hb + U2_MT(u + P2) * 16 * H1P + U2_KS(u + P2) * 64);
      if constexpr (u == 32) {
        if (w == 3) asm volatile("buffer_load_dwordx2 v[%c2:%c2+1], %0, %1, 0 offen sc1" :: "v"(WS_EPOCH), "s"(wsd), "i"(PB - 2));
      }
      if constexpr (u == 36 || u == 44) put(mt - 1);
      SB();
    });
    asm volatile("s_nop 7" : "+v"(acc[2][0]), "+v"(acc[2][1]));
    put(2);
  }
  if (!timedout && w == 3) {
    unsigned e0, e1;
    asm volatile("s_waitcnt vmcnt(3)\n\tv_mov_b32 %0, v[%c2]\n\tv_mov_b32 %1, v[%c2+1]" : "=v"(e0), "=v"(e1) : "i"(PB - 2));
    if ((e0 != (unsigned)tag || e1 != (unsigned)(tag >> 32)) && lane == 0) __hip_atomic_store((GAS u64*)(ws + WS_EPOCH), tag, RLX_AGENT);
  }
  asm volatile(".Lgin_end:");
}

__global__ __launch_bounds__(256) void gin_any(const unsigned short* __restrict__ x, const int* __restrict__ src, const int* __restrict__ dst,
                                               const unsigned short* __restrict__ epsp, const unsigned short* __restrict__ w1,
                                               const unsigned short* __restrict__ b1, const unsigned short* __restrict__ w2,
                                               const unsigned short* __restrict__ b2, unsigned short* __restrict__ out, int N, int E) {
  __shared__ float h[D], h1[H];
  const int node = blockIdx.x, c = threadIdx.x, lane = c & 63;
  const float xs = bflo(x[(size_t)node * D + c]);
  float a = xs;
  for (int eb = 0; eb < E; eb += 64) {
    const int e = eb + lane, dv = e < E ? dst[e] : -1, sv = e < E ? src[e] : 0;
    u64 mk = __ballot(dv == node);
    while (mk) {
      const int s = __builtin_amdgcn_readlane(sv, __builtin_ctzll(mk));
      mk &= mk - 1;
      if ((unsigned)s < (unsigned)N) a += bflo(x[(size_t)s * D + c]);
    }
  }
  h[c] = bflo(pack_bf16(fmaf(bflo(epsp[0]), xs, a), 0.f));
  __syncthreads();
  for (int j = c; j < H; j += 256) {
    float s = bflo(b1[j]);
    for (int k = 0; k < D; ++k) s = fmaf(h[k], bflo(w1[k * H + j]), s);
    h1[j] = bflo(relu_pack(s, 0.f));
  }
  __syncthreads();
  float s = bflo(b2[c]);
  for (int k = 0; k < H; ++k) s = fmaf(h1[k], bflo(w2[k * D + c]), s);
  out[(size_t)node * D + c] = (unsigned short)pack_bf16(s, 0.f);
}

extern "C" void kernel_launch(void* const* d_in, const int* in_sizes, int n_in, void* d_out, int out_size, void* d_ws, size_t ws_size,
                              hipStream_t stream) {
  const unsigned short* x = (const unsigned short*)d_in[0];
  const int* src = (const int*)d_in[1];
  const int* dst = (const int*)d_in[2];
  const unsigned short* eps = (const unsigned short*)d_in[3];
  const unsigned short* w1 = (const unsigned short*)d_in[4];
  const unsigned short* b1 = (const unsigned short*)d_in[5];
  const unsigned short* w2 = (const unsigned short*)d_in[6];
  const unsigned short* b2 = (const unsigned short*)d_in[7];
  const int N = in_sizes[0] / D, E = in_sizes[1], T = (N + TM - 1) / TM;
  if (T >= 128 && T <= 256 && E <= EPB * T && E % 4 == 0 && ws_size >= WS_BYTES) {
    (void)hipFuncSetAttribute((const void*)gin_one, hipFuncAttributeMaxDynamicSharedMemorySize, L_END);
    gin_one<<<T, 512, L_END, stream>>>(x, src, dst, eps, w1, b1, w2, b2, (unsigned short*)d_out, (unsigned char*)d_ws, N, E, T);
  } else if (N > 0) {
    gin_any<<<N, 256, 0, stream>>>(x, src, dst, eps, w1, b1, w2, b2, (unsigned short*)d_out, N, E);
  }
}
```

```cpp
#include <hip/hip_runtime.h>
#include <hip/hip_bf16.h>
#include <stdint.h>
#include <type_traits>

typedef unsigned v4u __attribute__((ext_vector_type(4)));
typedef unsigned v2u __attribute__((ext_vector_type(2)));
typedef float f32x4 __attribute__((ext_vector_type(4)));
typedef float f32x2 __attribute__((ext_vector_type(2)));
typedef short bf16x8 __attribute__((ext_vector_type(8)));
typedef short s16x2 __attribute__((ext_vector_type(2)));
typedef __bf16 bf16x2_t __attribute__((ext_vector_type(2)));
typedef unsigned long long u64;
#define GAS __attribute__((address_space(1)))
#define LAS __attribute__((address_space(3)))
#define DI __device__ __forceinline__
#define INL __attribute__((always_inline))
#define RLX_AGENT __ATOMIC_RELAXED, __HIP_MEMORY_SCOPE_AGENT
#define RLX_WG __ATOMIC_RELAXED, __HIP_MEMORY_SCOPE_WORKGROUP
#define SB() __builtin_amdgcn_sched_barrier(0)
#define PRIO(p) __builtin_amdgcn_s_setprio(p)

constexpr int D = 256, H = 512, TM = 40;
constexpr int EPB = 640;
constexpr int CAP = 1536;
constexpr int HP = 544, H1P = 1056;
constexpr int R = 3;
constexpr unsigned OOB = 0x40000000u;
constexpr unsigned EOOB = OOB >> 3;
constexpr int SC1 = 16;
constexpr unsigned SPIN_CAP = 4096;
constexpr unsigned WS_EPOCH = 0, WS_WFLAG = 4096, WS_W2T = 65536, WS_CELL = 1u << 20, WS_BYTES = WS_CELL + 256u * 16384u;
constexpr int L_H = 0, L_H1 = L_H + 48 * HP, L_FLAT = L_H1 + 48 * H1P, L_CNT = L_FLAT + (CAP + 128) * 4, L_MISC = L_CNT + 256,
              L_B1 = L_MISC + 32, L_B2 = L_B1 + 2048, L_END = L_B2 + 512;
constexpr int L_PSTG = L_H;
constexpr int L_PCELL = L_H1, L_PCNT = L_H1 + 320 * 64;

DI unsigned pack_bf16(float a, float b) { f32x2 v = {a, b}; bf16x2_t r = __builtin_convertvector(v, bf16x2_t); return __builtin_bit_cast(unsigned, r); }
DI float bflo(unsigned u) { return __builtin_bit_cast(float, u << 16); }
DI float bfhi(unsigned u) { return __builtin_bit_cast(float, u & 0xffff0000u); }
DI float asf(unsigned u) { return __builtin_bit_cast(float, u); }
DI unsigned asu(float u) { return __builtin_bit_cast(unsigned, u); }
DI f32x4 mfma(v4u a, v4u b, f32x4 c) {
  return __builtin_amdgcn_mfma_f32_16x16x32_bf16(__builtin_bit_cast(bf16x8, a), __builtin_bit_cast(bf16x8, b), c, 0, 0, 0);
}
DI unsigned relu_pack(float a, float b) {
  const s16x2 v = __builtin_bit_cast(s16x2, pack_bf16(a, b));
  return __builtin_bit_cast(unsigned, __builtin_elementwise_max(v, s16x2{0, 0}));
}
DI void bar() { asm volatile("s_waitcnt lgkmcnt(0)" ::: "memory"); __builtin_amdgcn_s_barrier(); asm volatile("" ::: "memory"); }
template <int CTRL, int ROWS> DI unsigned dpp_add(unsigned x) { return x + (unsigned)__builtin_amdgcn_update_dpp(0, (int)x, CTRL, ROWS, 0xF, false); }
DI unsigned wave_incl_scan(unsigned x) {
  x = dpp_add<0x111, 0xF>(x); x = dpp_add<0x112, 0xF>(x); x = dpp_add<0x114, 0xF>(x); x = dpp_add<0x118, 0xF>(x);
  x = dpp_add<0x142, 0xA>(x); x = dpp_add<0x143, 0xC>(x);
  return x;
}
DI __amdgpu_buffer_rsrc_t rsrc(const void* p, unsigned bytes) { return __builtin_amdgcn_make_buffer_rsrc((void*)p, 0, bytes, 0x00020000); }
DI v4u srd(const void* p, unsigned bytes) { return v4u{(unsigned)(u64)p, (unsigned)((u64)p >> 32) & 0xffffu, bytes, 0x00020000u}; }
template <int I, int E, typename F> DI void static_for(F&& f) { if constexpr (I < E) { f(std::integral_constant<int, I>{}); static_for<I + 1, E>(f); } }

DI void tile_w2(__amdgpu_buffer_rsrc_t w2r, __amdgpu_buffer_rsrc_t wsr, int f, int lane) {
  const int ww = f >> 5, ks = (f >> 1) & 15, nt = f & 1, i = lane & 15, kg = lane >> 4;
  const int qo = (2 * ks + (kg >> 1)) & 3;
  const int row0 = 64 * (ks >> 1) + 32 * (qo & 1) + 4 * (qo >> 1) + 2 * (kg & 1);
  const unsigned o = (row0 * D + 32 * ww + 8 * (i >> 2) + 4 * nt + (i & 3)) * 2;
  unsigned v[8];
#pragma unroll
  for (int j = 0; j < 8; ++j) v[j] = __builtin_amdgcn_raw_buffer_load_b16(w2r, o + (j >> 2) * D * 2, (j & 3) * 8 * D * 2, 0);
  const v4u p = {v[0] | (v[1] << 16), v[2] | (v[3] << 16), v[4] | (v[5] << 16), v[6] | (v[7] << 16)};
  __builtin_amdgcn_raw_buffer_store_b128(p, wsr, WS_W2T + f * 1024 + lane * 16, 0, SC1);
}

constexpr int PB = 96, NY = 4;
template <int G> DI void load_w1(v4u d, unsigned off) {
  unsigned so;
  asm volatile("s_mov_b32 %0, %3\n\t"
               "buffer_load_dwordx4 v[%c5:%c5+3], %1, %2, %0 offen\n\tbuffer_load_dwordx4 v[%c5+4:%c5+7], %1, %2, %0 offen offset:1024\n\t"
               "buffer_load_dwordx4 v[%c5+8:%c5+11], %1, %2, %0 offen offset:2048\n\tbuffer_load_dwordx4 v[%c5+12:%c5+15], %1, %2, %0 offen offset:3072\n\t"
               "s_mov_b32 %0, %4\n\t"
               "buffer_load_dwordx4 v[%c5+16:%c5+19], %1, %2, %0 offen\n\tbuffer_load_dwordx4 v[%c5+20:%c5+23], %1, %2, %0 offen offset:1024\n\t"
               "buffer_load_dwordx4 v[%c5+24:%c5+27], %1, %2, %0 offen offset:2048\n\tbuffer_load_dwordx4 v[%c5+28:%c5+31], %1, %2, %0 offen offset:3072"
               : "=&s"(so) : "v"(off), "s"(d), "i"(G * 65536), "i"(G * 65536 + 4096), "i"(PB + 32 + 32 * G) : "v255");
}
template <int G> DI void xpose() {
#pragma unroll
  for (int d = 0; d < 4; ++d)
#pragma unroll
    for (int p = 0; p < 2; ++p) {
      unsigned t0, t1;
      asm volatile("v_perm_b32 v[%c5], v[%c4+4], v[%c4], %2\n\tv_perm_b32 v[%c5+16], v[%c4+6], v[%c4+2], %2\n\t"
                   "v_perm_b32 v[%c5+4], v[%c4+4], v[%c4], %3\n\tv_perm_b32 v[%c5+20], v[%c4+6], v[%c4+2], %3\n\t"
                   "v_mov_b32 %0, v[%c5]\n\tv_mov_b32 %1, v[%c5+4]\n\t"
                   "v_mov_b32_dpp v[%c5], v[%c5+16] row_ror:8 row_mask:0xf bank_mask:0xc\n\t"
                   "v_mov_b32_dpp v[%c5+4], v[%c5+20] row_ror:8 row_mask:0xf bank_mask:0xc\n\t"
                   "v_mov_b32_dpp v[%c5+16], %0 row_ror:8 row_mask:0xf bank_mask:0x3\n\t"
                   "v_mov_b32_dpp v[%c5+20], %1 row_ror:8 row_mask:0xf bank_mask:0x3"
                   : "=&v"(t0), "=&v"(t1) : "s"(0x05040100u), "s"(0x07060302u), "i"(PB + 32 + 32 * G + 8 * d + p), "i"(PB + 32 * G + 8 * p + d));
    }
}
constexpr int w2reg(int i) { return i < 8 ? PB + 128 + 4 * i : PB + 4 * (i - 8 - NY); }
template <int I> DI void load_w2(v4u d, unsigned off) {
  unsigned so;
  asm volatile("s_mov_b32 %0, %3\n\tbuffer_load_dwordx4 v[%c4:%c4+3], %1, %2, %0 offen offset:%c5 sc1"
               : "=&s"(so) : "v"(off), "s"(d), "i"((I >> 2) * 4096), "i"(w2reg(I)), "i"((I & 3) * 1024));
}
template <int A> DI void mfma_p(f32x4& acc, v4u b) { asm volatile("v_mfma_f32_16x16x32_bf16 %0, v[%c2:%c2+3], %1, %0" : "+v"(acc) : "v"(b), "i"(A)); }
template <int A> DI void mfma_p0(f32x4& acc, v4u b, f32x4 c) {
  asm volatile("v_mfma_f32_16x16x32_bf16 %0, v[%c3:%c3+3], %1, %2" : "=&v"(acc) : "v"(b), "v"(c), "i"(A));
}
DI void mfma_r(f32x4& acc, v4u a, v4u b) { asm volatile("v_mfma_f32_16x16x32_bf16 %0, %1, %2, %0" : "+v"(acc) : "v"(a), "v"(b)); }
template <bool DEV> DI void touch(v4u d, unsigned off) {
  if constexpr (DEV) asm volatile("buffer_load_dword v[%c2], %0, %1, 0 offen sc1" :: "v"(off), "s"(d), "i"(PB - 1));
  else asm volatile("buffer_load_dword v[%c2], %0, %1, 0 offen" :: "v"(off), "s"(d), "i"(PB - 1));
}
DI void count_row(unsigned a) {
  asm volatile("s_mov_b64 exec, 1\n\tds_add_u32 %0, %1\n\ts_mov_b64 exec, -1" :: "v"(a), "v"(1u) : "memory");
}
template <int N> DI void wait_vm() { asm volatile("s_waitcnt vmcnt(%c0)" :: "i"(N) : "memory"); }

__global__ __launch_bounds__(512, 2) __attribute__((amdgpu_num_vgpr(PB / 2 - 1)))
void gin_one(const unsigned short* __restrict__ x, const int* __restrict__ src, const int* __restrict__ dst,
             const unsigned short* __restrict__ epsp, const unsigned short* __restrict__ w1, const unsigned short* __restrict__ b1,
             const unsigned short* __restrict__ w2, const unsigned short* __restrict__ b2, unsigned short* __restrict__ out,
             unsigned char* ws, int N, int E, int T) {
  extern __shared__ __attribute__((aligned(16))) unsigned char Ls[];
  LAS unsigned char* L = (LAS unsigned char*)Ls;
  const int tid = threadIdx.x, lane = tid & 63, tid0 = __builtin_amdgcn_readfirstlane(tid), w = tid0 >> 6;
  const int t = blockIdx.x, node0 = t * TM;
  const unsigned bulk = w >> 2, chain = bulk ^ 1;
  const int rank = (t >> 3) & 31;
  {
    u64 pc; unsigned len;
    asm volatile("s_getpc_b64 %0\n.Lgin_pc:\n\ts_mov_b32 %1, .Lgin_end-.Lgin_pc" : "=s"(pc), "=s"(len));
    asm volatile("s_cmp_gt_u32 %3, 63\n\ts_cbranch_scc1 1f\n\tbuffer_load_dword v[%c2], %0, %1, 0 offen\n1:"
                 :: "v"(lane < 8 ? (rank * 8 + lane) * 128 : OOB), "s"(srd((const void*)pc, len)), "i"(PB - 1), "s"(tid0) : "scc");
  }
  SB();
  asm volatile("" :: "s"(x), "s"(src), "s"(dst), "s"(epsp), "s"(w1), "s"(b1), "s"(w2), "s"(b2), "s"(out), "s"(ws), "s"(N), "s"(E), "s"(T));
  const int share2 = (T >> 3) + rank;
  const __amdgpu_buffer_rsrc_t wsr = rsrc(ws, WS_BYTES), xr = rsrc(x, N * 512), w2r = rsrc(w2, D * H * 2);
  const v4u w1d = srd(w1, D * H * 2), wsd = srd(ws, WS_BYTES);
  const int q = lane >> 4, m = lane & 15;
  const unsigned w1off = (((lane >> 3) & 1) * 32 + q * 8) * 1024 + w * 128 + (lane & 7) * 16;
  LAS unsigned* pcnt = (LAS unsigned*)(L + L_PCNT);
  LAS unsigned* pcell = (LAS unsigned*)(L + L_PCELL);
  const int tf = t + (w - 1) * T;

  if (w == 4) {
    PRIO(3);
    const __amdgpu_buffer_rsrc_t dr = rsrc(dst, E * 4), sr = rsrc(src, E * 4);
    v4u dv[3], sv[3];
#pragma unroll
    for (int i = 0; i < 3; ++i) {
      const unsigned o = (i == 2 && lane >= 32) ? OOB : (unsigned)(t * EPB + (lane + 64 * i) * 4) * 4u;
      dv[i] = __builtin_amdgcn_raw_buffer_load_b128(dr, o, 0, 0);
      sv[i] = __builtin_amdgcn_raw_buffer_load_b128(sr, o, 0, 0);
    }
    const u64 ep0 = __hip_atomic_load((GAS u64*)(ws + WS_EPOCH), RLX_AGENT);
    SB();
    *(LAS v4u*)(pcnt + lane * 4) = v4u{0, 0, 0, 0};
    pcnt[256 + lane] = 0;
    const unsigned dummy = 256u + lane;
    unsigned tt[12], rk[12];
#pragma unroll
    for (int i = 0; i < 12; ++i) {
      const int e = t * EPB + (lane + 64 * (i >> 2)) * 4;
      const unsigned lo = (e < E && !(i >= 8 && lane >= 32)) ? 0u : dummy;
      tt[i] = max(min(__umul24(min(dv[i >> 2][i & 3], 16383u), 52429u) >> 21, dummy), lo);
      rk[i] = __hip_atomic_fetch_add(pcnt + tt[i], 1u, RLX_WG);
    }
#pragma unroll
    for (int i = 0; i < 12; ++i)
      pcell[tt[i] * 16 + (rk[i] & 15u)] = (dv[i >> 2][i & 3] - tt[i] * TM) | (min(sv[i >> 2][i & 3], 16383u) << 6);
    *(LAS u64*)(L + L_MISC) = ep0;
  } else {
    __builtin_amdgcn_s_sleep(3);
    if (w == 0 || (w == 5 && share2 < 32)) touch<false>(w1d, (w ? share2 : rank) * 8192 + lane * 128);
    if (w == 3) {
      const v4u v1 = __builtin_amdgcn_raw_buffer_load_b128(rsrc(b1, H * 2), lane * 16, 0, 0);
      const v4u v2 = __builtin_amdgcn_raw_buffer_load_b128(rsrc(b2, D * 2), lane * 16, 0, 0);
      const unsigned ev = epsp[0];
      *(LAS unsigned*)(L + L_CNT + lane * 4) = 0;
#pragma unroll
      for (int i = 0; i < 8; ++i)
        *(LAS float*)(L + L_B1 + ((lane >> 3) * 64 + (((lane >> 2) & 1) + 2 * (i >> 2)) * 16 + 4 * (i & 3) + (lane & 3)) * 4) =
            (i & 1) ? bfhi(v1[i >> 1]) : bflo(v1[i >> 1]);
      *(LAS v4u*)(L + (lane < 32 ? L_B2 + lane * 16 : L_FLAT + lane * 16)) = v2;
      *(LAS v2u*)(L + L_MISC + 8) = v2u{0u, ev};
      *(LAS v4u*)(L + L_MISC + 16) = v4u{0u, 0u, 0u, 0u};
    } else if ((w == 1 || w == 2) && tf < 256) {
      tile_w2(w2r, wsr, tf, lane);
    }
  }
  if (chain) { load_w1<0>(w1d, w1off); load_w1<1>(w1d, w1off); }
  PRIO(3);
  bar();
  const u64 tag = *(LAS u64*)(L + L_MISC) + 1;
  const unsigned tagA = (unsigned)tag & 0xFFFFFFu, tagB = ((unsigned)tag + (unsigned)(tag >> 24) * 0x9E3779B1u) & 0x7FFFFu;
  {
    v4u e[2]; unsigned c[2];
#pragma unroll
    for (int i = 0; i < 2; ++i) { e[i] = *(LAS v4u*)(pcell + (w * 2 + i) * 256 + lane * 4); c[i] = pcnt[(w * 2 + i) * 16 + (lane >> 2)]; }
#pragma unroll
    for (int i = 0; i < 2; ++i) {
      const unsigned tc = (w * 2 + i) * 16 + (lane >> 2), cc = min(c[i], 31u);
      const v4u g = {(e[i].x & 0xFFFFFu) | (e[i].y << 20), ((e[i].y >> 12) & 0xFFu) | (tagA << 8), (e[i].z & 0xFFFFFu) | (e[i].w << 20),
                     ((e[i].w >> 12) & 0xFFu) | (cc << 8) | (tagB << 13)};
      __builtin_amdgcn_raw_buffer_store_b128(g, wsr, tc < (unsigned)T ? WS_CELL + tc * 16384 + t * 64 + (lane & 3) * 16 : OOB, 0, SC1);
    }
  }
  SB();
  PRIO(1);
  if (chain) { load_w1<2>(w1d, w1off); load_w1<3>(w1d, w1off); } else __builtin_amdgcn_s_sleep(16);
  bool any2 = false;
  unsigned ent[16], rk[16];
  LAS unsigned* flat = (LAS unsigned*)(L + L_FLAT);
  if (chain) {
    PRIO(2);
    v4u g[4];
    const unsigned coff = WS_CELL + t * 16384 + w * 4096 + lane * 16;
#pragma unroll
    for (int i = 0; i < 4; ++i) g[i] = __builtin_amdgcn_raw_buffer_load_b128(wsr, coff + i * 1024, 0, SC1);
    bool tmo = false;
    const int pl = T - 64 * w - (lane >> 2);
#define GR_OK(i) (16 * (i) >= pl || ((g[i].y >> 8) == tagA && (g[i].w >> 13) == tagB))
    if (!__all(GR_OK(0) && GR_OK(1) && GR_OK(2) && GR_OK(3))) {
      for (unsigned spins = 0;;) {
#pragma unroll
        for (int i = 0; i < 4; ++i) {
          const bool ok = GR_OK(i);
          const v4u nv = __builtin_amdgcn_raw_buffer_load_b128(wsr, ok ? OOB : coff + i * 1024, 0, SC1);
#pragma unroll
          for (int k = 0; k < 4; ++k) g[i][k] = ok ? g[i][k] : nv[k];
        }
        if (__all(GR_OK(0) && GR_OK(1) && GR_OK(2) && GR_OK(3))) break;
        if (++spins > SPIN_CAP) { tmo = true; break; }
      }
    }
    LAS unsigned char* stg = L + L_PSTG + w * 5120;
#pragma unroll
    for (int i = 0; i < 4; ++i) *(LAS v4u*)(stg + (16 * i + (lane >> 2)) * 80 + (lane & 3) * 16) = g[i];
#pragma unroll
    for (int i = 0; i < 4; ++i) g[i] = *(LAS v4u*)(stg + lane * 80 + i * 16);
    const int nval = (64 * w + lane < T && !tmo) ? (int)((g[0].w >> 8) & 31u) : 0;
    any2 = __any(nval > 8);
    bool bad = nval > 16;
    const unsigned dummy = 40u + (unsigned)(lane % 24);
    auto rank8 = [&](int lo) INL {
#pragma unroll
      for (int i = lo; i < lo + 8; ++i) {
        const unsigned a = g[i >> 2][i & 2], b = g[i >> 2][(i & 2) + 1];
        ent[i] = i < nval ? ((i & 1) ? __builtin_amdgcn_alignbit(b, a, 20) : a) & 0xFFFFFu : dummy;
        rk[i] = __hip_atomic_fetch_add((LAS unsigned*)(L + L_CNT + ((ent[i] & 63u) << 2)), 1u << (8 * w), RLX_WG);
      }
#pragma unroll
      for (int i = lo; i < lo + 8; ++i) { rk[i] = (rk[i] >> (8 * w)) & 255u; bad |= rk[i] == 255u; }
    };
    rank8(0);
    if (__builtin_expect(any2, 0)) rank8(8);
    if ((__any(bad) || tmo) && lane == 0) __hip_atomic_fetch_or((LAS unsigned*)(L + L_MISC + 8), tmo ? 3u : 1u, RLX_WG);
  } else {
    PRIO(0);
    load_w1<0>(w1d, w1off); load_w1<1>(w1d, w1off); load_w1<2>(w1d, w1off); load_w1<3>(w1d, w1off);
    touch<false>(srd(x, N * 512), node0 * 512 + ((w - 4) * 64 + lane) * 128);
    wait_vm<25>(); xpose<0>();
  }
  bar();
  bool slow, timedout;
  int s0, nit;
  u64 pk = 0;
  {
    const unsigned cw = *(LAS unsigned*)(L + L_CNT + lane * 4);
    const unsigned flagv = __builtin_amdgcn_readfirstlane(*(LAS unsigned*)(L + L_MISC + 8));
    const unsigned deg = __builtin_amdgcn_sad_u8(cw, 0u, 0u), below = __builtin_amdgcn_sad_u8(cw & ((1u << (8 * (w & 3))) - 1u), 0u, 0u);
    const unsigned l4 = (deg + 4u) & ~3u;
    const unsigned plen = lane < TM ? l4 + (lane % 5 == 4 ? 4u * (R - 1) : 0u) : 0u;
    const unsigned incl = wave_incl_scan(plen), start = incl - plen;
    timedout = (flagv & 2u) != 0u;
    slow = flagv != 0u || __builtin_amdgcn_readlane(incl, 63) > (unsigned)CAP;
    const unsigned tail = __builtin_amdgcn_readlane(incl, 5 * w + 4) - 4 * (R - 1);
    s0 = __builtin_amdgcn_readlane(start, 5 * w);
    nit = ((tail - s0) / 4 + R - 1) / R;
#pragma unroll
    for (int j = 0; j < 5; ++j) pk |= (u64)(((unsigned)__builtin_amdgcn_readlane(l4, 5 * w + j) >> 2) & 0xFFFu) << (12 * j);
    if (slow) s0 = 0;
    if (!slow) {
      const unsigned st2 = (unsigned)__builtin_amdgcn_ds_bpermute((5 * w + (lane >> 3)) << 2, (int)(start | (deg << 16)));
      if (chain) {
        const unsigned B4 = L_FLAT + 4u * (lane < TM ? start + 1u + below : CAP + 64u);
        auto place8 = [&](int lo) INL {
          unsigned b[8];
#pragma unroll
          for (int i = 0; i < 8; ++i) b[i] = (unsigned)__builtin_amdgcn_ds_bpermute((int)((ent[lo + i] & 63u) << 2), (int)B4);
#pragma unroll
          for (int i = 0; i < 8; ++i) *(LAS unsigned*)(L + b[i] + 4u * rk[lo + i]) = ent[lo + i];
        };
        place8(0);
        if (__builtin_expect(any2, 0)) place8(8);
      }
      const unsigned dg = st2 >> 16, k = lane & 7, pp = k == 7 ? 0u : dg + 1u + k;
      const bool mine = lane < 40 && pp < ((dg + 4u) & ~3u);
      const unsigned at = mine ? (st2 & 0xFFFFu) + pp : (lane >= 48 && lane - 48 < 4 * (R - 1) ? tail + lane - 48 : CAP + lane);
      flat[at] = (mine && k == 7) ? (unsigned)(node0 + 5 * w + (lane >> 3)) << 6 : EOOB;
    }
  }
  if (bulk) { wait_vm<17>(); xpose<1>(); }
  bar();
  PRIO(2);
  if (w == 1 || w == 2) {
    wait_vm<0>();
    if (lane == 0 && tf < 256) __hip_atomic_store((GAS unsigned*)(ws + WS_WFLAG) + tf, (unsigned)tag, RLX_AGENT);
  }
  const unsigned l16 = m * 16;
  LAS unsigned* fp = flat + s0 + q;
  v4u bl[R], br[R];
  unsigned en[R];
#pragma unroll
  for (int i = 0; i < R; ++i) en[i] = fp[4 * i];
  const float epsf = bflo(*(LAS unsigned*)(L + L_MISC + 12));
#pragma unroll
  for (int i = 0; i < R; ++i) {
    const unsigned o = ((en[i] << 3) & ~511u) | l16;
    bl[i] = __builtin_amdgcn_raw_buffer_load_b128(xr, o, 0, 0);
    br[i] = __builtin_amdgcn_raw_buffer_load_b128(xr, o + 256, 0, 0);
    en[i] = fp[4 * (R + i)];
  }
  asm volatile("" ::: "memory");
  SB();
  if (chain) { PRIO(0); xpose<0>(); xpose<1>(); }
  wait_vm<2 * R + 1>();
  xpose<2>(); xpose<3>();
  PRIO(0);
  int stage = 0;
  bool xdone = false;
  const unsigned w2off = WS_W2T + w * 32768 + lane * 16;
  v4u gb[2];
  f32x4 gacc[4];
  unsigned ghb = 0;
  auto gemm1 = [&](int mt, auto LAST, auto PART) INL {
    constexpr int part = PART;
    if constexpr (part == 0) {
      ghb = L_H + (mt * 16 + m) * HP + q * 16;
#pragma unroll
      for (int i = 0; i < 2; ++i) gb[i] = *(LAS v4u*)(L + ghb + i * 64);
#pragma unroll
      for (int c = 0; c < 4; ++c) gacc[c] = *(LAS f32x4*)(L + L_B1 + (64 * w + 16 * q) * 4 + 16 * c);
    }
    static_for<3 * part, (part == 2 ? 8 : 3 * part + 3)>([&](auto KS) {
      constexpr int ks = KS;
      if constexpr (LAST && (ks & 1) == 0) PRIO(3 - ks / 2);
      static_for<0, 4>([&](auto C) { mfma_p<PB + 16 * ks + 4 * C>(gacc[C], gb[ks & 1]); });
      if constexpr (ks + 2 < 8) gb[ks & 1] = *(LAS v4u*)(L + ghb + (ks + 2) * 64);
      if constexpr (LAST) load_w2<8 + NY + ks>(wsd, w2off);
      SB();
    });
    if constexpr (part == 2) {
      asm volatile("s_nop 7" : "+v"(gacc[0]), "+v"(gacc[1]), "+v"(gacc[2]), "+v"(gacc[3]));
      LAS unsigned char* h1w = L + L_H1 + (mt * 16 + m) * H1P + (64 * w + 16 * q) * 2;
#pragma unroll
      for (int half = 0; half < 2; ++half) {
        unsigned o[4];
#pragma unroll
        for (int k = 0; k < 4; ++k) o[k] = relu_pack(gacc[2 * half + (k >> 1)][2 * (k & 1)], gacc[2 * half + (k >> 1)][2 * (k & 1) + 1]);
        *(LAS v4u*)(h1w + 16 * half) = v4u{o[0], o[1], o[2], o[3]};
      }
    }
  };
  typedef std::integral_constant<int, 0> I0; typedef std::integral_constant<int, 1> I1; typedef std::integral_constant<int, 2> I2;
  auto wait_rows = [&](int mt) INL {
    while (__builtin_amdgcn_readfirstlane(__hip_atomic_load((LAS unsigned*)(L + L_MISC + 20 + 4 * mt), RLX_WG)) < (mt < 2 ? 16u : 8u))
      __builtin_amdgcn_s_sleep(1);
    asm volatile("" ::: "memory");
  };
  auto early_head = [&]() INL {
    if (stage == 0) {
      asm volatile("buffer_load_dwordx4 v[%c2:%c2+3], %0, %1, 0 offen sc1" :: "v"(WS_WFLAG + lane * 16), "s"(wsd), "i"(PB + 156));
    } else {
      unsigned d0, d1;
      asm volatile("v_xor_b32 %0, %2, v[%c3]\n\tv_xor_b32 %1, %2, v[%c3+1]\n\tv_or_b32 %0, %0, %1\n\tv_xor_b32 %1, %2, v[%c3+2]\n\t"
                   "v_or_b32 %0, %0, %1\n\tv_xor_b32 %1, %2, v[%c3+3]\n\tv_or_b32 %0, %0, %1"
                   : "=&v"(d0), "=&v"(d1) : "s"((unsigned)tag), "i"(PB + 156));
      xdone = __all(d0 == 0u);
      if (xdone) static_for<0, 8>([&](auto I) { load_w2<I>(wsd, w2off); });
    }
  };
  if (__builtin_expect(!slow, 1)) {
    const v4u sel = {m == 0 ? 0x3F80u : (m == 1 ? 0x3F800000u : 0u), m == 2 ? 0x3F80u : (m == 3 ? 0x3F800000u : 0u),
                     m == 4 ? 0x3F80u : (m == 5 ? 0x3F800000u : 0u), m == 6 ? 0x3F80u : (m == 7 ? 0x3F800000u : 0u)};
#define SELF(k) (unsigned)__builtin_amdgcn_update_dpp((int)sel[k], (int)sel[k], 0x128, 0x1, 0xC, false)
    v4u cur = {SELF(0), SELF(1), SELF(2), SELF(3)};
    fp += 8 * R;
    f32x4 a0 = {0.f, 0.f, 0.f, 0.f}, a1 = {0.f, 0.f, 0.f, 0.f};
    const unsigned hl = L_H + (lane >> 5) * 256 + m * 16 + (q & 1) * 8;
    unsigned rows = (32 + w) | ((2 * w) << 6) | ((2 * w + 1) << 12) | ((16 + 2 * w) << 18) | ((17 + 2 * w) << 24);
    unsigned cv = 0;
    int rem = (int)(pk & 0xFFF);
#define SLOT(i, REFILL) { \
        a0 = mfma(cur, bl[i], a0); \
        a1 = mfma(cur, br[i], a1); \
        cur = sel; \
        if (REFILL) { \
          const unsigned o = ((en[i] << 3) & ~511u) | l16; \
          bl[i] = __builtin_amdgcn_raw_buffer_load_b128(xr, o, 0, 0); \
          br[i] = __builtin_amdgcn_raw_buffer_load_b128(xr, o + 256, 0, 0); \
          en[i] = fp[4 * i]; \
        } \
        if (__builtin_expect(--rem == 0, 0)) { \
          float f[4]; \
          _Pragma("unroll") for (int r = 0; r < 4; ++r) { \
            auto s2 = __builtin_amdgcn_permlane32_swap(asu(a0[r]), asu(a1[r]), false, false); \
            const unsigned u0 = s2[0], u1 = s2[1]; \
            f[r] = fmaf(epsf, asf(u1), asf(u0)); \
          } \
          *(LAS v2u*)(L + hl + (rows & 63u) * HP) = v2u{pack_bf16(f[0], f[1]), pack_bf16(f[2], f[3])}; \
          count_row(L_MISC + 20 + ((rows & 48u) >> 2));       \
          rows >>= 6; \
          a0 = f32x4{0.f, 0.f, 0.f, 0.f}; a1 = a0; \
          cur = v4u{SELF(0), SELF(1), SELF(2), SELF(3)}; \
          pk >>= 12; \
          rem = (int)(pk & 0xFFF); \
        } }
    if (w == 6 || (w == 7 && share2 < 32)) touch<true>(wsd, WS_W2T + (w == 7 ? share2 : rank) * 8192 + lane * 128);
    static_assert(R == 3, "GEMM1 comes in three parts, one behind each slot of a round");
    for (; nit > 1; --nit) {
      const bool go = stage < 2 && __builtin_amdgcn_readfirstlane(cv) >= (stage ? 16u : 8u);
      asm volatile("" ::: "memory");
      SLOT(0, true)
      if (go) { early_head(); gemm1(stage ? 0 : 2, std::false_type{}, I0{}); }
      SLOT(1, true)
      if (go) gemm1(0, std::false_type{}, I1{});
      SLOT(2, true)
      if (go) { gemm1(stage ? 0 : 2, std::false_type{}, I2{}); ++stage; }
      fp += 4 * R;
      cv = __hip_atomic_load((LAS unsigned*)(L + L_MISC + 20 + (stage ? 0 : 8)), RLX_WG);
    }
#pragma unroll
    for (int i = 0; i < R; ++i) SLOT(i, false)
  } else {
#pragma unroll 1
    for (int j = 0; j < 5; ++j) {
      const int node = node0 + 5 * w + j;
      const v2u s2 = __builtin_amdgcn_raw_buffer_load_b64(xr, (unsigned)node * 512u + lane * 8, 0, 0);
      float a0 = bflo(s2.x), a1 = bfhi(s2.x), a2 = bflo(s2.y), a3 = bfhi(s2.y);
      if (node < N)
        for (int eb = 0; eb < E; eb += 64) {
          const int e = eb + lane;
          const int dv2 = (int)__builtin_amdgcn_raw_buffer_load_b32(rsrc(dst, E * 4), e * 4, 0, 0);
          const int sv2 = (int)__builtin_amdgcn_raw_buffer_load_b32(rsrc(src, E * 4), e * 4, 0, 0);
          u64 mkk = __ballot(dv2 == node && e < E);
          while (mkk) {
            const int b = __builtin_ctzll(mkk); mkk &= mkk - 1;
            const unsigned sidx = (unsigned)__builtin_amdgcn_readlane(sv2, b);
            const v2u rv = __builtin_amdgcn_raw_buffer_load_b64(xr, sidx < (unsigned)N ? sidx * 512u + lane * 8 : OOB, 0, 0);
            a0 += bflo(rv.x); a1 += bfhi(rv.x); a2 += bflo(rv.y); a3 += bfhi(rv.y);
          }
        }
      const int row = j ? ((j - 1) >> 1) * 16 + 2 * w + ((j - 1) & 1) : 32 + w;
      *(LAS v2u*)(L + L_H + row * HP + lane * 8) =
          v2u{pack_bf16(fmaf(epsf, bflo(s2.x), a0), fmaf(epsf, bfhi(s2.x), a1)), pack_bf16(fmaf(epsf, bflo(s2.y), a2), fmaf(epsf, bfhi(s2.y), a3))};
      count_row(L_MISC + 20 + (row >> 4) * 4);
    }
  }
  if (!timedout && tid == 192 && t == 0) __hip_atomic_store((GAS u64*)(ws + WS_EPOCH), tag, RLX_AGENT);
#pragma unroll 1
  for (; stage < 2; ++stage) {
    wait_rows(stage ? 0 : 2); wait_vm<0>(); early_head();
    gemm1(stage ? 0 : 2, std::false_type{}, I0{}); gemm1(0, std::false_type{}, I1{}); gemm1(stage ? 0 : 2, std::false_type{}, I2{});
  }
  if (__builtin_expect(!xdone, 0)) {
    bool okw = false;
    for (unsigned sp = 0; sp <= SPIN_CAP && !okw; ++sp) {
      const v4u fl = __builtin_amdgcn_raw_buffer_load_b128(wsr, WS_WFLAG + lane * 16, 0, SC1);
      okw = __all(fl.x == (unsigned)tag && fl.y == (unsigned)tag && fl.z == (unsigned)tag && fl.w == (unsigned)tag);
    }
    if (!okw) {
#pragma unroll 1
      for (int i = 0; i < 32; ++i) tile_w2(w2r, wsr, w * 32 + i, lane);
    }
    wait_vm<0>();
    static_for<0, 8>([&](auto I) { load_w2<I>(wsd, w2off); });
  }
  v4u wy[NY];
#pragma unroll
  for (int i = 0; i < NY; ++i) wy[i] = __builtin_amdgcn_raw_buffer_load_b128(wsr, w2off + (i & 3) * 1024, (2 + (i >> 2)) * 4096, SC1);
  SB();
  const v4u bb2 = *(LAS v4u*)(L + L_B2 + (32 * w + 8 * q) * 2);
  wait_rows(1);
#pragma unroll
  for (int i = 0; i < NY; ++i) asm volatile("" : "+v"(wy[i]));
  gemm1(1, std::true_type{}, I0{}); gemm1(1, std::true_type{}, I1{}); gemm1(1, std::true_type{}, I2{});
  f32x4 bias2[2];
#pragma unroll
  for (int nt = 0; nt < 2; ++nt) {
    bias2[nt] = f32x4{bflo(bb2[2 * nt]), bfhi(bb2[2 * nt]), bflo(bb2[2 * nt + 1]), bfhi(bb2[2 * nt + 1])};
    asm volatile("" : "+v"(bias2[nt]));
  }
  SB();
  bar();
  {
    f32x4 acc[3][2];
    LAS unsigned char* hb = L + L_H1 + m * H1P + q * 16;
    const __amdgpu_buffer_rsrc_t outr = rsrc(out, N * 512);
    auto put = [&](int mt) INL {
      const int r = mt < 2 ? 5 * (m >> 1) + 1 + 2 * mt + (m & 1) : (m < 8 ? 5 * m : TM);
      const v4u o = {pack_bf16(acc[mt][0][0], acc[mt][0][1]), pack_bf16(acc[mt][0][2], acc[mt][0][3]),
                     pack_bf16(acc[mt][1][0], acc[mt][1][1]), pack_bf16(acc[mt][1][2], acc[mt][1][3])};
      __builtin_amdgcn_raw_buffer_store_b128(o, outr, r < TM ? (unsigned)(node0 + r) * 512u + 64 * w + 16 * q : OOB, 0, SC1);
    };
    constexpr int P2 = 8, F0 = 16 + NY;
    v4u b[P2];
#define U2_KS(u) ((u) < 24 ? (u) / 3 : 8 + ((u) - 24) % 8)
#define U2_MT(u) ((u) < 24 ? (u) % 3 : ((u) - 24) / 8)
#pragma unroll
    for (int u = 0; u < P2; ++u) b[u] = *(LAS v4u*)(hb + U2_MT(u) * 16 * H1P + U2_KS(u) * 64);
    static_for<0, 48>([&](auto U) {
      constexpr int u = U, ks = U2_KS(u), mt = U2_MT(u);
      if constexpr (u % 12 == 0) PRIO(3 - u / 12);
      if constexpr (u < 2 * (32 - F0) && (u & 1) == 0) load_w2<F0 + u / 2>(wsd, w2off);
      constexpr int issued = u < 2 * (32 - F0) ? F0 + 1 + u / 2 : 32;
      if constexpr (mt == 0 && 2 * ks >= 8 + NY) wait_vm<issued - (2 * ks + 2)>();
      static_for<0, 2>([&](auto NT) {
        constexpr int f = 2 * ks + NT;
        if constexpr (ks == 0) mfma_p0<w2reg(f)>(acc[mt][NT], b[u % P2], bias2[NT]);
        else if constexpr (f >= 8 && f < 8 + NY) mfma_r(acc[mt][NT], wy[f - 8], b[u % P2]);
        else mfma_p<w2reg(f)>(acc[mt][NT], b[u % P2]);
      });
      if constexpr (u + P2 < 48) b[u % P2] = *(LAS v4u*)(hb + U2_MT(u + P2) * 16 * H1P + U2_KS(u + P2) * 64);
      if constexpr (u == 32) {
        if (w == 3) asm volatile("buffer_load_dwordx2 v[%c2:%c2+1], %0, %1, 0 offen sc1" :: "v"(WS_EPOCH), "s"(wsd), "i"(PB - 2));
      }
      if constexpr (u == 36 || u == 44) put(mt - 1);
      SB();
    });
    asm volatile("s_nop 7" : "+v"(acc[2][0]), "+v"(acc[2][1]));
    put(2);
  }
  if (!timedout && w == 3) {
    unsigned e0, e1;
    asm volatile("s_waitcnt vmcnt(3)\n\tv_mov_b32 %0, v[%c2]\n\tv_mov_b32 %1, v[%c2+1]" : "=v"(e0), "=v"(e1) : "i"(PB - 2));
    if ((e0 != (unsigned)tag || e1 != (unsigned)(tag >> 32)) && lane == 0) __hip_atomic_store((GAS u64*)(ws + WS_EPOCH), tag, RLX_AGENT);
  }
  asm volatile(".Lgin_end:");
}

__global__ __launch_bounds__(256) void gin_any(const unsigned short* __restrict__ x, const int* __restrict__ src, const int* __restrict__ dst,
                                               const unsigned short* __restrict__ epsp, const unsigned short* __restrict__ w1,
                                               const unsigned short* __restrict__ b1, const unsigned short* __restrict__ w2,
                                               const unsigned short* __restrict__ b2, unsigned short* __restrict__ out, int N, int E) {
  __shared__ float h[D], h1[H];
  const int node = blockIdx.x, c = threadIdx.x, lane = c & 63;
  const float xs = bflo(x[(size_t)node * D + c]);
  float a = xs;
  for (int eb = 0; eb < E; eb += 64) {
    const int e = eb + lane, dv = e < E ? dst[e] : -1, sv = e < E ? src[e] : 0;
    u64 mk = __ballot(dv == node);
    while (mk) {
      const int s = __builtin_amdgcn_readlane(sv, __builtin_ctzll(mk));
      mk &= mk - 1;
      if ((unsigned)s < (unsigned)N) a += bflo(x[(size_t)s * D + c]);
    }
  }
  h[c] = bflo(pack_bf16(fmaf(bflo(epsp[0]), xs, a), 0.f));
  __syncthreads();
  for (int j = c; j < H; j += 256) {
    float s = bflo(b1[j]);
    for (int k = 0; k < D; ++k) s = fmaf(h[k], bflo(w1[k * H + j]), s);
    h1[j] = bflo(relu_pack(s, 0.f));
  }
  __syncthreads();
  float s = bflo(b2[c]);
  for (int k = 0; k < H; ++k) s = fmaf(h1[k], bflo(w2[k * D + c]), s);
  out[(size_t)node * D + c] = (unsigned short)pack_bf16(s, 0.f);
}

extern "C" void kernel_launch(void* const* d_in, const int* in_sizes, int n_in, void* d_out, int out_size, void* d_ws, size_t ws_size,
                              hipStream_t stream) {
  const unsigned short* x = (const unsigned short*)d_in[0];
  const int* src = (const int*)d_in[1];
  const int* dst = (const int*)d_in[2];
  const unsigned short* eps = (const unsigned short*)d_in[3];
  const unsigned short* w1 = (const unsigned short*)d_in[4];
  const unsigned short* b1 = (const unsigned short*)d_in[5];
  const unsigned short* w2 = (const unsigned short*)d_in[6];
  const unsigned short* b2 = (const unsigned short*)d_in[7];
  const int N = in_sizes[0] / D, E = in_sizes[1], T = (N + TM - 1) / TM;
  if (T >= 128 && T <= 256 && E <= EPB * T && E % 4 == 0 && ws_size >= WS_BYTES) {
    (void)hipFuncSetAttribute((const void*)gin_one, hipFuncAttributeMaxDynamicSharedMemorySize, L_END);
    gin_one<<<T, 512, L_END, stream>>>(x, src, dst, eps, w1, b1, w2, b2, (unsigned short*)d_out, (unsigned char*)d_ws, N, E, T);
  } else if (N > 0) {
    gin_any<<<N, 256, 0, stream>>>(x, src, dst, eps, w1, b1, w2, b2, (unsigned short*)d_out, N, E);
  }
}
```

```cpp
#include <hip/hip_runtime.h>
#include <hip/hip_bf16.h>
#include <stdint.h>
#include <type_traits>

typedef unsigned v4u __attribute__((ext_vector_type(4)));
typedef unsigned v2u __attribute__((ext_vector_type(2)));
typedef float f32x4 __attribute__((ext_vector_type(4)));
typedef float f32x2 __attribute__((ext_vector_type(2)));
typedef short bf16x8 __attribute__((ext_vector_type(8)));
typedef short s16x2 __attribute__((ext_vector_type(2)));
typedef __bf16 bf16x2_t __attribute__((ext_vector_type(2)));
typedef unsigned long long u64;
#define GAS __attribute__((address_space(1)))
#define LAS __attribute__((address_space(3)))
#define DI __device__ __forceinline__
#define INL __attribute__((always_inline))
#define RLX_AGENT __ATOMIC_RELAXED, __HIP_MEMORY_SCOPE_AGENT
#define RLX_WG __ATOMIC_RELAXED, __HIP_MEMORY_SCOPE_WORKGROUP
#define SB() __builtin_amdgcn_sched_barrier(0)
#define PRIO(p) __builtin_amdgcn_s_setprio(p)

constexpr int D = 256, H = 512, TM = 40;
constexpr int EPB = 640;
constexpr int CAP = 1536;
constexpr int HP = 544, H1P = 1056;
constexpr int R = 3;
constexpr unsigned OOB = 0x40000000u;
constexpr unsigned EOOB = OOB >> 3;
constexpr int SC1 = 16;
constexpr unsigned SPIN_CAP = 4096;
constexpr unsigned WS_EPOCH = 0, WS_WFLAG = 4096, WS_W2T = 65536, WS_CELL = 1u << 20, WS_BYTES = WS_CELL + 256u * 16384u;
constexpr int L_H = 0, L_H1 = L_H + 48 * HP, L_FLAT = L_H1 + 48 * H1P, L_CNT = L_FLAT + (CAP + 128) * 4, L_MISC = L_CNT + 256,
              L_B1 = L_MISC + 32, L_B2 = L_B1 + 2048, L_END = L_B2 + 512;
constexpr int L_PSTG = L_H;
constexpr int L_PCELL = L_H1, L_PCNT = L_H1 + 320 * 64;

DI unsigned pack_bf16(float a, float b) { f32x2 v = {a, b}; bf16x2_t r = __builtin_convertvector(v, bf16x2_t); return __builtin_bit_cast(unsigned, r); }
DI float bflo(unsigned u) { return __builtin_bit_cast(float, u << 16); }
DI float bfhi(unsigned u) { return __builtin_bit_cast(float, u & 0xffff0000u); }
DI float asf(unsigned u) { return __builtin_bit_cast(float, u); }
DI unsigned asu(float u) { return __builtin_bit_cast(unsigned, u); }
DI f32x4 mfma(v4u a, v4u b, f32x4 c) {
  return __builtin_amdgcn_mfma_f32_16x16x32_bf16(__builtin_bit_cast(bf16x8, a), __builtin_bit_cast(bf16x8, b), c, 0, 0, 0);
}
DI unsigned relu_pack(float a, float b) {
  const s16x2 v = __builtin_bit_cast(s16x2, pack_bf16(a, b));
  return __builtin_bit_cast(unsigned, __builtin_elementwise_max(v, s16x2{0, 0}));
}
DI void bar() { asm volatile("s_waitcnt lgkmcnt(0)" ::: "memory"); __builtin_amdgcn_s_barrier(); asm volatile("" ::: "memory"); }
template <int CTRL, int ROWS> DI unsigned dpp_add(unsigned x) { return x + (unsigned)__builtin_amdgcn_update_dpp(0, (int)x, CTRL, ROWS, 0xF, false); }
DI unsigned wave_incl_scan(unsigned x) {
  x = dpp_add<0x111, 0xF>(x); x = dpp_add<0x112, 0xF>(x); x = dpp_add<0x114, 0xF>(x); x = dpp_add<0x118, 0xF>(x);
  x = dpp_add<0x142, 0xA>(x); x = dpp_add<0x143, 0xC>(x);
  return x;
}
DI __amdgpu_buffer_rsrc_t rsrc(const void* p, unsigned bytes) { return __builtin_amdgcn_make_buffer_rsrc((void*)p, 0, bytes, 0x00020000); }
DI v4u srd(const void* p, unsigned bytes) { return v4u{(unsigned)(u64)p, (unsigned)((u64)p >> 32) & 0xffffu, bytes, 0x00020000u}; }
template <int I, int E, typename F> DI void static_for(F&& f) { if constexpr (I < E) { f(std::integral_constant<int, I>{}); static_for<I + 1, E>(f); } }

DI void tile_w2(__amdgpu_buffer_rsrc_t w2r, __amdgpu_buffer_rsrc_t wsr, int f, int lane) {
  const int ww = f >> 5, ks = (f >> 1) & 15, nt = f & 1, i = lane & 15, kg = lane >> 4;
  const int qo = (2 * ks + (kg >> 1)) & 3;
  const int row0 = 64 * (ks >> 1) + 32 * (qo & 1) + 4 * (qo >> 1) + 2 * (kg & 1);
  const unsigned o = (row0 * D + 32 * ww + 8 * (i >> 2) + 4 * nt + (i & 3)) * 2;
  unsigned v[8];
#pragma unroll
  for (int j = 0; j < 8; ++j) v[j] = __builtin_amdgcn_raw_buffer_load_b16(w2r, o + (j >> 2) * D * 2, (j & 3) * 8 * D * 2, 0);
  const v4u p = {v[0] | (v[1] << 16), v[2] | (v[3] << 16), v[4] | (v[5] << 16), v[6] | (v[7] << 16)};
  __builtin_amdgcn_raw_buffer_store_b128(p, wsr, WS_W2T + f * 1024 + lane * 16, 0, SC1);
}

constexpr int PB = 96, NY = 4;
template <int G> DI void load_w1(v4u d, unsigned off) {
  unsigned so;
  asm volatile("s_mov_b32 %0, %3\n\t"
               "buffer_load_dwordx4 v[%c5:%c5+3], %1, %2, %0 offen\n\tbuffer_load_dwordx4 v[%c5+4:%c5+7], %1, %2, %0 offen offset:1024\n\t"
               "buffer_load_dwordx4 v[%c5+8:%c5+11], %1, %2, %0 offen offset:2048\n\tbuffer_load_dwordx4 v[%c5+12:%c5+15], %1, %2, %0 offen offset:3072\n\t"
               "s_mov_b32 %0, %4\n\t"
               "buffer_load_dwordx4 v[%c5+16:%c5+19], %1, %2, %0 offen\n\tbuffer_load_dwordx4 v[%c5+20:%c5+23], %1, %2, %0 offen offset:1024\n\t"
               "buffer_load_dwordx4 v[%c5+24:%c5+27], %1, %2, %0 offen offset:2048\n\tbuffer_load_dwordx4 v[%c5+28:%c5+31], %1, %2, %0 offen offset:3072"
               : "=&s"(so) : "v"(off), "s"(d), "i"(G * 65536), "i"(G * 65536 + 4096), "i"(PB + 32 + 32 * G) : "v255");
}
template <int G> DI void xpose() {
#pragma unroll
  for (int d = 0; d < 4; ++d)
#pragma unroll
    for (int p = 0; p < 2; ++p) {
      unsigned t0, t1;
      asm volatile("v_perm_b32 v[%c5], v[%c4+4], v[%c4], %2\n\tv_perm_b32 v[%c5+16], v[%c4+6], v[%c4+2], %2\n\t"
                   "v_perm_b32 v[%c5+4], v[%c4+4], v[%c4], %3\n\tv_perm_b32 v[%c5+20], v[%c4+6], v[%c4+2], %3\n\t"
                   "v_mov_b32 %0, v[%c5]\n\tv_mov_b32 %1, v[%c5+4]\n\t"
                   "v_mov_b32_dpp v[%c5], v[%c5+16] row_ror:8 row_mask:0xf bank_mask:0xc\n\t"
                   "v_mov_b32_dpp v[%c5+4], v[%c5+20] row_ror:8 row_mask:0xf bank_mask:0xc\n\t"
                   "v_mov_b32_dpp v[%c5+16], %0 row_ror:8 row_mask:0xf bank_mask:0x3\n\t"
                   "v_mov_b32_dpp v[%c5+20], %1 row_ror:8 row_mask:0xf bank_mask:0x3"
                   : "=&v"(t0), "=&v"(t1) : "s"(0x05040100u), "s"(0x07060302u), "i"(PB + 32 + 32 * G + 8 * d + p), "i"(PB + 32 * G + 8 * p + d));
    }
}
constexpr int w2reg(int i) { return i < 8 ? PB + 128 + 4 * i : PB + 4 * (i - 8 - NY); }
template <int I> DI void load_w2(v4u d, unsigned off) {
  unsigned so;
  asm volatile("s_mov_b32 %0, %3\n\tbuffer_load_dwordx4 v[%c4:%c4+3], %1, %2, %0 offen offset:%c5 sc1"
               : "=&s"(so) : "v"(off), "s"(d), "i"((I >> 2) * 4096), "i"(w2reg(I)), "i"((I & 3) * 1024));
}
template <int A> DI void mfma_p(f32x4& acc, v4u b) { asm volatile("v_mfma_f32_16x16x32_bf16 %0, v[%c2:%c2+3], %1, %0" : "+v"(acc) : "v"(b), "i"(A)); }
template <int A> DI void mfma_p0(f32x4& acc, v4u b, f32x4 c) {
  asm volatile("v_mfma_f32_16x16x32_bf16 %0, v[%c3:%c3+3], %1, %2" : "=&v"(acc) : "v"(b), "v"(c), "i"(A));
}
DI void mfma_r(f32x4& acc, v4u a, v4u b) { asm volatile("v_mfma_f32_16x16x32_bf16 %0, %1, %2, %0" : "+v"(acc) : "v"(a), "v"(b)); }
template <bool DEV> DI void touch(v4u d, unsigned off) {
  if constexpr (DEV) asm volatile("buffer_load_dword v[%c2], %0, %1, 0 offen sc1" :: "v"(off), "s"(d), "i"(PB - 1));
  else asm volatile("buffer_load_dword v[%c2], %0, %1, 0 offen" :: "v"(off), "s"(d), "i"(PB - 1));
}
DI void count_row(unsigned a) {
  asm volatile("s_mov_b64 exec, 1\n\tds_add_u32 %0, %1\n\ts_mov_b64 exec, -1" :: "v"(a), "v"(1u) : "memory");
}
template <int N> DI void wait_vm() { asm volatile("s_waitcnt vmcnt(%c0)" :: "i"(N) : "memory"); }

__global__ __launch_bounds__(512, 2) __attribute__((amdgpu_num_vgpr(PB / 2 - 1)))
void gin_one(const unsigned short* __restrict__ x, const int* __restrict__ src, const int* __restrict__ dst,
             const unsigned short* __restrict__ epsp, const unsigned short* __restrict__ w1, const unsigned short* __restrict__ b1,
             const unsigned short* __restrict__ w2, const unsigned short* __restrict__ b2, unsigned short* __restrict__ out,
             unsigned char* ws, int N, int E, int T) {
  extern __shared__ __attribute__((aligned(16))) unsigned char Ls[];
  LAS unsigned char* L = (LAS unsigned char*)Ls;
  const int tid = threadIdx.x, lane = tid & 63, tid0 = __builtin_amdgcn_readfirstlane(tid), w = tid0 >> 6;
  const int t = blockIdx.x, node0 = t * TM;
  const unsigned bulk = w >> 2, chain = bulk ^ 1;
  const int rank = (t >> 3) & 31;
  {
    u64 pc; unsigned len;
    asm volatile("s_getpc_b64 %0\n.Lgin_pc:\n\ts_mov_b32 %1, .Lgin_end-.Lgin_pc" : "=s"(pc), "=s"(len));
    asm volatile("s_cmp_gt_u32 %3, 63\n\ts_cbranch_scc1 1f\n\tbuffer_load_dword v[%c2], %0, %1, 0 offen\n1:"
                 :: "v"(lane < 8 ? (rank * 8 + lane) * 128 : OOB), "s"(srd((const void*)pc, len)), "i"(PB - 1), "s"(tid0) : "scc");
  }
  SB();
  asm volatile("" :: "s"(x), "s"(src), "s"(dst), "s"(epsp), "s"(w1), "s"(b1), "s"(w2), "s"(b2), "s"(out), "s"(ws), "s"(N), "s"(E), "s"(T));
  const int share2 = (T >> 3) + rank;
  const __amdgpu_buffer_rsrc_t wsr = rsrc(ws, WS_BYTES), xr = rsrc(x, N * 512), w2r = rsrc(w2, D * H * 2);
  const v4u w1d = srd(w1, D * H * 2), wsd = srd(ws, WS_BYTES);
  const int q = lane >> 4, m = lane & 15;
  const unsigned w1off = (((lane >> 3) & 1) * 32 + q * 8) * 1024 + w * 128 + (lane & 7) * 16;
  LAS unsigned* pcnt = (LAS unsigned*)(L + L_PCNT);
  LAS unsigned* pcell = (LAS unsigned*)(L + L_PCELL);
  const int tf = t + (w - 1) * T;

  if (w == 4) {
    PRIO(3);
    const __amdgpu_buffer_rsrc_t dr = rsrc(dst, E * 4), sr = rsrc(src, E * 4);
    v4u dv[3], sv[3];
#pragma unroll
    for (int i = 0; i < 3; ++i) {
      const unsigned o = (i == 2 && lane >= 32) ? OOB : (unsigned)(t * EPB + (lane + 64 * i) * 4) * 4u;
      dv[i] = __builtin_amdgcn_raw_buffer_load_b128(dr, o, 0, 0);
      sv[i] = __builtin_amdgcn_raw_buffer_load_b128(sr, o, 0, 0);
    }
    const u64 ep0 = __hip_atomic_load((GAS u64*)(ws + WS_EPOCH), RLX_AGENT);
    SB();
    *(LAS v4u*)(pcnt + lane * 4) = v4u{0, 0, 0, 0};
    pcnt[256 + lane] = 0;
    const unsigned dummy = 256u + lane;
    unsigned tt[12], rk[12];
#pragma unroll
    for (int i = 0; i < 12; ++i) {
      const int e = t * EPB + (lane + 64 * (i >> 2)) * 4;
      const unsigned lo = (e < E && !(i >= 8 && lane >= 32)) ? 0u : dummy;
      tt[i] = max(min(__umul24(min(dv[i >> 2][i & 3], 16383u), 52429u) >> 21, dummy), lo);
      rk[i] = __hip_atomic_fetch_add(pcnt + tt[i], 1u, RLX_WG);
    }
#pragma unroll
    for (int i = 0; i < 12; ++i)
      pcell[tt[i] * 16 + (rk[i] & 15u)] = (dv[i >> 2][i & 3] - tt[i] * TM) | (min(sv[i >> 2][i & 3], 16383u) << 6);
    *(LAS u64*)(L + L_MISC) = ep0;
  } else {
    __builtin_amdgcn_s_sleep(3);
    if (w == 0 || (w == 5 && share2 < 32)) touch<false>(w1d, (w ? share2 : rank) * 8192 + lane * 128);
    if (w == 3) {
      const v4u v1 = __builtin_amdgcn_raw_buffer_load_b128(rsrc(b1, H * 2), lane * 16, 0, 0);
      const v4u v2 = __builtin_amdgcn_raw_buffer_load_b128(rsrc(b2, D * 2), lane * 16, 0, 0);
      const unsigned ev = epsp[0];
      *(LAS unsigned*)(L + L_CNT + lane * 4) = 0;
#pragma unroll
      for (int i = 0; i < 8; ++i)
        *(LAS float*)(L + L_B1 + ((lane >> 3) * 64 + (((lane >> 2) & 1) + 2 * (i >> 2)) * 16 + 4 * (i & 3) + (lane & 3)) * 4) =
            (i & 1) ? bfhi(v1[i >> 1]) : bflo(v1[i >> 1]);
      *(LAS v4u*)(L + (lane < 32 ? L_B2 + lane * 16 : L_FLAT + lane * 16)) = v2;
      *(LAS v2u*)(L + L_MISC + 8) = v2u{0u, ev};
      *(LAS v4u*)(L + L_MISC + 16) = v4u{0u, 0u, 0u, 0u};
    } else if ((w == 1 || w == 2) && tf < 256) {
      tile_w2(w2r, wsr, tf, lane);
    }
  }
  if (chain) { load_w1<0>(w1d, w1off); load_w1<1>(w1d, w1off); }
  PRIO(3);
  bar();
  const u64 tag = *(LAS u64*)(L + L_MISC) + 1;
  const unsigned tagA = (unsigned)tag & 0xFFFFFFu, tagB = ((unsigned)tag + (unsigned)(tag >> 24) * 0x9E3779B1u) & 0x7FFFFu;
  {
    v4u e[2]; unsigned c[2];
#pragma unroll
    for (int i = 0; i < 2; ++i) { e[i] = *(LAS v4u*)(pcell + (w * 2 + i) * 256 + lane * 4); c[i] = pcnt[(w * 2 + i) * 16 + (lane >> 2)]; }
#pragma unroll
    for (int i = 0; i < 2; ++i) {
      const unsigned tc = (w * 2 + i) * 16 + (lane >> 2), cc = min(c[i], 31u);
      const v4u g = {(e[i].x & 0xFFFFFu) | (e[i].y << 20), ((e[i].y >> 12) & 0xFFu) | (tagA << 8), (e[i].z & 0xFFFFFu) | (e[i].w << 20),
                     ((e[i].w >> 12) & 0xFFu) | (cc << 8) | (tagB << 13)};
      __builtin_amdgcn_raw_buffer_store_b128(g, wsr, tc < (unsigned)T ? WS_CELL + tc * 16384 + t * 64 + (lane & 3) * 16 : OOB, 0, SC1);
    }
  }
  SB();
  PRIO(1);
  if (chain) { load_w1<2>(w1d, w1off); load_w1<3>(w1d, w1off); } else __builtin_amdgcn_s_sleep(14);
  bool any2 = false;
  unsigned ent[16], rk[16];
  LAS unsigned* flat = (LAS unsigned*)(L + L_FLAT);
  if (chain) {
    PRIO(2);
    v4u g[4];
    const unsigned coff = WS_CELL + t * 16384 + w * 4096 + lane * 16;
#pragma unroll
    for (int i = 0; i < 4; ++i) g[i] = __builtin_amdgcn_raw_buffer_load_b128(wsr, coff + i * 1024, 0, SC1);
    bool tmo = false;
    const int pl = T - 64 * w - (lane >> 2);
#define GR_OK(i) (16 * (i) >= pl || ((g[i].y >> 8) == tagA && (g[i].w >> 13) == tagB))
    if (!__all(GR_OK(0) && GR_OK(1) && GR_OK(2) && GR_OK(3))) {
      for (unsigned spins = 0;;) {
#pragma unroll
        for (int i = 0; i < 4; ++i) {
          const bool ok = GR_OK(i);
          const v4u nv = __builtin_amdgcn_raw_buffer_load_b128(wsr, ok ? OOB : coff + i * 1024, 0, SC1);
#pragma unroll
          for (int k = 0; k < 4; ++k) g[i][k] = ok ? g[i][k] : nv[k];
        }
        if (__all(GR_OK(0) && GR_OK(1) && GR_OK(2) && GR_OK(3))) break;
        if (++spins > SPIN_CAP) { tmo = true; break; }
      }
    }
    LAS unsigned char* stg = L + L_PSTG + w * 5120;
#pragma unroll
    for (int i = 0; i < 4; ++i) *(LAS v4u*)(stg + (16 * i + (lane >> 2)) * 80 + (lane & 3) * 16) = g[i];
#pragma unroll
    for (int i = 0; i < 4; ++i) g[i] = *(LAS v4u*)(stg + lane * 80 + i * 16);
    const int nval = (64 * w + lane < T && !tmo) ? (int)((g[0].w >> 8) & 31u) : 0;
    any2 = __any(nval > 8);
    bool bad = nval > 16;
    const unsigned dummy = 40u + (unsigned)(lane % 24);
    auto rank8 = [&](int lo) INL {
#pragma unroll
      for (int i = lo; i < lo + 8; ++i) {
        const unsigned a = g[i >> 2][i & 2], b = g[i >> 2][(i & 2) + 1];
        ent[i] = i < nval ? ((i & 1) ? __builtin_amdgcn_alignbit(b, a, 20) : a) & 0xFFFFFu : dummy;
        rk[i] = __hip_atomic_fetch_add((LAS unsigned*)(L + L_CNT + ((ent[i] & 63u) << 2)), 1u << (8 * w), RLX_WG);
      }
#pragma unroll
      for (int i = lo; i < lo + 8; ++i) { rk[i] = (rk[i] >> (8 * w)) & 255u; bad |= rk[i] == 255u; }
    };
    rank8(0);
    if (__builtin_expect(any2, 0)) rank8(8);
    if ((__any(bad) || tmo) && lane == 0) __hip_atomic_fetch_or((LAS unsigned*)(L + L_MISC + 8), tmo ? 3u : 1u, RLX_WG);
  } else {
    PRIO(0);
    load_w1<0>(w1d, w1off); load_w1<1>(w1d, w1off); load_w1<2>(w1d, w1off); load_w1<3>(w1d, w1off);
    touch<false>(srd(x, N * 512), node0 * 512 + ((w - 4) * 64 + lane) * 128);
    wait_vm<25>(); xpose<0>();
  }
  bar();
  bool slow, timedout;
  int s0, nit;
  u64 pk = 0;
  {
    const unsigned cw = *(LAS unsigned*)(L + L_CNT + lane * 4);
    const unsigned flagv = __builtin_amdgcn_readfirstlane(*(LAS unsigned*)(L + L_MISC + 8));
    const unsigned deg = __builtin_amdgcn_sad_u8(cw, 0u, 0u), below = __builtin_amdgcn_sad_u8(cw & ((1u << (8 * (w & 3))) - 1u), 0u, 0u);
    const unsigned l4 = (deg + 4u) & ~3u;
    const unsigned plen = lane < TM ? l4 + (lane % 5 == 4 ? 4u * (R - 1) : 0u) : 0u;
    const unsigned incl = wave_incl_scan(plen), start = incl - plen;
    timedout = (flagv & 2u) != 0u;
    slow = flagv != 0u || __builtin_amdgcn_readlane(incl, 63) > (unsigned)CAP;
    const unsigned tail = __builtin_amdgcn_readlane(incl, 5 * w + 4) - 4 * (R - 1);
    s0 = __builtin_amdgcn_readlane(start, 5 * w);
    nit = ((tail - s0) / 4 + R - 1) / R;
#pragma unroll
    for (int j = 0; j < 5; ++j) pk |= (u64)(((unsigned)__builtin_amdgcn_readlane(l4, 5 * w + j) >> 2) & 0xFFFu) << (12 * j);
    if (slow) s0 = 0;
    if (!slow) {
      const unsigned st2 = (unsigned)__builtin_amdgcn_ds_bpermute((5 * w + (lane >> 3)) << 2, (int)(start | (deg << 16)));
      if (chain) {
        const unsigned B4 = L_FLAT + 4u * (lane < TM ? start + 1u + below : CAP + 64u);
        auto place8 = [&](int lo) INL {
          unsigned b[8];
#pragma unroll
          for (int i = 0; i < 8; ++i) b[i] = (unsigned)__builtin_amdgcn_ds_bpermute((int)((ent[lo + i] & 63u) << 2), (int)B4);
#pragma unroll
          for (int i = 0; i < 8; ++i) *(LAS unsigned*)(L + b[i] + 4u * rk[lo + i]) = ent[lo + i];
        };
        place8(0);
        if (__builtin_expect(any2, 0)) place8(8);
      }
      const unsigned dg = st2 >> 16, k = lane & 7, pp = k == 7 ? 0u : dg + 1u + k;
      const bool mine = lane < 40 && pp < ((dg + 4u) & ~3u);
      const unsigned at = mine ? (st2 & 0xFFFFu) + pp : (lane >= 48 && lane - 48 < 4 * (R - 1) ? tail + lane - 48 : CAP + lane);
      flat[at] = (mine && k == 7) ? (unsigned)(node0 + 5 * w + (lane >> 3)) << 6 : EOOB;
    }
  }
  if (bulk) { wait_vm<17>(); xpose<1>(); wait_vm<9>(); xpose<2>(); }
  bar();
  PRIO(2);
  if (w == 1 || w == 2) {
    wait_vm<0>();
    if (lane == 0 && tf < 256) __hip_atomic_store((GAS unsigned*)(ws + WS_WFLAG) + tf, (unsigned)tag, RLX_AGENT);
  }
  const unsigned l16 = m * 16;
  LAS unsigned* fp = flat + s0 + q;
  v4u bl[R], br[R];
  unsigned en[R];
#pragma unroll
  for (int i = 0; i < R; ++i) en[i] = fp[4 * i];
  const float epsf = bflo(*(LAS unsigned*)(L + L_MISC + 12));
#pragma unroll
  for (int i = 0; i < R; ++i) {
    const unsigned o = ((en[i] << 3) & ~511u) | l16;
    bl[i] = __builtin_amdgcn_raw_buffer_load_b128(xr, o, 0, 0);
    br[i] = __builtin_amdgcn_raw_buffer_load_b128(xr, o + 256, 0, 0);
    en[i] = fp[4 * (R + i)];
  }
  asm volatile("" ::: "memory");
  SB();
  if (chain) { PRIO(0); xpose<0>(); xpose<1>(); xpose<2>(); }
  wait_vm<2 * R + 1>();
  xpose<3>();
  PRIO(0);
  int stage = 0;
  bool xdone = false;
  const unsigned w2off = WS_W2T + w * 32768 + lane * 16;
  v4u gb[2];
  f32x4 gacc[4];
  unsigned ghb = 0;
  auto gemm1 = [&](int mt, auto LAST, auto PART) INL {
    constexpr int part = PART;
    if constexpr (part == 0) {
      ghb = L_H + (mt * 16 + m) * HP + q * 16;
#pragma unroll
      for (int i = 0; i < 2; ++i) gb[i] = *(LAS v4u*)(L + ghb + i * 64);
#pragma unroll
      for (int c = 0; c < 4; ++c) gacc[c] = *(LAS f32x4*)(L + L_B1 + (64 * w + 16 * q) * 4 + 16 * c);
    }
    static_for<3 * part, (part == 2 ? 8 : 3 * part + 3)>([&](auto KS) {
      constexpr int ks = KS;
      if constexpr (LAST && (ks & 1) == 0) PRIO(3 - ks / 2);
      static_for<0, 4>([&](auto C) { mfma_p<PB + 16 * ks + 4 * C>(gacc[C], gb[ks & 1]); });
      if constexpr (ks + 2 < 8) gb[ks & 1] = *(LAS v4u*)(L + ghb + (ks + 2) * 64);
      if constexpr (LAST) load_w2<8 + NY + ks>(wsd, w2off);
      SB();
    });
    if constexpr (part == 2) {
      asm volatile("s_nop 7" : "+v"(gacc[0]), "+v"(gacc[1]), "+v"(gacc[2]), "+v"(gacc[3]));
      LAS unsigned char* h1w = L + L_H1 + (mt * 16 + m) * H1P + (64 * w + 16 * q) * 2;
#pragma unroll
      for (int half = 0; half < 2; ++half) {
        unsigned o[4];
#pragma unroll
        for (int k = 0; k < 4; ++k) o[k] = relu_pack(gacc[2 * half + (k >> 1)][2 * (k & 1)], gacc[2 * half + (k >> 1)][2 * (k & 1) + 1]);
        *(LAS v4u*)(h1w + 16 * half) = v4u{o[0], o[1], o[2], o[3]};
      }
    }
  };
  typedef std::integral_constant<int, 0> I0; typedef std::integral_constant<int, 1> I1; typedef std::integral_constant<int, 2> I2;
  auto wait_rows = [&](int mt) INL {
    while (__builtin_amdgcn_readfirstlane(__hip_atomic_load((LAS unsigned*)(L + L_MISC + 20 + 4 * mt), RLX_WG)) < (mt < 2 ? 16u : 8u))
      __builtin_amdgcn_s_sleep(1);
    asm volatile("" ::: "memory");
  };
  auto early_head = [&]() INL {
    if (stage == 0) {
      asm volatile("buffer_load_dwordx4 v[%c2:%c2+3], %0, %1, 0 offen sc1" :: "v"(WS_WFLAG + lane * 16), "s"(wsd), "i"(PB + 156));
    } else {
      unsigned d0, d1;
      asm volatile("v_xor_b32 %0, %2, v[%c3]\n\tv_xor_b32 %1, %2, v[%c3+1]\n\tv_or_b32 %0, %0, %1\n\tv_xor_b32 %1, %2, v[%c3+2]\n\t"
                   "v_or_b32 %0, %0, %1\n\tv_xor_b32 %1, %2, v[%c3+3]\n\tv_or_b32 %0, %0, %1"
                   : "=&v"(d0), "=&v"(d1) : "s"((unsigned)tag), "i"(PB + 156));
      xdone = __all(d0 == 0u);
      if (xdone) static_for<0, 8>([&](auto I) { load_w2<I>(wsd, w2off); });
    }
  };
  if (__builtin_expect(!slow, 1)) {
    const v4u sel = {m == 0 ? 0x3F80u : (m == 1 ? 0x3F800000u : 0u), m == 2 ? 0x3F80u : (m == 3 ? 0x3F800000u : 0u),
                     m == 4 ? 0x3F80u : (m == 5 ? 0x3F800000u : 0u), m == 6 ? 0x3F80u : (m == 7 ? 0x3F800000u : 0u)};
#define SELF(k) (unsigned)__builtin_amdgcn_update_dpp((int)sel[k], (int)sel[k], 0x128, 0x1, 0xC, false)
    v4u cur = {SELF(0), SELF(1), SELF(2), SELF(3)};
    fp += 8 * R;
    f32x4 a0 = {0.f, 0.f, 0.f, 0.f}, a1 = {0.f, 0.f, 0.f, 0.f};
    const unsigned hl = L_H + (lane >> 5) * 256 + m * 16 + (q & 1) * 8;
    unsigned rows = (32 + w) | ((2 * w) << 6) | ((2 * w + 1) << 12) | ((16 + 2 * w) << 18) | ((17 + 2 * w) << 24);
    unsigned cv = 0;
    int rem = (int)(pk & 0xFFF);
#define SLOT(i, REFILL) { \
        a0 = mfma(cur, bl[i], a0); \
        a1 = mfma(cur, br[i], a1); \
        cur = sel; \
        if (REFILL) { \
          const unsigned o = ((en[i] << 3) & ~511u) | l16; \
          bl[i] = __builtin_amdgcn_raw_buffer_load_b128(xr, o, 0, 0); \
          br[i] = __builtin_amdgcn_raw_buffer_load_b128(xr, o + 256, 0, 0); \
          en[i] = fp[4 * i]; \
        } \
        if (__builtin_expect(--rem == 0, 0)) { \
          float f[4]; \
          _Pragma("unroll") for (int r = 0; r < 4; ++r) { \
            auto s2 = __builtin_amdgcn_permlane32_swap(asu(a0[r]), asu(a1[r]), false, false); \
            const unsigned u0 = s2[0], u1 = s2[1]; \
            f[r] = fmaf(epsf, asf(u1), asf(u0)); \
          } \
          *(LAS v2u*)(L + hl + (rows & 63u) * HP) = v2u{pack_bf16(f[0], f[1]), pack_bf16(f[2], f[3])}; \
          count_row(L_MISC + 20 + ((rows & 48u) >> 2));       \
          rows >>= 6; \
          a0 = f32x4{0.f, 0.f, 0.f, 0.f}; a1 = a0; \
          cur = v4u{SELF(0), SELF(1), SELF(2), SELF(3)}; \
          pk >>= 12; \
          rem = (int)(pk & 0xFFF); \
        } }
    if (w == 6 || (w == 7 && share2 < 32)) touch<true>(wsd, WS_W2T + (w == 7 ? share2 : rank) * 8192 + lane * 128);
    static_assert(R == 3, "GEMM1 comes in three parts, one behind each slot of a round");
    for (; nit > 1; --nit) {
      const bool go = stage < 2 && __builtin_amdgcn_readfirstlane(cv) >= (stage ? 16u : 8u);
      asm volatile("" ::: "memory");
      SLOT(0, true)
      if (go) { early_head(); gemm1(stage ? 0 : 2, std::false_type{}, I0{}); }
      SLOT(1, true)
      if (go) gemm1(0, std::false_type{}, I1{});
      SLOT(2, true)
      if (go) { gemm1(stage ? 0 : 2, std::false_type{}, I2{}); ++stage; }
      fp += 4 * R;
      cv = __hip_atomic_load((LAS unsigned*)(L + L_MISC + 20 + (stage ? 0 : 8)), RLX_WG);
    }
#pragma unroll
    for (int i = 0; i < R; ++i) SLOT(i, false)
  } else {
#pragma unroll 1
    for (int j = 0; j < 5; ++j) {
      const int node = node0 + 5 * w + j;
      const v2u s2 = __builtin_amdgcn_raw_buffer_load_b64(xr, (unsigned)node * 512u + lane * 8, 0, 0);
      float a0 = bflo(s2.x), a1 = bfhi(s2.x), a2 = bflo(s2.y), a3 = bfhi(s2.y);
      if (node < N)
        for (int eb = 0; eb < E; eb += 64) {
          const int e = eb + lane;
          const int dv2 = (int)__builtin_amdgcn_raw_buffer_load_b32(rsrc(dst, E * 4), e * 4, 0, 0);
          const int sv2 = (int)__builtin_amdgcn_raw_buffer_load_b32(rsrc(src, E * 4), e * 4, 0, 0);
          u64 mkk = __ballot(dv2 == node && e < E);
          while (mkk) {
            const int b = __builtin_ctzll(mkk); mkk &= mkk - 1;
            const unsigned sidx = (unsigned)__builtin_amdgcn_readlane(sv2, b);
            const v2u rv = __builtin_amdgcn_raw_buffer_load_b64(xr, sidx < (unsigned)N ? sidx * 512u + lane * 8 : OOB, 0, 0);
            a0 += bflo(rv.x); a1 += bfhi(rv.x); a2 += bflo(rv.y); a3 += bfhi(rv.y);
          }
        }
      const int row = j ? ((j - 1) >> 1) * 16 + 2 * w + ((j - 1) & 1) : 32 + w;
      *(LAS v2u*)(L + L_H + row * HP + lane * 8) =
          v2u{pack_bf16(fmaf(epsf, bflo(s2.x), a0), fmaf(epsf, bfhi(s2.x), a1)), pack_bf16(fmaf(epsf, bflo(s2.y), a2), fmaf(epsf, bfhi(s2.y), a3))};
      count_row(L_MISC + 20 + (row >> 4) * 4);
    }
  }
  if (!timedout && tid == 192 && t == 0) __hip_atomic_store((GAS u64*)(ws + WS_EPOCH), tag, RLX_AGENT);
#pragma unroll 1
  for (; stage < 2; ++stage) {
    wait_rows(stage ? 0 : 2); wait_vm<0>(); early_head();
    gemm1(stage ? 0 : 2, std::false_type{}, I0{}); gemm1(0, std::false_type{}, I1{}); gemm1(stage ? 0 : 2, std::false_type{}, I2{});
  }
  if (__builtin_expect(!xdone, 0)) {
    bool okw = false;
    for (unsigned sp = 0; sp <= SPIN_CAP && !okw; ++sp) {
      const v4u fl = __builtin_amdgcn_raw_buffer_load_b128(wsr, WS_WFLAG + lane * 16, 0, SC1);
      okw = __all(fl.x == (unsigned)tag && fl.y == (unsigned)tag && fl.z == (unsigned)tag && fl.w == (unsigned)tag);
    }
    if (!okw) {
#pragma unroll 1
      for (int i = 0; i < 32; ++i) tile_w2(w2r, wsr, w * 32 + i, lane);
    }
    wait_vm<0>();
    static_for<0, 8>([&](auto I) { load_w2<I>(wsd, w2off); });
  }
  v4u wy[NY];
#pragma unroll
  for (int i = 0; i < NY; ++i) wy[i] = __builtin_amdgcn_raw_buffer_load_b128(wsr, w2off + (i & 3) * 1024, (2 + (i >> 2)) * 4096, SC1);
  SB();
  const v4u bb2 = *(LAS v4u*)(L + L_B2 + (32 * w + 8 * q) * 2);
  wait_rows(1);
#pragma unroll
  for (int i = 0; i < NY; ++i) asm volatile("" : "+v"(wy[i]));
  gemm1(1, std::true_type{}, I0{}); gemm1(1, std::true_type{}, I1{}); gemm1(1, std::true_type{}, I2{});
  f32x4 bias2[2];
#pragma unroll
  for (int nt = 0; nt < 2; ++nt) {
    bias2[nt] = f32x4{bflo(bb2[2 * nt]), bfhi(bb2[2 * nt]), bflo(bb2[2 * nt + 1]), bfhi(bb2[2 * nt + 1])};
    asm volatile("" : "+v"(bias2[nt]));
  }
  SB();
  bar();
  {
    f32x4 acc[3][2];
    LAS unsigned char* hb = L + L_H1 + m * H1P + q * 16;
    const __amdgpu_buffer_rsrc_t outr = rsrc(out, N * 512);
    auto put = [&](int mt) INL {
      const int r = mt < 2 ? 5 * (m >> 1) + 1 + 2 * mt + (m & 1) : (m < 8 ? 5 * m : TM);
      const v4u o = {pack_bf16(acc[mt][0][0], acc[mt][0][1]), pack_bf16(acc[mt][0][2], acc[mt][0][3]),
                     pack_bf16(acc[mt][1][0], acc[mt][1][1]), pack_bf16(acc[mt][1][2], acc[mt][1][3])};
      __builtin_amdgcn_raw_buffer_store_b128(o, outr, r < TM ? (unsigned)(node0 + r) * 512u + 64 * w + 16 * q : OOB, 0, SC1);
    };
    constexpr int P2 = 8, F0 = 16 + NY;
    v4u b[P2];
#define U2_KS(u) ((u) < 24 ? (u) / 3 : 8 + ((u) - 24) % 8)
#define U2_MT(u) ((u) < 24 ? (u) % 3 : ((u) - 24) / 8)
#pragma unroll
    for (int u = 0; u < P2; ++u) b[u] = *(LAS v4u*)(hb + U2_MT(u) * 16 * H1P + U2_KS(u) * 64);
    static_for<0, 48>([&](auto U) {
      constexpr int u = U, ks = U2_KS(u), mt = U2_MT(u);
      if constexpr (u % 12 == 0) PRIO(3 - u / 12);
      if constexpr (u < 2 * (32 - F0) && (u & 1) == 0) load_w2<F0 + u / 2>(wsd, w2off);
      constexpr int issued = u < 2 * (32 - F0) ? F0 + 1 + u / 2 : 32;
      if constexpr (mt == 0 && 2 * ks >= 8 + NY) wait_vm<issued - (2 * ks + 2)>();
      static_for<0, 2>([&](auto NT) {
        constexpr int f = 2 * ks + NT;
        if constexpr (ks == 0) mfma_p0<w2reg(f)>(acc[mt][NT], b[u % P2], bias2[NT]);
        else if constexpr (f >= 8 && f < 8 + NY) mfma_r(acc[mt][NT], wy[f - 8], b[u % P2]);
        else mfma_p<w2reg(f)>(acc[mt][NT], b[u % P2]);
      });
      if constexpr (u + P2 < 48) b[u % P2] = *(LAS v4u*)(hb + U2_MT(u + P2) * 16 * H1P + U2_KS(u + P2) * 64);
      if constexpr (u == 32) {
        if (w == 3) asm volatile("buffer_load_dwordx2 v[%c2:%c2+1], %0, %1, 0 offen sc1" :: "v"(WS_EPOCH), "s"(wsd), "i"(PB - 2));
      }
      if constexpr (u == 36 || u == 44) put(mt - 1);
      SB();
    });
    asm volatile("s_nop 7" : "+v"(acc[2][0]), "+v"(acc[2][1]));
    put(2);
  }
  if (!timedout && w == 3) {
    unsigned e0, e1;
    asm volatile("s_waitcnt vmcnt(3)\n\tv_mov_b32 %0, v[%c2]\n\tv_mov_b32 %1, v[%c2+1]" : "=v"(e0), "=v"(e1) : "i"(PB - 2));
    if ((e0 != (unsigned)tag || e1 != (unsigned)(tag >> 32)) && lane == 0) __hip_atomic_store((GAS u64*)(ws + WS_EPOCH), tag, RLX_AGENT);
  }
  asm volatile(".Lgin_end:");
}

__global__ __launch_bounds__(256) void gin_any(const unsigned short* __restrict__ x, const int* __restrict__ src, const int* __restrict__ dst,
                                               const unsigned short* __restrict__ epsp, const unsigned short* __restrict__ w1,
                                               const unsigned short* __restrict__ b1, const unsigned short* __restrict__ w2,
                                               const unsigned short* __restrict__ b2, unsigned short* __restrict__ out, int N, int E) {
  __shared__ float h[D], h1[H];
  const int node = blockIdx.x, c = threadIdx.x, lane = c & 63;
  const float xs = bflo(x[(size_t)node * D + c]);
  float a = xs;
  for (int eb = 0; eb < E; eb += 64) {
    const int e = eb + lane, dv = e < E ? dst[e] : -1, sv = e < E ? src[e] : 0;
    u64 mk = __ballot(dv == node);
    while (mk) {
      const int s = __builtin_amdgcn_readlane(sv, __builtin_ctzll(mk));
      mk &= mk - 1;
      if ((unsigned)s < (unsigned)N) a += bflo(x[(size_t)s * D + c]);
    }
  }
  h[c] = bflo(pack_bf16(fmaf(bflo(epsp[0]), xs, a), 0.f));
  __syncthreads();
  for (int j = c; j < H; j += 256) {
    float s = bflo(b1[j]);
    for (int k = 0; k < D; ++k) s = fmaf(h[k], bflo(w1[k * H + j]), s);
    h1[j] = bflo(relu_pack(s, 0.f));
  }
  __syncthreads();
  float s = bflo(b2[c]);
  for (int k = 0; k < H; ++k) s = fmaf(h1[k], bflo(w2[k * D + c]), s);
  out[(size_t)node * D + c] = (unsigned short)pack_bf16(s, 0.f);
}

extern "C" void kernel_launch(void* const* d_in, const int* in_sizes, int n_in, void* d_out, int out_size, void* d_ws, size_t ws_size,
                              hipStream_t stream) {
  const unsigned short* x = (const unsigned short*)d_in[0];
  const int* src = (const int*)d_in[1];
  const int* dst = (const int*)d_in[2];
  const unsigned short* eps = (const unsigned short*)d_in[3];
  const unsigned short* w1 = (const unsigned short*)d_in[4];
  const unsigned short* b1 = (const unsigned short*)d_in[5];
  const unsigned short* w2 = (const unsigned short*)d_in[6];
  const unsigned short* b2 = (const unsigned short*)d_in[7];
  const int N = in_sizes[0] / D, E = in_sizes[1], T = (N + TM - 1) / TM;
  if (T >= 128 && T <= 256 && E <= EPB * T && E % 4 == 0 && ws_size >= WS_BYTES) {
    (void)hipFuncSetAttribute((const void*)gin_one, hipFuncAttributeMaxDynamicSharedMemorySize, L_END);
    gin_one<<<T, 512, L_END, stream>>>(x, src, dst, eps, w1, b1, w2, b2, (unsigned short*)d_out, (unsigned char*)d_ws, N, E, T);
  } else if (N > 0) {
    gin_any<<<N, 256, 0, stream>>>(x, src, dst, eps, w1, b1, w2, b2, (unsigned short*)d_out, N, E);
  }
}
```

```cpp
#include <hip/hip_runtime.h>
#include <hip/hip_bf16.h>
#include <stdint.h>
#include <type_traits>

typedef unsigned v4u __attribute__((ext_vector_type(4)));
typedef unsigned v2u __attribute__((ext_vector_type(2)));
typedef float f32x4 __attribute__((ext_vector_type(4)));
typedef float f32x2 __attribute__((ext_vector_type(2)));
typedef short bf16x8 __attribute__((ext_vector_type(8)));
typedef short s16x2 __attribute__((ext_vector_type(2)));
typedef __bf16 bf16x2_t __attribute__((ext_vector_type(2)));
typedef unsigned long long u64;
#define GAS __attribute__((address_space(1)))
#define LAS __attribute__((address_space(3)))
#define DI __device__ __forceinline__
#define INL __attribute__((always_inline))
#define RLX_AGENT __ATOMIC_RELAXED, __HIP_MEMORY_SCOPE_AGENT
#define RLX_WG __ATOMIC_RELAXED, __HIP_MEMORY_SCOPE_WORKGROUP
#define SB() __builtin_amdgcn_sched_barrier(0)
#define PRIO(p) __builtin_amdgcn_s_setprio(p)

constexpr int D = 256, H = 512, TM = 40;
constexpr int EPB = 640;
constexpr int CAP = 1536;
constexpr int HP = 544, H1P = 1056;
constexpr int R = 3;
constexpr unsigned OOB = 0x40000000u;
constexpr unsigned EOOB = OOB >> 3;
constexpr int SC1 = 16;
constexpr unsigned SPIN_CAP = 4096;
constexpr unsigned WS_EPOCH = 0, WS_WFLAG = 4096, WS_W2T = 65536, WS_CELL = 1u << 20, WS_BYTES = WS_CELL + 256u * 16384u;
constexpr int L_H = 0, L_H1 = L_H + 48 * HP, L_FLAT = L_H1 + 48 * H1P, L_CNT = L_FLAT + (CAP + 128) * 4, L_MISC = L_CNT + 256,
              L_B1 = L_MISC + 32, L_B2 = L_B1 + 2048, L_END = L_B2 + 512;
constexpr int L_PSTG = L_H;
constexpr int L_PCELL = L_H1, L_PCNT = L_H1 + 320 * 64;

DI unsigned pack_bf16(float a, float b) { f32x2 v = {a, b}; bf16x2_t r = __builtin_convertvector(v, bf16x2_t); return __builtin_bit_cast(unsigned, r); }
DI float bflo(unsigned u) { return __builtin_bit_cast(float, u << 16); }
DI float bfhi(unsigned u) { return __builtin_bit_cast(float, u & 0xffff0000u); }
DI float asf(unsigned u) { return __builtin_bit_cast(float, u); }
DI unsigned asu(float u) { return __builtin_bit_cast(unsigned, u); }
DI f32x4 mfma(v4u a, v4u b, f32x4 c) {
  return __builtin_amdgcn_mfma_f32_16x16x32_bf16(__builtin_bit_cast(bf16x8, a), __builtin_bit_cast(bf16x8, b), c, 0, 0, 0);
}
DI unsigned relu_pack(float a, float b) {
  const s16x2 v = __builtin_bit_cast(s16x2, pack_bf16(a, b));
  return __builtin_bit_cast(unsigned, __builtin_elementwise_max(v, s16x2{0, 0}));
}
DI void bar() { asm volatile("s_waitcnt lgkmcnt(0)" ::: "memory"); __builtin_amdgcn_s_barrier(); asm volatile("" ::: "memory"); }
template <int CTRL, int ROWS> DI unsigned dpp_add(unsigned x) { return x + (unsigned)__builtin_amdgcn_update_dpp(0, (int)x, CTRL, ROWS, 0xF, false); }
DI unsigned wave_incl_scan(unsigned x) {
  x = dpp_add<0x111, 0xF>(x); x = dpp_add<0x112, 0xF>(x); x = dpp_add<0x114, 0xF>(x); x = dpp_add<0x118, 0xF>(x);
  x = dpp_add<0x142, 0xA>(x); x = dpp_add<0x143, 0xC>(x);
  return x;
}
DI __amdgpu_buffer_rsrc_t rsrc(const void* p, unsigned bytes) { return __builtin_amdgcn_make_buffer_rsrc((void*)p, 0, bytes, 0x00020000); }
DI v4u srd(const void* p, unsigned bytes) { return v4u{(unsigned)(u64)p, (unsigned)((u64)p >> 32) & 0xffffu, bytes, 0x00020000u}; }
template <int I, int E, typename F> DI void static_for(F&& f) { if constexpr (I < E) { f(std::integral_constant<int, I>{}); static_for<I + 1, E>(f); } }

DI void tile_w2(__amdgpu_buffer_rsrc_t w2r, __amdgpu_buffer_rsrc_t wsr, int f, int lane) {
  const int ww = f >> 5, ks = (f >> 1) & 15, nt = f & 1, i = lane & 15, kg = lane >> 4;
  const int qo = (2 * ks + (kg >> 1)) & 3;
  const int row0 = 64 * (ks >> 1) + 32 * (qo & 1) + 4 * (qo >> 1) + 2 * (kg & 1);
  const unsigned o = (row0 * D + 32 * ww + 8 * (i >> 2) + 4 * nt + (i & 3)) * 2;
  unsigned v[8];
#pragma unroll
  for (int j = 0; j < 8; ++j) v[j] = __builtin_amdgcn_raw_buffer_load_b16(w2r, o + (j >> 2) * D * 2, (j & 3) * 8 * D * 2, 0);
  const v4u p = {v[0] | (v[1] << 16), v[2] | (v[3] << 16), v[4] | (v[5] << 16), v[6] | (v[7] << 16)};
  __builtin_amdgcn_raw_buffer_store_b128(p, wsr, WS_W2T + f * 1024 + lane * 16, 0, SC1);
}

constexpr int PB = 96, NY = 4;
template <int G> DI void load_w1(v4u d, unsigned off) {
  unsigned so;
  asm volatile("s_mov_b32 %0, %3\n\t"
               "buffer_load_dwordx4 v[%c5:%c5+3], %1, %2, %0 offen\n\tbuffer_load_dwordx4 v[%c5+4:%c5+7], %1, %2, %0 offen offset:1024\n\t"
               "buffer_load_dwordx4 v[%c5+8:%c5+11], %1, %2, %0 offen offset:2048\n\tbuffer_load_dwordx4 v[%c5+12:%c5+15], %1, %2, %0 offen offset:3072\n\t"
               "s_mov_b32 %0, %4\n\t"
               "buffer_load_dwordx4 v[%c5+16:%c5+19], %1, %2, %0 offen\n\tbuffer_load_dwordx4 v[%c5+20:%c5+23], %1, %2, %0 offen offset:1024\n\t"
               "buffer_load_dwordx4 v[%c5+24:%c5+27], %1, %2, %0 offen offset:2048\n\tbuffer_load_dwordx4 v[%c5+28:%c5+31], %1, %2, %0 offen offset:3072"
               : "=&s"(so) : "v"(off), "s"(d), "i"(G * 65536), "i"(G * 65536 + 4096), "i"(PB + 32 + 32 * G) : "v255");
}
template <int G> DI void xpose() {
#pragma unroll
  for (int d = 0; d < 4; ++d)
#pragma unroll
    for (int p = 0; p < 2; ++p) {
      unsigned t0, t1;
      asm volatile("v_perm_b32 v[%c5], v[%c4+4], v[%c4], %2\n\tv_perm_b32 v[%c5+16], v[%c4+6], v[%c4+2], %2\n\t"
                   "v_perm_b32 v[%c5+4], v[%c4+4], v[%c4], %3\n\tv_perm_b32 v[%c5+20], v[%c4+6], v[%c4+2], %3\n\t"
                   "v_mov_b32 %0, v[%c5]\n\tv_mov_b32 %1, v[%c5+4]\n\t"
                   "v_mov_b32_dpp v[%c5], v[%c5+16] row_ror:8 row_mask:0xf bank_mask:0xc\n\t"
                   "v_mov_b32_dpp v[%c5+4], v[%c5+20] row_ror:8 row_mask:0xf bank_mask:0xc\n\t"
                   "v_mov_b32_dpp v[%c5+16], %0 row_ror:8 row_mask:0xf bank_mask:0x3\n\t"
                   "v_mov_b32_dpp v[%c5+20], %1 row_ror:8 row_mask:0xf bank_mask:0x3"
                   : "=&v"(t0), "=&v"(t1) : "s"(0x05040100u), "s"(0x07060302u), "i"(PB + 32 + 32 * G + 8 * d + p), "i"(PB + 32 * G + 8 * p + d));
    }
}
constexpr int w2reg(int i) { return i < 8 ? PB + 128 + 4 * i : PB + 4 * (i - 8 - NY); }
template <int I> DI void load_w2(v4u d, unsigned off) {
  unsigned so;
  asm volatile("s_mov_b32 %0, %3\n\tbuffer_load_dwordx4 v[%c4:%c4+3], %1, %2, %0 offen offset:%c5 sc1"
               : "=&s"(so) : "v"(off), "s"(d), "i"((I >> 2) * 4096), "i"(w2reg(I)), "i"((I & 3) * 1024));
}
template <int A> DI void mfma_p(f32x4& acc, v4u b) { asm volatile("v_mfma_f32_16x16x32_bf16 %0, v[%c2:%c2+3], %1, %0" : "+v"(acc) : "v"(b), "i"(A)); }
template <int A> DI void mfma_p0(f32x4& acc, v4u b, f32x4 c) {
  asm volatile("v_mfma_f32_16x16x32_bf16 %0, v[%c3:%c3+3], %1, %2" : "=&v"(acc) : "v"(b), "v"(c), "i"(A));
}
DI void mfma_r(f32x4& acc, v4u a, v4u b) { asm volatile("v_mfma_f32_16x16x32_bf16 %0, %1, %2, %0" : "+v"(acc) : "v"(a), "v"(b)); }
template <bool DEV> DI void touch(v4u d, unsigned off) {
  if constexpr (DEV) asm volatile("buffer_load_dword v[%c2], %0, %1, 0 offen sc1" :: "v"(off), "s"(d), "i"(PB - 1));
  else asm volatile("buffer_load_dword v[%c2], %0, %1, 0 offen" :: "v"(off), "s"(d), "i"(PB - 1));
}
DI void count_row(unsigned a) {
  asm volatile("s_mov_b64 exec, 1\n\tds_add_u32 %0, %1\n\ts_mov_b64 exec, -1" :: "v"(a), "v"(1u) : "memory");
}
template <int N> DI void wait_vm() { asm volatile("s_waitcnt vmcnt(%c0)" :: "i"(N) : "memory"); }

__global__ __launch_bounds__(512, 2) __attribute__((amdgpu_num_vgpr(PB / 2 - 1)))
void gin_one(const unsigned short* __restrict__ x, const int* __restrict__ src, const int* __restrict__ dst,
             const unsigned short* __restrict__ epsp, const unsigned short* __restrict__ w1, const unsigned short* __restrict__ b1,
             const unsigned short* __restrict__ w2, const unsigned short* __restrict__ b2, unsigned short* __restrict__ out,
             unsigned char* ws, int N, int E, int T) {
  extern __shared__ __attribute__((aligned(16))) unsigned char Ls[];
  LAS unsigned char* L = (LAS unsigned char*)Ls;
  const int tid = threadIdx.x, lane = tid & 63, tid0 = __builtin_amdgcn_readfirstlane(tid), w = tid0 >> 6;
  const int t = blockIdx.x, node0 = t * TM;
  const unsigned bulk = w >> 2, chain = bulk ^ 1;
  const int rank = (t >> 3) & 31;
  {
    u64 pc; unsigned len;
    asm volatile("s_getpc_b64 %0\n.Lgin_pc:\n\ts_mov_b32 %1, .Lgin_end-.Lgin_pc" : "=s"(pc), "=s"(len));
    asm volatile("s_cmp_gt_u32 %3, 63\n\ts_cbranch_scc1 1f\n\tbuffer_load_dword v[%c2], %0, %1, 0 offen\n1:"
                 :: "v"(lane < 8 ? (rank * 8 + lane) * 128 : OOB), "s"(srd((const void*)pc, len)), "i"(PB - 1), "s"(tid0) : "scc");
  }
  SB();
  asm volatile("" :: "s"(x), "s"(src), "s"(dst), "s"(epsp), "s"(w1), "s"(b1), "s"(w2), "s"(b2), "s"(out), "s"(ws), "s"(N), "s"(E), "s"(T));
  const int share2 = (T >> 3) + rank;
  const __amdgpu_buffer_rsrc_t wsr = rsrc(ws, WS_BYTES), xr = rsrc(x, N * 512), w2r = rsrc(w2, D * H * 2);
  const v4u w1d = srd(w1, D * H * 2), wsd = srd(ws, WS_BYTES);
  const int q = lane >> 4, m = lane & 15;
  const unsigned w1off = (((lane >> 3) & 1) * 32 + q * 8) * 1024 + w * 128 + (lane & 7) * 16;
  LAS unsigned* pcnt = (LAS unsigned*)(L + L_PCNT);
  LAS unsigned* pcell = (LAS unsigned*)(L + L_PCELL);
  const int tf = t + (w - 1) * T;

  if (w == 4) {
    PRIO(3);
    const __amdgpu_buffer_rsrc_t dr = rsrc(dst, E * 4), sr = rsrc(src, E * 4);
    v4u dv[3], sv[3];
#pragma unroll
    for (int i = 0; i < 3; ++i) {
      const unsigned o = (i == 2 && lane >= 32) ? OOB : (unsigned)(t * EPB + (lane + 64 * i) * 4) * 4u;
      dv[i] = __builtin_amdgcn_raw_buffer_load_b128(dr, o, 0, 0);
      sv[i] = __builtin_amdgcn_raw_buffer_load_b128(sr, o, 0, 0);
    }
    const u64 ep0 = __hip_atomic_load((GAS u64*)(ws + WS_EPOCH), RLX_AGENT);
    SB();
    *(LAS v4u*)(pcnt + lane * 4) = v4u{0, 0, 0, 0};
    pcnt[256 + lane] = 0;
    const unsigned dummy = 256u + lane;
    unsigned tt[12], rk[12];
#pragma unroll
    for (int i = 0; i < 12; ++i) {
      const int e = t * EPB + (lane + 64 * (i >> 2)) * 4;
      const unsigned lo = (e < E && !(i >= 8 && lane >= 32)) ? 0u : dummy;
      tt[i] = max(min(__umul24(min(dv[i >> 2][i & 3], 16383u), 52429u) >> 21, dummy), lo);
      rk[i] = __hip_atomic_fetch_add(pcnt + tt[i], 1u, RLX_WG);
    }
#pragma unroll
    for (int i = 0; i < 12; ++i)
      pcell[tt[i] * 16 + (rk[i] & 15u)] = (dv[i >> 2][i & 3] - tt[i] * TM) | (min(sv[i >> 2][i & 3], 16383u) << 6);
    *(LAS u64*)(L + L_MISC) = ep0;
  } else {
    __builtin_amdgcn_s_sleep(3);
    if (w == 0 || (w == 5 && share2 < 32)) touch<false>(w1d, (w ? share2 : rank) * 8192 + lane * 128);
    if (w == 3) {
      const v4u v1 = __builtin_amdgcn_raw_buffer_load_b128(rsrc(b1, H * 2), lane * 16, 0, 0);
      const v4u v2 = __builtin_amdgcn_raw_buffer_load_b128(rsrc(b2, D * 2), lane * 16, 0, 0);
      const unsigned ev = epsp[0];
      *(LAS unsigned*)(L + L_CNT + lane * 4) = 0;
#pragma unroll
      for (int i = 0; i < 8; ++i)
        *(LAS float*)(L + L_B1 + ((lane >> 3) * 64 + (((lane >> 2) & 1) + 2 * (i >> 2)) * 16 + 4 * (i & 3) + (lane & 3)) * 4) =
            (i & 1) ? bfhi(v1[i >> 1]) : bflo(v1[i >> 1]);
      *(LAS v4u*)(L + (lane < 32 ? L_B2 + lane * 16 : L_FLAT + lane * 16)) = v2;
      *(LAS v2u*)(L + L_MISC + 8) = v2u{0u, ev};
      *(LAS v4u*)(L + L_MISC + 16) = v4u{0u, 0u, 0u, 0u};
    } else if ((w == 1 || w == 2) && tf < 256) {
      tile_w2(w2r, wsr, tf, lane);
    }
  }
  if (chain) { load_w1<0>(w1d, w1off); load_w1<1>(w1d, w1off); }
  PRIO(3);
  bar();
  const u64 tag = *(LAS u64*)(L + L_MISC) + 1;
  const unsigned tagA = (unsigned)tag & 0xFFFFFFu, tagB = ((unsigned)tag + (unsigned)(tag >> 24) * 0x9E3779B1u) & 0x7FFFFu;
  {
    v4u e[2]; unsigned c[2];
#pragma unroll
    for (int i = 0; i < 2; ++i) { e[i] = *(LAS v4u*)(pcell + (w * 2 + i) * 256 + lane * 4); c[i] = pcnt[(w * 2 + i) * 16 + (lane >> 2)]; }
#pragma unroll
    for (int i = 0; i < 2; ++i) {
      const unsigned tc = (w * 2 + i) * 16 + (lane >> 2), cc = min(c[i], 31u);
      const v4u g = {(e[i].x & 0xFFFFFu) | (e[i].y << 20), ((e[i].y >> 12) & 0xFFu) | (tagA << 8), (e[i].z & 0xFFFFFu) | (e[i].w << 20),
                     ((e[i].w >> 12) & 0xFFu) | (cc << 8) | (tagB << 13)};
      __builtin_amdgcn_raw_buffer_store_b128(g, wsr, tc < (unsigned)T ? WS_CELL + tc * 16384 + t * 64 + (lane & 3) * 16 : OOB, 0, SC1);
    }
  }
  SB();
  PRIO(1);
  if (chain) { load_w1<2>(w1d, w1off); load_w1<3>(w1d, w1off); } else __builtin_amdgcn_s_sleep(20);
  bool any2 = false;
  unsigned ent[16], rk[16];
  LAS unsigned* flat = (LAS unsigned*)(L + L_FLAT);
  if (chain) {
    PRIO(2);
    v4u g[4];
    const unsigned coff = WS_CELL + t * 16384 + w * 4096 + lane * 16;
#pragma unroll
    for (int i = 0; i < 4; ++i) g[i] = __builtin_amdgcn_raw_buffer_load_b128(wsr, coff + i * 1024, 0, SC1);
    bool tmo = false;
    const int pl = T - 64 * w - (lane >> 2);
#define GR_OK(i) (16 * (i) >= pl || ((g[i].y >> 8) == tagA && (g[i].w >> 13) == tagB))
    if (!__all(GR_OK(0) && GR_OK(1) && GR_OK(2) && GR_OK(3))) {
      for (unsigned spins = 0;;) {
#pragma unroll
        for (int i = 0; i < 4; ++i) {
          const bool ok = GR_OK(i);
          const v4u nv = __builtin_amdgcn_raw_buffer_load_b128(wsr, ok ? OOB : coff + i * 1024, 0, SC1);
#pragma unroll
          for (int k = 0; k < 4; ++k) g[i][k] = ok ? g[i][k] : nv[k];
        }
        if (__all(GR_OK(0) && GR_OK(1) && GR_OK(2) && GR_OK(3))) break;
        if (++spins > SPIN_CAP) { tmo = true; break; }
      }
    }
    LAS unsigned char* stg = L + L_PSTG + w * 5120;
#pragma unroll
    for (int i = 0; i < 4; ++i) *(LAS v4u*)(stg + (16 * i + (lane >> 2)) * 80 + (lane & 3) * 16) = g[i];
#pragma unroll
    for (int i = 0; i < 4; ++i) g[i] = *(LAS v4u*)(stg + lane * 80 + i * 16);
    const int nval = (64 * w + lane < T && !tmo) ? (int)((g[0].w >> 8) & 31u) : 0;
    any2 = __any(nval > 8);
    bool bad = nval > 16;
    const unsigned dummy = 40u + (unsigned)(lane % 24);
    auto rank8 = [&](int lo) INL {
#pragma unroll
      for (int i = lo; i < lo + 8; ++i) {
        const unsigned a = g[i >> 2][i & 2], b = g[i >> 2][(i & 2) + 1];
        ent[i] = i < nval ? ((i & 1) ? __builtin_amdgcn_alignbit(b, a, 20) : a) & 0xFFFFFu : dummy;
        rk[i] = __hip_atomic_fetch_add((LAS unsigned*)(L + L_CNT + ((ent[i] & 63u) << 2)), 1u << (8 * w), RLX_WG);
      }
#pragma unroll
      for (int i = lo; i < lo + 8; ++i) { rk[i] = (rk[i] >> (8 * w)) & 255u; bad |= rk[i] == 255u; }
    };
    rank8(0);
    if (__builtin_expect(any2, 0)) rank8(8);
    if ((__any(bad) || tmo) && lane == 0) __hip_atomic_fetch_or((LAS unsigned*)(L + L_MISC + 8), tmo ? 3u : 1u, RLX_WG);
  } else {
    PRIO(0);
    load_w1<0>(w1d, w1off); load_w1<1>(w1d, w1off); load_w1<2>(w1d, w1off); load_w1<3>(w1d, w1off);
    touch<false>(srd(x, N * 512), node0 * 512 + ((w - 4) * 64 + lane) * 128);
    wait_vm<25>(); xpose<0>();
  }
  bar();
  bool slow, timedout;
  int s0, nit;
  u64 pk = 0;
  {
    const unsigned cw = *(LAS unsigned*)(L + L_CNT + lane * 4);
    const unsigned flagv = __builtin_amdgcn_readfirstlane(*(LAS unsigned*)(L + L_MISC + 8));
    const unsigned deg = __builtin_amdgcn_sad_u8(cw, 0u, 0u), below = __builtin_amdgcn_sad_u8(cw & ((1u << (8 * (w & 3))) - 1u), 0u, 0u);
    const unsigned l4 = (deg + 4u) & ~3u;
    const unsigned plen = lane < TM ? l4 + (lane % 5 == 4 ? 4u * (R - 1) : 0u) : 0u;
    const unsigned incl = wave_incl_scan(plen), start = incl - plen;
    timedout = (flagv & 2u) != 0u;
    slow = flagv != 0u || __builtin_amdgcn_readlane(incl, 63) > (unsigned)CAP;
    const unsigned tail = __builtin_amdgcn_readlane(incl, 5 * w + 4) - 4 * (R - 1);
    s0 = __builtin_amdgcn_readlane(start, 5 * w);
    nit = ((tail - s0) / 4 + R - 1) / R;
#pragma unroll
    for (int j = 0; j < 5; ++j) pk |= (u64)(((unsigned)__builtin_amdgcn_readlane(l4, 5 * w + j) >> 2) & 0xFFFu) << (12 * j);
    if (slow) s0 = 0;
    if (!slow) {
      const unsigned st2 = (unsigned)__builtin_amdgcn_ds_bpermute((5 * w + (lane >> 3)) << 2, (int)(start | (deg << 16)));
      if (chain) {
        const unsigned B4 = L_FLAT + 4u * (lane < TM ? start + 1u + below : CAP + 64u);
        auto place8 = [&](int lo) INL {
          unsigned b[8];
#pragma unroll
          for (int i = 0; i < 8; ++i) b[i] = (unsigned)__builtin_amdgcn_ds_bpermute((int)((ent[lo + i] & 63u) << 2), (int)B4);
#pragma unroll
          for (int i = 0; i < 8; ++i) *(LAS unsigned*)(L + b[i] + 4u * rk[lo + i]) = ent[lo + i];
        };
        place8(0);
        if (__builtin_expect(any2, 0)) place8(8);
      }
      const unsigned dg = st2 >> 16, k = lane & 7, pp = k == 7 ? 0u : dg + 1u + k;
      const bool mine = lane < 40 && pp < ((dg + 4u) & ~3u);
      const unsigned at = mine ? (st2 & 0xFFFFu) + pp : (lane >= 48 && lane - 48 < 4 * (R - 1) ? tail + lane - 48 : CAP + lane);
      flat[at] = (mine && k == 7) ? (unsigned)(node0 + 5 * w + (lane >> 3)) << 6 : EOOB;
    }
  }
  if (bulk) { wait_vm<17>(); xpose<1>(); wait_vm<9>(); xpose<2>(); }
  bar();
  PRIO(2);
  if (w == 1 || w == 2) {
    wait_vm<0>();
    if (lane == 0 && tf < 256) __hip_atomic_store((GAS unsigned*)(ws + WS_WFLAG) + tf, (unsigned)tag, RLX_AGENT);
  }
  const unsigned l16 = m * 16;
  LAS unsigned* fp = flat + s0 + q;
  v4u bl[R], br[R];
  unsigned en[R];
#pragma unroll
  for (int i = 0; i < R; ++i) en[i] = fp[4 * i];
  const float epsf = bflo(*(LAS unsigned*)(L + L_MISC + 12));
#pragma unroll
  for (int i = 0; i < R; ++i) {
    const unsigned o = ((en[i] << 3) & ~511u) | l16;
    bl[i] = __builtin_amdgcn_raw_buffer_load_b128(xr, o, 0, 0);
    br[i] = __builtin_amdgcn_raw_buffer_load_b128(xr, o + 256, 0, 0);
    en[i] = fp[4 * (R + i)];
  }
  asm volatile("" ::: "memory");
  SB();
  if (chain) { PRIO(0); xpose<0>(); xpose<1>(); xpose<2>(); }
  wait_vm<2 * R + 1>();
  xpose<3>();
  PRIO(0);
  int stage = 0;
  bool xdone = false;
  const unsigned w2off = WS_W2T + w * 32768 + lane * 16;
  v4u gb[2];
  f32x4 gacc[4];
  unsigned ghb = 0;
  auto gemm1 = [&](int mt, auto LAST, auto PART) INL {
    constexpr int part = PART;
    if constexpr (part == 0) {
      ghb = L_H + (mt * 16 + m) * HP + q * 16;
#pragma unroll
      for (int i = 0; i < 2; ++i) gb[i] = *(LAS v4u*)(L + ghb + i * 64);
#pragma unroll
      for (int c = 0; c < 4; ++c) gacc[c] = *(LAS f32x4*)(L + L_B1 + (64 * w + 16 * q) * 4 + 16 * c);
    }
    static_for<3 * part, (part == 2 ? 8 : 3 * part + 3)>([&](auto KS) {
      constexpr int ks = KS;
      if constexpr (LAST && (ks & 1) == 0) PRIO(3 - ks / 2);
      static_for<0, 4>([&](auto C) { mfma_p<PB + 16 * ks + 4 * C>(gacc[C], gb[ks & 1]); });
      if constexpr (ks + 2 < 8) gb[ks & 1] = *(LAS v4u*)(L + ghb + (ks + 2) * 64);
      if constexpr (LAST) load_w2<8 + NY + ks>(wsd, w2off);
      SB();
    });
    if constexpr (part == 2) {
      asm volatile("s_nop 7" : "+v"(gacc[0]), "+v"(gacc[1]), "+v"(gacc[2]), "+v"(gacc[3]));
      LAS unsigned char* h1w = L + L_H1 + (mt * 16 + m) * H1P + (64 * w + 16 * q) * 2;
#pragma unroll
      for (int half = 0; half < 2; ++half) {
        unsigned o[4];
#pragma unroll
        for (int k = 0; k < 4; ++k) o[k] = relu_pack(gacc[2 * half + (k >> 1)][2 * (k & 1)], gacc[2 * half + (k >> 1)][2 * (k & 1) + 1]);
        *(LAS v4u*)(h1w + 16 * half) = v4u{o[0], o[1], o[2], o[3]};
      }
    }
  };
  typedef std::integral_constant<int, 0> I0; typedef std::integral_constant<int, 1> I1; typedef std::integral_constant<int, 2> I2;
  auto wait_rows = [&](int mt) INL {
    while (__builtin_amdgcn_readfirstlane(__hip_atomic_load((LAS unsigned*)(L + L_MISC + 20 + 4 * mt), RLX_WG)) < (mt < 2 ? 16u : 8u))
      __builtin_amdgcn_s_sleep(1);
    asm volatile("" ::: "memory");
  };
  auto early_head = [&]() INL {
    if (stage == 0) {
      asm volatile("buffer_load_dwordx4 v[%c2:%c2+3], %0, %1, 0 offen sc1" :: "v"(WS_WFLAG + lane * 16), "s"(wsd), "i"(PB + 156));
    } else {
      unsigned d0, d1;
      asm volatile("v_xor_b32 %0, %2, v[%c3]\n\tv_xor_b32 %1, %2, v[%c3+1]\n\tv_or_b32 %0, %0, %1\n\tv_xor_b32 %1, %2, v[%c3+2]\n\t"
                   "v_or_b32 %0, %0, %1\n\tv_xor_b32 %1, %2, v[%c3+3]\n\tv_or_b32 %0, %0, %1"
                   : "=&v"(d0), "=&v"(d1) : "s"((unsigned)tag), "i"(PB + 156));
      xdone = __all(d0 == 0u);
      if (xdone) static_for<0, 8>([&](auto I) { load_w2<I>(wsd, w2off); });
    }
  };
  if (__builtin_expect(!slow, 1)) {
    const v4u sel = {m == 0 ? 0x3F80u : (m == 1 ? 0x3F800000u : 0u), m == 2 ? 0x3F80u : (m == 3 ? 0x3F800000u : 0u),
                     m == 4 ? 0x3F80u : (m == 5 ? 0x3F800000u : 0u), m == 6 ? 0x3F80u : (m == 7 ? 0x3F800000u : 0u)};
#define SELF(k) (unsigned)__builtin_amdgcn_update_dpp((int)sel[k], (int)sel[k], 0x128, 0x1, 0xC, false)
    v4u cur = {SELF(0), SELF(1), SELF(2), SELF(3)};
    fp += 8 * R;
    f32x4 a0 = {0.f, 0.f, 0.f, 0.f}, a1 = {0.f, 0.f, 0.f, 0.f};
    const unsigned hl = L_H + (lane >> 5) * 256 + m * 16 + (q & 1) * 8;
    unsigned rows = (32 + w) | ((2 * w) << 6) | ((2 * w + 1) << 12) | ((16 + 2 * w) << 18) | ((17 + 2 * w) << 24);
    unsigned cv = 0;
    int rem = (int)(pk & 0xFFF);
#define SLOT(i, REFILL) { \
        a0 = mfma(cur, bl[i], a0); \
        a1 = mfma(cur, br[i], a1); \
        cur = sel; \
        if (REFILL) { \
          const unsigned o = ((en[i] << 3) & ~511u) | l16; \
          bl[i] = __builtin_amdgcn_raw_buffer_load_b128(xr, o, 0, 0); \
          br[i] = __builtin_amdgcn_raw_buffer_load_b128(xr, o + 256, 0, 0); \
          en[i] = fp[4 * i]; \
        } \
        if (__builtin_expect(--rem == 0, 0)) { \
          float f[4]; \
          _Pragma("unroll") for (int r = 0; r < 4; ++r) { \
            auto s2 = __builtin_amdgcn_permlane32_swap(asu(a0[r]), asu(a1[r]), false, false); \
            const unsigned u0 = s2[0], u1 = s2[1]; \
            f[r] = fmaf(epsf, asf(u1), asf(u0)); \
          } \
          *(LAS v2u*)(L + hl + (rows & 63u) * HP) = v2u{pack_bf16(f[0], f[1]), pack_bf16(f[2], f[3])}; \
          count_row(L_MISC + 20 + ((rows & 48u) >> 2));       \
          rows >>= 6; \
          a0 = f32x4{0.f, 0.f, 0.f, 0.f}; a1 = a0; \
          cur = v4u{SELF(0), SELF(1), SELF(2), SELF(3)}; \
          pk >>= 12; \
          rem = (int)(pk & 0xFFF); \
        } }
    if (w == 6 || (w == 7 && share2 < 32)) touch<true>(wsd, WS_W2T + (w == 7 ? share2 : rank) * 8192 + lane * 128);
    static_assert(R == 3, "GEMM1 comes in three parts, one behind each slot of a round");
    for (; nit > 1; --nit) {
      const bool go = stage < 2 && __builtin_amdgcn_readfirstlane(cv) >= (stage ? 16u : 8u);
      asm volatile("" ::: "memory");
      SLOT(0, true)
      if (go) { early_head(); gemm1(stage ? 0 : 2, std::false_type{}, I0{}); }
      SLOT(1, true)
      if (go) gemm1(0, std::false_type{}, I1{});
      SLOT(2, true)
      if (go) { gemm1(stage ? 0 : 2, std::false_type{}, I2{}); ++stage; }
      fp += 4 * R;
      cv = __hip_atomic_load((LAS unsigned*)(L + L_MISC + 20 + (stage ? 0 : 8)), RLX_WG);
    }
#pragma unroll
    for (int i = 0; i < R; ++i) SLOT(i, false)
  } else {
#pragma unroll 1
    for (int j = 0; j < 5; ++j) {
      const int node = node0 + 5 * w + j;
      const v2u s2 = __builtin_amdgcn_raw_buffer_load_b64(xr, (unsigned)node * 512u + lane * 8, 0, 0);
      float a0 = bflo(s2.x), a1 = bfhi(s2.x), a2 = bflo(s2.y), a3 = bfhi(s2.y);
      if (node < N)
        for (int eb = 0; eb < E; eb += 64) {
          const int e = eb + lane;
          const int dv2 = (int)__builtin_amdgcn_raw_buffer_load_b32(rsrc(dst, E * 4), e * 4, 0, 0);
          const int sv2 = (int)__builtin_amdgcn_raw_buffer_load_b32(rsrc(src, E * 4), e * 4, 0, 0);
          u64 mkk = __ballot(dv2 == node && e < E);
          while (mkk) {
            const int b = __builtin_ctzll(mkk); mkk &= mkk - 1;
            const unsigned sidx = (unsigned)__builtin_amdgcn_readlane(sv2, b);
            const v2u rv = __builtin_amdgcn_raw_buffer_load_b64(xr, sidx < (unsigned)N ? sidx * 512u + lane * 8 : OOB, 0, 0);
            a0 += bflo(rv.x); a1 += bfhi(rv.x); a2 += bflo(rv.y); a3 += bfhi(rv.y);
          }
        }
      const int row = j ? ((j - 1) >> 1) * 16 + 2 * w + ((j - 1) & 1) : 32 + w;
      *(LAS v2u*)(L + L_H + row * HP + lane * 8) =
          v2u{pack_bf16(fmaf(epsf, bflo(s2.x), a0), fmaf(epsf, bfhi(s2.x), a1)), pack_bf16(fmaf(epsf, bflo(s2.y), a2), fmaf(epsf, bfhi(s2.y), a3))};
      count_row(L_MISC + 20 + (row >> 4) * 4);
    }
  }
  if (!timedout && tid == 192 && t == 0) __hip_atomic_store((GAS u64*)(ws + WS_EPOCH), tag, RLX_AGENT);
#pragma unroll 1
  for (; stage < 2; ++stage) {
    wait_rows(stage ? 0 : 2); wait_vm<0>(); early_head();
    gemm1(stage ? 0 : 2, std::false_type{}, I0{}); gemm1(0, std::false_type{}, I1{}); gemm1(stage ? 0 : 2, std::false_type{}, I2{});
  }
  if (__builtin_expect(!xdone, 0)) {
    bool okw = false;
    for (unsigned sp = 0; sp <= SPIN_CAP && !okw; ++sp) {
      const v4u fl = __builtin_amdgcn_raw_buffer_load_b128(wsr, WS_WFLAG + lane * 16, 0, SC1);
      okw = __all(fl.x == (unsigned)tag && fl.y == (unsigned)tag && fl.z == (unsigned)tag && fl.w == (unsigned)tag);
    }
    if (!okw) {
#pragma unroll 1
      for (int i = 0; i < 32; ++i) tile_w2(w2r, wsr, w * 32 + i, lane);
    }
    wait_vm<0>();
    static_for<0, 8>([&](auto I) { load_w2<I>(wsd, w2off); });
  }
  v4u wy[NY];
#pragma unroll
  for (int i = 0; i < NY; ++i) wy[i] = __builtin_amdgcn_raw_buffer_load_b128(wsr, w2off + (i & 3) * 1024, (2 + (i >> 2)) * 4096, SC1);
  SB();
  const v4u bb2 = *(LAS v4u*)(L + L_B2 + (32 * w + 8 * q) * 2);
  wait_rows(1);
#pragma unroll
  for (int i = 0; i < NY; ++i) asm volatile("" : "+v"(wy[i]));
  gemm1(1, std::true_type{}, I0{}); gemm1(1, std::true_type{}, I1{}); gemm1(1, std::true_type{}, I2{});
  f32x4 bias2[2];
#pragma unroll
  for (int nt = 0; nt < 2; ++nt) {
    bias2[nt] = f32x4{bflo(bb2[2 * nt]), bfhi(bb2[2 * nt]), bflo(bb2[2 * nt + 1]), bfhi(bb2[2 * nt + 1])};
    asm volatile("" : "+v"(bias2[nt]));
  }
  SB();
  bar();
  {
    f32x4 acc[3][2];
    LAS unsigned char* hb = L + L_H1 + m * H1P + q * 16;
    const __amdgpu_buffer_rsrc_t outr = rsrc(out, N * 512);
    auto put = [&](int mt) INL {
      const int r = mt < 2 ? 5 * (m >> 1) + 1 + 2 * mt + (m & 1) : (m < 8 ? 5 * m : TM);
      const v4u o = {pack_bf16(acc[mt][0][0], acc[mt][0][1]), pack_bf16(acc[mt][0][2], acc[mt][0][3]),
                     pack_bf16(acc[mt][1][0], acc[mt][1][1]), pack_bf16(acc[mt][1][2], acc[mt][1][3])};
      __builtin_amdgcn_raw_buffer_store_b128(o, outr, r < TM ? (unsigned)(node0 + r) * 512u + 64 * w + 16 * q : OOB, 0, SC1);
    };
    constexpr int P2 = 8, F0 = 16 + NY;
    v4u b[P2];
#define U2_KS(u) ((u) < 24 ? (u) / 3 : 8 + ((u) - 24) % 8)
#define U2_MT(u) ((u) < 24 ? (u) % 3 : ((u) - 24) / 8)
#pragma unroll
    for (int u = 0; u < P2; ++u) b[u] = *(LAS v4u*)(hb + U2_MT(u) * 16 * H1P + U2_KS(u) * 64);
    static_for<0, 48>([&](auto U) {
      constexpr int u = U, ks = U2_KS(u), mt = U2_MT(u);
      if constexpr (u % 12 == 0) PRIO(3 - u / 12);
      if constexpr (u < 2 * (32 - F0) && (u & 1) == 0) load_w2<F0 + u / 2>(wsd, w2off);
      constexpr int issued = u < 2 * (32 - F0) ? F0 + 1 + u / 2 : 32;
      if constexpr (mt == 0 && 2 * ks >= 8 + NY) wait_vm<issued - (2 * ks + 2)>();
      static_for<0, 2>([&](auto NT) {
        constexpr int f = 2 * ks + NT;
        if constexpr (ks == 0) mfma_p0<w2reg(f)>(acc[mt][NT], b[u % P2], bias2[NT]);
        else if constexpr (f >= 8 && f < 8 + NY) mfma_r(acc[mt][NT], wy[f - 8], b[u % P2]);
        else mfma_p<w2reg(f)>(acc[mt][NT], b[u % P2]);
      });
      if constexpr (u + P2 < 48) b[u % P2] = *(LAS v4u*)(hb + U2_MT(u + P2) * 16 * H1P + U2_KS(u + P2) * 64);
      if constexpr (u == 32) {
        if (w == 3) asm volatile("buffer_load_dwordx2 v[%c2:%c2+1], %0, %1, 0 offen sc1" :: "v"(WS_EPOCH), "s"(wsd), "i"(PB - 2));
      }
      if constexpr (u == 36 || u == 44) put(mt - 1);
      SB();
    });
    asm volatile("s_nop 7" : "+v"(acc[2][0]), "+v"(acc[2][1]));
    put(2);
  }
  if (!timedout && w == 3) {
    unsigned e0, e1;
    asm volatile("s_waitcnt vmcnt(3)\n\tv_mov_b32 %0, v[%c2]\n\tv_mov_b32 %1, v[%c2+1]" : "=v"(e0), "=v"(e1) : "i"(PB - 2));
    if ((e0 != (unsigned)tag || e1 != (unsigned)(tag >> 32)) && lane == 0) __hip_atomic_store((GAS u64*)(ws + WS_EPOCH), tag, RLX_AGENT);
  }
  asm volatile(".Lgin_end:");
}

__global__ __launch_bounds__(256) void gin_any(const unsigned short* __restrict__ x, const int* __restrict__ src, const int* __restrict__ dst,
                                               const unsigned short* __restrict__ epsp, const unsigned short* __restrict__ w1,
                                               const unsigned short* __restrict__ b1, const unsigned short* __restrict__ w2,
                                               const unsigned short* __restrict__ b2, unsigned short* __restrict__ out, int N, int E) {
  __shared__ float h[D], h1[H];
  const int node = blockIdx.x, c = threadIdx.x, lane = c & 63;
  const float xs = bflo(x[(size_t)node * D + c]);
  float a = xs;
  for (int eb = 0; eb < E; eb += 64) {
    const int e = eb + lane, dv = e < E ? dst[e] : -1, sv = e < E ? src[e] : 0;
    u64 mk = __ballot(dv == node);
    while (mk) {
      const int s = __builtin_amdgcn_readlane(sv, __builtin_ctzll(mk));
      mk &= mk - 1;
      if ((unsigned)s < (unsigned)N) a += bflo(x[(size_t)s * D + c]);
    }
  }
  h[c] = bflo(pack_bf16(fmaf(bflo(epsp[0]), xs, a), 0.f));
  __syncthreads();
  for (int j = c; j < H; j += 256) {
    float s = bflo(b1[j]);
    for (int k = 0; k < D; ++k) s = fmaf(h[k], bflo(w1[k * H + j]), s);
    h1[j] = bflo(relu_pack(s, 0.f));
  }
  __syncthreads();
  float s = bflo(b2[c]);
  for (int k = 0; k < H; ++k) s = fmaf(h1[k], bflo(w2[k * D + c]), s);
  out[(size_t)node * D + c] = (unsigned short)pack_bf16(s, 0.f);
}

extern "C" void kernel_launch(void* const* d_in, const int* in_sizes, int n_in, void* d_out, int out_size, void* d_ws, size_t ws_size,
                              hipStream_t stream) {
  const unsigned short* x = (const unsigned short*)d_in[0];
  const int* src = (const int*)d_in[1];
  const int* dst = (const int*)d_in[2];
  const unsigned short* eps = (const unsigned short*)d_in[3];
  const unsigned short* w1 = (const unsigned short*)d_in[4];
  const unsigned short* b1 = (const unsigned short*)d_in[5];
  const unsigned short* w2 = (const unsigned short*)d_in[6];
  const unsigned short* b2 = (const unsigned short*)d_in[7];
  const int N = in_sizes[0] / D, E = in_sizes[1], T = (N + TM - 1) / TM;
  if (T >= 128 && T <= 256 && E <= EPB * T && E % 4 == 0 && ws_size >= WS_BYTES) {
    (void)hipFuncSetAttribute((const void*)gin_one, hipFuncAttributeMaxDynamicSharedMemorySize, L_END);
    gin_one<<<T, 512, L_END, stream>>>(x, src, dst, eps, w1, b1, w2, b2, (unsigned short*)d_out, (unsigned char*)d_ws, N, E, T);
  } else if (N > 0) {
    gin_any<<<N, 256, 0, stream>>>(x, src, dst, eps, w1, b1, w2, b2, (unsigned short*)d_out, N, E);
  }
}
```
